```python
import torch

def _softmax(x, axis=-1):
    return torch.softmax(x, dim=axis)
def _gelu(x):
    return torch.nn.functional.gelu(x, approximate="tanh")
def _silu(x):
    return torch.nn.functional.silu(x)
def _sigmoid(x):
    return torch.sigmoid(x)
def _layernorm(x, axis=-1, eps=1e-5):
    m = x.mean(dim=axis, keepdim=True)
    v = x.var(dim=axis, keepdim=True, unbiased=False)
    return (x - m) / torch.sqrt(v + eps)
def _scatter_add(tgt, idx, src, axis=0):
    return tgt.clone().index_add_(axis, idx, src)
def _segment_sum(src, seg, n):
    out = torch.zeros((n,) + src.shape[1:], dtype=src.dtype)
    return out.index_add_(0, seg, src)
def _pad(x, pw):
    flat = tuple(v for pair in reversed(pw) for v in pair)
    return torch.nn.functional.pad(x, flat)

kernel_name = 'standalone__gqa_softmax__near_ridge'

def setup_inputs():

    _w = (lambda a: (lambda t: t.to(torch.bfloat16) if t.is_floating_point() else t)(torch.as_tensor(a)))
    B, Hq, Hkv, Sq, Sk, D = 32, 16, 8, 256, 512, 256
    return {
        "q": _w((lambda *s: torch.randn(*s, dtype=torch.bfloat16) * 0.8)(B, Hq, Sq, D)),
        "k": _w((lambda *s: torch.randn(*s, dtype=torch.bfloat16) * 0.8)(B, Hkv, Sk, D)),
        "v": _w((lambda *s: torch.randn(*s, dtype=torch.bfloat16) * 0.05)(B, Hkv, Sk, D)),
    }

def reference(q, k, v):
    B, Hq, Sq, D = q.shape
    Hkv = k.shape[1]
    rep = Hq // Hkv
    k = (lambda x, n, axis: torch.repeat_interleave(x, n, dim=axis))(k, rep, axis=1)
    v = (lambda x, n, axis: torch.repeat_interleave(x, n, dim=axis))(v, rep, axis=1)
    scale = D ** -0.5
    scores = torch.einsum("bhqd,bhkd->bhqk", q, k) * scale
    p = _softmax(scores, axis=-1)
    return torch.einsum("bhqk,bhkd->bhqd", p, v)
```

```cpp
#include <hip/hip_runtime.h>
#include <hip/hip_bf16.h>
typedef short bf16x8 __attribute__((ext_vector_type(8)));
typedef short s16x4 __attribute__((ext_vector_type(4)));
typedef float f32x16 __attribute__((ext_vector_type(16)));
typedef unsigned u32x4 __attribute__((ext_vector_type(4)));
typedef unsigned u32x2 __attribute__((ext_vector_type(2)));
typedef __attribute__((address_space(3))) const char* lds_cptr;
typedef short v4i16_t __attribute__((ext_vector_type(4)));
constexpr int D = 256, SQ = 256, SK = 512, KT = 32, NT = SK / KT;
constexpr int KSLOT = 16 * 1056, VSLOT = 16 * 1152, NS = 3;
constexpr int LDS_K = 0, LDS_V = NS * KSLOT, LDS_BYTES = NS * (KSLOT + VSLOT);
constexpr float C2 = 0.0625f * 1.4426950408889634f;
constexpr float THR = 8.f;
#define MFMA(a, b, c) __builtin_amdgcn_mfma_f32_32x32x16_bf16(a, b, c, 0, 0, 0)
#define SBAR() __builtin_amdgcn_sched_barrier(0)
__device__ __forceinline__ unsigned cvtpk(float lo, float hi) { unsigned r; asm("v_cvt_pk_bf16_f32 %0, %1, %2" : "=v"(r) : "v"(lo), "v"(hi)); return r; }
__device__ __forceinline__ void glds16(const void* g, unsigned lds_base) {
  unsigned sv; asm volatile("s_mov_b32 %0, m0\n\ts_mov_b32 m0, %2\n\ts_nop 0\n\tglobal_load_lds_dwordx4 %1, off\n\ts_mov_b32 m0, %0" : "=&s"(sv) : "v"(g), "s"(lds_base) : "memory"); }
__device__ __forceinline__ s16x4 vtr(lds_cptr p) { return __builtin_bit_cast(s16x4, __builtin_amdgcn_ds_read_tr16_b64_v4i16((__attribute__((address_space(3))) v4i16_t*)p)); }
#define MX3(a, b, c) __builtin_fmaxf(__builtin_fmaxf((a), (b)), (c))

__global__ __launch_bounds__(512, 2) void attn_v0(const unsigned short* __restrict__ Q, const unsigned short* __restrict__ K, const unsigned short* __restrict__ V, unsigned short* __restrict__ O) {
  extern __shared__ __attribute__((aligned(16))) char lds[];
  const int tid = threadIdx.x, lane = tid & 63, r32 = lane & 31, hi = lane >> 5;
  const int wid = __builtin_amdgcn_readfirstlane(tid >> 6);
  const int bx = blockIdx.x, x = bx & 7, y = bx >> 3;
  const int kvp = (y >> 1) * 8 + x, hsel = y & 1;
  const int bh = kvp * 2 + hsel;
  const unsigned short* Qw = Q + ((size_t)bh * SQ + wid * 32) * D;
  const unsigned short* Kb = K + (size_t)kvp * SK * D;
  const unsigned short* Vb = V + (size_t)kvp * SK * D;
  const unsigned lds0 = (unsigned)(uintptr_t)lds;
  const int e = lane >> 5, cl = lane & 31;
  const unsigned short* ksrc = Kb + (size_t)(4 * wid + e) * D + ((cl ^ e) * 8);
  const unsigned short* vsrc = Vb + (size_t)(4 * wid + e) * D + ((cl ^ (e << 2)) * 8);
  const unsigned kdst = lds0 + LDS_K + (2 * wid) * 1056, vdst = lds0 + LDS_V + (2 * wid) * 1152;
#define DMA(t, s) do { glds16(ksrc + (size_t)(t) * KT * D, kdst + (s) * KSLOT); glds16(ksrc + (size_t)(t) * KT * D + 2 * D, kdst + (s) * KSLOT + 1056); \
    glds16(vsrc + (size_t)(t) * KT * D, vdst + (s) * VSLOT); glds16(vsrc + (size_t)(t) * KT * D + 2 * D, vdst + (s) * VSLOT + 1152); } while (0)
  DMA(0, 0); DMA(1, 1);
  bf16x8 qr[16];
#pragma unroll
  for (int s = 0; s < 16; ++s) qr[s] = *reinterpret_cast<const bf16x8*>(&Qw[(size_t)r32 * D + s * 16 + hi * 8]);
  const lds_cptr kp0 = (lds_cptr)lds + LDS_K + (r32 >> 1) * 1056 + (r32 & 1) * 512 + 16 * (hi ^ (r32 & 1));
  const int q4 = (lane & 15) >> 2, p4 = lane & 3, blk = (lane >> 4) & 1, vrow = 4 * hi + q4;
  const lds_cptr vp0 = (lds_cptr)lds + LDS_V + (vrow >> 1) * 1152 + (vrow & 1) * 512 + 64 * (0 ^ (vrow & 1)) + 32 * blk + 8 * p4;
  const lds_cptr vp1 = (lds_cptr)lds + LDS_V + (vrow >> 1) * 1152 + (vrow & 1) * 512 + 64 * (1 ^ (vrow & 1)) + 32 * blk + 8 * p4;
  f32x16 o[8];
#pragma unroll
  for (int i = 0; i < 8; ++i) o[i] = f32x16{};
  float mref = -1e30f, lsum = 0.f;
  int sl = 0;
  for (int t = 0; t < NT; ++t) {
    if (t + 1 < NT) asm volatile("s_waitcnt vmcnt(4) lgkmcnt(0)\n\ts_barrier" ::: "memory"); else asm volatile("s_waitcnt vmcnt(0) lgkmcnt(0)\n\ts_barrier" ::: "memory");
    { int s2 = sl + 2; if (s2 >= NS) s2 -= NS; if (t + 2 < NT) DMA(t + 2, s2); }
    f32x16 p = f32x16{};
    const lds_cptr kp = kp0 + sl * KSLOT;
#pragma unroll
    for (int s = 0; s < 16; ++s) { bf16x8 kf = *(const __attribute__((address_space(3))) bf16x8*)(kp + 32 * s); p = MFMA(kf, qr[s], p); }
    float a = MX3(p[0], p[1], p[2]), b = MX3(p[3], p[4], p[5]);
    a = MX3(a, p[6], p[7]); b = MX3(b, p[8], p[9]); a = MX3(a, p[10], p[11]); b = MX3(b, p[12], p[13]); a = MX3(a, p[14], p[15]);
    float m = __builtin_fmaxf(a, b);
    { auto rr = __builtin_amdgcn_permlane32_swap(__float_as_uint(m), __float_as_uint(m), false, false); m = __builtin_fmaxf(__uint_as_float(rr[0]), __uint_as_float(rr[1])); }
    m *= C2;
    if (__builtin_expect(__any(m - mref > THR), 0)) {
      const float mn = __builtin_fmaxf(m, mref); const float f = __builtin_amdgcn_exp2f(mref - mn); mref = mn; lsum *= f;
#pragma unroll
      for (int i = 0; i < 8; ++i)
#pragma unroll
        for (int r = 0; r < 16; ++r) o[i][r] *= f;
    }
    const float nm = -mref; float ps = 0.f;
#pragma unroll
    for (int r = 0; r < 16; ++r) { p[r] = __builtin_amdgcn_exp2f(__builtin_fmaf(p[r], C2, nm)); ps += p[r]; }
    lsum += ps;
    u32x4 w0 = {cvtpk(p[0], p[1]), cvtpk(p[2], p[3]), cvtpk(p[4], p[5]), cvtpk(p[6], p[7])};
    u32x4 w1 = {cvtpk(p[8], p[9]), cvtpk(p[10], p[11]), cvtpk(p[12], p[13]), cvtpk(p[14], p[15])};
    const bf16x8 pb0 = __builtin_bit_cast(bf16x8, w0), pb1 = __builtin_bit_cast(bf16x8, w1);
#pragma unroll
    for (int db = 0; db < 8; ++db) {
      const lds_cptr vp = ((db & 1) ? vp1 : vp0) + sl * VSLOT + 128 * (db >> 1);
      s16x4 l0 = vtr(vp), h0 = vtr(vp + 4 * 1152), l1 = vtr(vp + 8 * 1152), h1 = vtr(vp + 12 * 1152);
      bf16x8 v0 = {l0[0], l0[1], l0[2], l0[3], h0[0], h0[1], h0[2], h0[3]};
      bf16x8 v1 = {l1[0], l1[1], l1[2], l1[3], h1[0], h1[1], h1[2], h1[3]};
      o[db] = MFMA(v0, pb0, o[db]); o[db] = MFMA(v1, pb1, o[db]);
    }
    sl = sl + 1; if (sl >= NS) sl = 0;
  }
  { auto rr = __builtin_amdgcn_permlane32_swap(__float_as_uint(lsum), __float_as_uint(lsum), false, false); lsum = __uint_as_float(rr[0]) + __uint_as_float(rr[1]); }
  const float il = 1.0f / lsum;
  unsigned short* Ow = O + ((size_t)bh * SQ + wid * 32 + r32) * D;
#pragma unroll
  for (int db = 0; db < 8; ++db) {
#pragma unroll
    for (int g = 0; g < 4; g += 2) {
      unsigned a0 = cvtpk(o[db][4 * g] * il, o[db][4 * g + 1] * il), a1 = cvtpk(o[db][4 * g + 2] * il, o[db][4 * g + 3] * il);
      unsigned b0 = cvtpk(o[db][4 * g + 4] * il, o[db][4 * g + 5] * il), b1 = cvtpk(o[db][4 * g + 6] * il, o[db][4 * g + 7] * il);
      auto r0 = __builtin_amdgcn_permlane32_swap(a0, b0, false, false); auto r1 = __builtin_amdgcn_permlane32_swap(a1, b1, false, false);
      u32x4 w = {r0[0], r1[0], r0[1], r1[1]};
      *(u32x4*)(Ow + db * 32 + g * 8 + hi * 8) = w;
    }
  }
}
extern "C" void kernel_launch(void* const* d_in, const int* in_sizes, int n_in, void* d_out, int out_size, void* d_ws, size_t ws_size, hipStream_t stream) {
  hipFuncSetAttribute((const void*)attn_v0, hipFuncAttributeMaxDynamicSharedMemorySize, LDS_BYTES);
  attn_v0<<<512, 512, LDS_BYTES, stream>>>((const unsigned short*)d_in[0], (const unsigned short*)d_in[1], (const unsigned short*)d_in[2], (unsigned short*)d_out);
}
```

```cpp
#include <hip/hip_runtime.h>
#include <hip/hip_bf16.h>
typedef short bf16x8 __attribute__((ext_vector_type(8)));
typedef short s16x4 __attribute__((ext_vector_type(4)));
typedef float f32x16 __attribute__((ext_vector_type(16)));
typedef unsigned u32x4 __attribute__((ext_vector_type(4)));
typedef unsigned u32x2 __attribute__((ext_vector_type(2)));
typedef __attribute__((address_space(3))) const char* lds_cptr;
typedef short v4i16_t __attribute__((ext_vector_type(4)));
constexpr int D = 256, SQ = 256, SK = 512, KT = 32, NT = SK / KT;
constexpr int KSLOT = 16 * 1056, VSLOT = 16 * 1152, NS = 3;
constexpr int LDS_K = 0, LDS_V = NS * KSLOT, LDS_BYTES = NS * (KSLOT + VSLOT);
constexpr float C2 = 0.0625f * 1.4426950408889634f;
constexpr float THR = 8.f;
#define MFMA(a, b, c) __builtin_amdgcn_mfma_f32_32x32x16_bf16(a, b, c, 0, 0, 0)
#define SBAR() __builtin_amdgcn_sched_barrier(0)
__device__ __forceinline__ unsigned cvtpk(float lo, float hi) { unsigned r; asm("v_cvt_pk_bf16_f32 %0, %1, %2" : "=v"(r) : "v"(lo), "v"(hi)); return r; }
__device__ __forceinline__ void glds16(const void* g, unsigned lds_base) {
  unsigned sv; asm volatile("s_mov_b32 %0, m0\n\ts_mov_b32 m0, %2\n\ts_nop 0\n\tglobal_load_lds_dwordx4 %1, off\n\ts_mov_b32 m0, %0" : "=&s"(sv) : "v"(g), "s"(lds_base) : "memory"); }
__device__ __forceinline__ s16x4 vtr(lds_cptr p) { return __builtin_bit_cast(s16x4, __builtin_amdgcn_ds_read_tr16_b64_v4i16((__attribute__((address_space(3))) v4i16_t*)p)); }
#define MX3(a, b, c) __builtin_fmaxf(__builtin_fmaxf((a), (b)), (c))

__global__ __launch_bounds__(512, 2) void attn_v0(const unsigned short* __restrict__ Q, const unsigned short* __restrict__ K, const unsigned short* __restrict__ V, unsigned short* __restrict__ O) {
  extern __shared__ __attribute__((aligned(16))) char lds[];
  const int tid = threadIdx.x, lane = tid & 63, r32 = lane & 31, hi = lane >> 5;
  const int wid = __builtin_amdgcn_readfirstlane(tid >> 6);
  const int bx = blockIdx.x, x = bx & 7, y = bx >> 3;
  const int kvp = (y >> 1) * 8 + x, hsel = y & 1;
  const int bh = kvp * 2 + hsel;
  const unsigned short* Qw = Q + ((size_t)bh * SQ + wid * 32) * D;
  const unsigned short* Kb = K + (size_t)kvp * SK * D;
  const unsigned short* Vb = V + (size_t)kvp * SK * D;
  const unsigned lds0 = (unsigned)(uintptr_t)lds;
  const int e = lane >> 5, cl = lane & 31;
  const unsigned short* ksrc = Kb + (size_t)(4 * wid + e) * D + ((cl ^ e) * 8);
  const unsigned short* vsrc = Vb + (size_t)(4 * wid + e) * D + ((cl ^ (e << 2)) * 8);
  const unsigned kdst = lds0 + LDS_K + (2 * wid) * 1056, vdst = lds0 + LDS_V + (2 * wid) * 1152;
#define DMA(t, s) do { glds16(ksrc + (size_t)(t) * KT * D, kdst + (s) * KSLOT); glds16(ksrc + (size_t)(t) * KT * D + 2 * D, kdst + (s) * KSLOT + 1056); \
    glds16(vsrc + (size_t)(t) * KT * D, vdst + (s) * VSLOT); glds16(vsrc + (size_t)(t) * KT * D + 2 * D, vdst + (s) * VSLOT + 1152); } while (0)
  DMA(0, 0); DMA(1, 1);
  bf16x8 qr[16];
#pragma unroll
  for (int s = 0; s < 16; ++s) qr[s] = *reinterpret_cast<const bf16x8*>(&Qw[(size_t)r32 * D + s * 16 + hi * 8]);
  const lds_cptr kp0 = (lds_cptr)lds + LDS_K + (r32 >> 1) * 1056 + (r32 & 1) * 512 + 16 * (hi ^ (r32 & 1));
  const int q4 = (lane & 15) >> 2, p4 = lane & 3, blk = (lane >> 4) & 1, vrow = 4 * hi + q4;
  const lds_cptr vp0 = (lds_cptr)lds + LDS_V + (vrow >> 1) * 1152 + (vrow & 1) * 512 + 64 * (0 ^ (vrow & 1)) + 32 * blk + 8 * p4;
  const lds_cptr vp1 = (lds_cptr)lds + LDS_V + (vrow >> 1) * 1152 + (vrow & 1) * 512 + 64 * (1 ^ (vrow & 1)) + 32 * blk + 8 * p4;
  f32x16 o[8];
#pragma unroll
  for (int i = 0; i < 8; ++i) o[i] = f32x16{};
  float mref = -1e30f, lsum = 0.f;
  int sl = 0;
  for (int t = 0; t < NT; ++t) {
    if (t + 1 < NT) asm volatile("s_waitcnt vmcnt(4) lgkmcnt(0)\n\ts_barrier" ::: "memory"); else asm volatile("s_waitcnt vmcnt(0) lgkmcnt(0)\n\ts_barrier" ::: "memory");
    { int s2 = sl + 2; if (s2 >= NS) s2 -= NS; if (t + 2 < NT) DMA(t + 2, s2); }
    f32x16 p = f32x16{};
    const lds_cptr kp = kp0 + sl * KSLOT;
#pragma unroll
    for (int s = 0; s < 16; ++s) { bf16x8 kf = *(const __attribute__((address_space(3))) bf16x8*)(kp + 32 * s); p = MFMA(kf, qr[s], p); }
    float a = MX3(p[0], p[1], p[2]), b = MX3(p[3], p[4], p[5]);
    a = MX3(a, p[6], p[7]); b = MX3(b, p[8], p[9]); a = MX3(a, p[10], p[11]); b = MX3(b, p[12], p[13]); a = MX3(a, p[14], p[15]);
    float m = __builtin_fmaxf(a, b);
    { auto rr = __builtin_amdgcn_permlane32_swap(__float_as_uint(m), __float_as_uint(m), false, false); m = __builtin_fmaxf(__uint_as_float(rr[0]), __uint_as_float(rr[1])); }
    m *= C2;
    if (__builtin_expect(__any(m - mref > THR), 0)) {
      const float mn = __builtin_fmaxf(m, mref); const float f = __builtin_amdgcn_exp2f(mref - mn); mref = mn; lsum *= f;
#pragma unroll
      for (int i = 0; i < 8; ++i)
#pragma unroll
        for (int r = 0; r < 16; ++r) o[i][r] *= f;
    }
    const float nm = -mref; float ps = 0.f;
#pragma unroll
    for (int r = 0; r < 16; ++r) { p[r] = __builtin_amdgcn_exp2f(__builtin_fmaf(p[r], C2, nm)); ps += p[r]; }
    lsum += ps;
    u32x4 w0 = {cvtpk(p[0], p[1]), cvtpk(p[2], p[3]), cvtpk(p[4], p[5]), cvtpk(p[6], p[7])};
    u32x4 w1 = {cvtpk(p[8], p[9]), cvtpk(p[10], p[11]), cvtpk(p[12], p[13]), cvtpk(p[14], p[15])};
    const bf16x8 pb0 = __builtin_bit_cast(bf16x8, w0), pb1 = __builtin_bit_cast(bf16x8, w1);
#pragma unroll
    for (int db = 0; db < 8; ++db) {
      const lds_cptr vp = ((db & 1) ? vp1 : vp0) + sl * VSLOT + 128 * (db >> 1);
      s16x4 l0 = vtr(vp), h0 = vtr(vp + 4 * 1152), l1 = vtr(vp + 8 * 1152), h1 = vtr(vp + 12 * 1152);
      bf16x8 v0 = {l0[0], l0[1], l0[2], l0[3], h0[0], h0[1], h0[2], h0[3]};
      bf16x8 v1 = {l1[0], l1[1], l1[2], l1[3], h1[0], h1[1], h1[2], h1[3]};
      o[db] = MFMA(v0, pb0, o[db]); o[db] = MFMA(v1, pb1, o[db]);
    }
    sl = sl + 1; if (sl >= NS) sl = 0;
  }
  { auto rr = __builtin_amdgcn_permlane32_swap(__float_as_uint(lsum), __float_as_uint(lsum), false, false); lsum = __uint_as_float(rr[0]) + __uint_as_float(rr[1]); }
  const float il = 1.0f / lsum;
  unsigned short* Ow = O + ((size_t)bh * SQ + wid * 32 + r32) * D;
#pragma unroll
  for (int db = 0; db < 8; ++db) {
#pragma unroll
    for (int g = 0; g < 4; g += 2) {
      unsigned a0 = cvtpk(o[db][4 * g] * il, o[db][4 * g + 1] * il), a1 = cvtpk(o[db][4 * g + 2] * il, o[db][4 * g + 3] * il);
      unsigned b0 = cvtpk(o[db][4 * g + 4] * il, o[db][4 * g + 5] * il), b1 = cvtpk(o[db][4 * g + 6] * il, o[db][4 * g + 7] * il);
      auto r0 = __builtin_amdgcn_permlane32_swap(a0, b0, false, false); auto r1 = __builtin_amdgcn_permlane32_swap(a1, b1, false, false);
      u32x4 w = {r0[0], r1[0], r0[1], r1[1]};
      *(u32x4*)(Ow + db * 32 + g * 8 + hi * 8) = w;
    }
  }
}

template <int NT> __global__ __launch_bounds__(256) void probe(const u32x4* __restrict__ q, const u32x4* __restrict__ k, const u32x4* __restrict__ v, u32x4* __restrict__ o, int n) {
  int i = blockIdx.x * 256 + threadIdx.x; int stride = gridDim.x * 256;
  for (; i < n; i += stride) {
    u32x4 a, b, c;
    if (NT) { a = __builtin_nontemporal_load(q + i); b = __builtin_nontemporal_load(k + i); c = __builtin_nontemporal_load(v + i); }
    else { a = q[i]; b = k[i]; c = v[i]; }
    if (NT) __builtin_nontemporal_store(a ^ b ^ c, o + i); else o[i] = a ^ b ^ c;
  }
}
__global__ void spin(unsigned* w, int ticks) {
  unsigned long long t0 = __builtin_amdgcn_s_memrealtime(); float x = threadIdx.x;
  while (__builtin_amdgcn_s_memrealtime() - t0 < (unsigned long long)ticks) { for (int i = 0; i < 64; ++i) x = x * 1.0001f + 0.5f; }
  if (x == 12345.f) w[0] = 1;
}
__global__ void touch(const unsigned* q, const unsigned* k, const unsigned* v, const unsigned* o, unsigned* w, int stride_dw, int n) {
  int i = blockIdx.x * 256 + threadIdx.x; if (i >= n) return;
  unsigned a = __builtin_nontemporal_load(q + (size_t)i * stride_dw) ^ __builtin_nontemporal_load(k + (size_t)i * stride_dw) ^ __builtin_nontemporal_load(v + (size_t)i * stride_dw) ^ __builtin_nontemporal_load(o + (size_t)i * stride_dw);
  if (a == 0x12345678u) w[i] = a;
}
#define ATT() attn_v0<<<512, 512, LDS_BYTES, stream>>>((const unsigned short*)d_in[0], (const unsigned short*)d_in[1], (const unsigned short*)d_in[2], (unsigned short*)d_out)
extern "C" void kernel_launch(void* const* d_in, const int* in_sizes, int n_in, void* d_out, int out_size, void* d_ws, size_t ws_size, hipStream_t stream) {
  hipFuncSetAttribute((const void*)attn_v0, hipFuncAttributeMaxDynamicSharedMemorySize, LDS_BYTES);
  int n = out_size / 8;
  touch<<<64, 256, 0, stream>>>((const unsigned*)d_in[0], (const unsigned*)d_in[1], (const unsigned*)d_in[2], (const unsigned*)d_out, (unsigned*)d_ws, 1024, 16384); ATT();
}
```

```cpp
#define KVNT 0
#define QNT 1
#define OMODE 1
#include <hip/hip_runtime.h>
#include <hip/hip_bf16.h>
typedef short bf16x8 __attribute__((ext_vector_type(8)));
typedef short s16x4 __attribute__((ext_vector_type(4)));
typedef float f32x16 __attribute__((ext_vector_type(16)));
typedef unsigned u32x4 __attribute__((ext_vector_type(4)));
typedef unsigned u32x2 __attribute__((ext_vector_type(2)));
typedef __attribute__((address_space(3))) const char* lds_cptr;
typedef short v4i16_t __attribute__((ext_vector_type(4)));
constexpr int D = 256, SQ = 256, SK = 512, KT = 32, NT = SK / KT;
constexpr int KSLOT = 16 * 1056, VSLOT = 16 * 1152, NS = 3;
constexpr int LDS_K = 0, LDS_V = NS * KSLOT, LDS_BYTES = NS * (KSLOT + VSLOT);
constexpr float C2 = 0.0625f * 1.4426950408889634f;
constexpr float THR = 8.f;
#define MFMA(a, b, c) __builtin_amdgcn_mfma_f32_32x32x16_bf16(a, b, c, 0, 0, 0)
#define SBAR() __builtin_amdgcn_sched_barrier(0)
__device__ __forceinline__ unsigned cvtpk(float lo, float hi) { unsigned r; asm("v_cvt_pk_bf16_f32 %0, %1, %2" : "=v"(r) : "v"(lo), "v"(hi)); return r; }
__device__ __forceinline__ void glds16(const void* g, unsigned lds_base) {
  unsigned sv; asm volatile("s_mov_b32 %0, m0\n\ts_mov_b32 m0, %2\n\ts_nop 0\n\tglobal_load_lds_dwordx4 %1, off\n\ts_mov_b32 m0, %0" : "=&s"(sv) : "v"(g), "s"(lds_base) : "memory"); }
__device__ __forceinline__ s16x4 vtr(lds_cptr p) { return __builtin_bit_cast(s16x4, __builtin_amdgcn_ds_read_tr16_b64_v4i16((__attribute__((address_space(3))) v4i16_t*)p)); }
#define MX3(a, b, c) __builtin_fmaxf(__builtin_fmaxf((a), (b)), (c))

__global__ __launch_bounds__(512, 2) void attn_v0(const unsigned short* __restrict__ Q, const unsigned short* __restrict__ K, const unsigned short* __restrict__ V, unsigned short* __restrict__ O) {
  extern __shared__ __attribute__((aligned(16))) char lds[];
  const int tid = threadIdx.x, lane = tid & 63, r32 = lane & 31, hi = lane >> 5;
  const int wid = __builtin_amdgcn_readfirstlane(tid >> 6);
  const int bx = blockIdx.x, x = bx & 7, y = bx >> 3;
  const int kvp = (y >> 1) * 8 + x, hsel = y & 1;
  const int bh = kvp * 2 + hsel;
  const unsigned short* Qw = Q + ((size_t)bh * SQ + wid * 32) * D;
  const unsigned short* Kb = K + (size_t)kvp * SK * D;
  const unsigned short* Vb = V + (size_t)kvp * SK * D;
  const unsigned lds0 = (unsigned)(uintptr_t)lds;
  const int e = lane >> 5, cl = lane & 31;
  const unsigned short* ksrc = Kb + (size_t)(4 * wid + e) * D + ((cl ^ e) * 8);
  const unsigned short* vsrc = Vb + (size_t)(4 * wid + e) * D + ((cl ^ (e << 2)) * 8);
  const unsigned kdst = lds0 + LDS_K + (2 * wid) * 1056, vdst = lds0 + LDS_V + (2 * wid) * 1152;
#define DMA(t, s) do { glds16(ksrc + (size_t)(t) * KT * D, kdst + (s) * KSLOT); glds16(ksrc + (size_t)(t) * KT * D + 2 * D, kdst + (s) * KSLOT + 1056); \
    glds16(vsrc + (size_t)(t) * KT * D, vdst + (s) * VSLOT); glds16(vsrc + (size_t)(t) * KT * D + 2 * D, vdst + (s) * VSLOT + 1152); } while (0)
  DMA(0, 0); DMA(1, 1);
  bf16x8 qr[16];
#pragma unroll
  for (int s = 0; s < 16; ++s) qr[s] = *reinterpret_cast<const bf16x8*>(&Qw[(size_t)r32 * D + s * 16 + hi * 8]);
  const lds_cptr kp0 = (lds_cptr)lds + LDS_K + (r32 >> 1) * 1056 + (r32 & 1) * 512 + 16 * (hi ^ (r32 & 1));
  const int q4 = (lane & 15) >> 2, p4 = lane & 3, blk = (lane >> 4) & 1, vrow = 4 * hi + q4;
  const lds_cptr vp0 = (lds_cptr)lds + LDS_V + (vrow >> 1) * 1152 + (vrow & 1) * 512 + 64 * (0 ^ (vrow & 1)) + 32 * blk + 8 * p4;
  const lds_cptr vp1 = (lds_cptr)lds + LDS_V + (vrow >> 1) * 1152 + (vrow & 1) * 512 + 64 * (1 ^ (vrow & 1)) + 32 * blk + 8 * p4;
  f32x16 o[8];
#pragma unroll
  for (int i = 0; i < 8; ++i) o[i] = f32x16{};
  float mref = -1e30f, lsum = 0.f;
  int sl = 0;
  for (int t = 0; t < NT; ++t) {
    if (t + 1 < NT) asm volatile("s_waitcnt vmcnt(4) lgkmcnt(0)\n\ts_barrier" ::: "memory"); else asm volatile("s_waitcnt vmcnt(0) lgkmcnt(0)\n\ts_barrier" ::: "memory");
    { int s2 = sl + 2; if (s2 >= NS) s2 -= NS; if (t + 2 < NT) DMA(t + 2, s2); }
    f32x16 p = f32x16{};
    const lds_cptr kp = kp0 + sl * KSLOT;
#pragma unroll
    for (int s = 0; s < 16; ++s) { bf16x8 kf = *(const __attribute__((address_space(3))) bf16x8*)(kp + 32 * s); p = MFMA(kf, qr[s], p); }
    float a = MX3(p[0], p[1], p[2]), b = MX3(p[3], p[4], p[5]);
    a = MX3(a, p[6], p[7]); b = MX3(b, p[8], p[9]); a = MX3(a, p[10], p[11]); b = MX3(b, p[12], p[13]); a = MX3(a, p[14], p[15]);
    float m = __builtin_fmaxf(a, b);
    { auto rr = __builtin_amdgcn_permlane32_swap(__float_as_uint(m), __float_as_uint(m), false, false); m = __builtin_fmaxf(__uint_as_float(rr[0]), __uint_as_float(rr[1])); }
    m *= C2;
    if (__builtin_expect(__any(m - mref > THR), 0)) {
      const float mn = __builtin_fmaxf(m, mref); const float f = __builtin_amdgcn_exp2f(mref - mn); mref = mn; lsum *= f;
#pragma unroll
      for (int i = 0; i < 8; ++i)
#pragma unroll
        for (int r = 0; r < 16; ++r) o[i][r] *= f;
    }
    const float nm = -mref; float ps = 0.f;
#pragma unroll
    for (int r = 0; r < 16; ++r) { p[r] = __builtin_amdgcn_exp2f(__builtin_fmaf(p[r], C2, nm)); ps += p[r]; }
    lsum += ps;
    u32x4 w0 = {cvtpk(p[0], p[1]), cvtpk(p[2], p[3]), cvtpk(p[4], p[5]), cvtpk(p[6], p[7])};
    u32x4 w1 = {cvtpk(p[8], p[9]), cvtpk(p[10], p[11]), cvtpk(p[12], p[13]), cvtpk(p[14], p[15])};
    const bf16x8 pb0 = __builtin_bit_cast(bf16x8, w0), pb1 = __builtin_bit_cast(bf16x8, w1);
#pragma unroll
    for (int db = 0; db < 8; ++db) {
      const lds_cptr vp = ((db & 1) ? vp1 : vp0) + sl * VSLOT + 128 * (db >> 1);
      s16x4 l0 = vtr(vp), h0 = vtr(vp + 4 * 1152), l1 = vtr(vp + 8 * 1152), h1 = vtr(vp + 12 * 1152);
      bf16x8 v0 = {l0[0], l0[1], l0[2], l0[3], h0[0], h0[1], h0[2], h0[3]};
      bf16x8 v1 = {l1[0], l1[1], l1[2], l1[3], h1[0], h1[1], h1[2], h1[3]};
      o[db] = MFMA(v0, pb0, o[db]); o[db] = MFMA(v1, pb1, o[db]);
    }
    sl = sl + 1; if (sl >= NS) sl = 0;
  }
  { auto rr = __builtin_amdgcn_permlane32_swap(__float_as_uint(lsum), __float_as_uint(lsum), false, false); lsum = __uint_as_float(rr[0]) + __uint_as_float(rr[1]); }
  const float il = 1.0f / lsum;
  unsigned short* Ow = O + ((size_t)bh * SQ + wid * 32 + r32) * D;
#pragma unroll
  for (int db = 0; db < 8; ++db) {
#pragma unroll
    for (int g = 0; g < 4; g += 2) {
      unsigned a0 = cvtpk(o[db][4 * g] * il, o[db][4 * g + 1] * il), a1 = cvtpk(o[db][4 * g + 2] * il, o[db][4 * g + 3] * il);
      unsigned b0 = cvtpk(o[db][4 * g + 4] * il, o[db][4 * g + 5] * il), b1 = cvtpk(o[db][4 * g + 6] * il, o[db][4 * g + 7] * il);
      auto r0 = __builtin_amdgcn_permlane32_swap(a0, b0, false, false); auto r1 = __builtin_amdgcn_permlane32_swap(a1, b1, false, false);
      u32x4 w = {r0[0], r1[0], r0[1], r1[1]};
      *(u32x4*)(Ow + db * 32 + g * 8 + hi * 8) = w;
    }
  }
}
#include <hip/hip_runtime.h>

#define RING 9
#define DELAY 0
#define VFULL 0
#define ROT 0
constexpr int SLOT = 16896;
constexpr int SK_LDS = RING * SLOT;
template <int NT> __device__ __forceinline__ void sglds16(const void* g, unsigned lds_base) {
  unsigned sv;
  if (NT) asm volatile("s_mov_b32 %0, m0\n\ts_mov_b32 m0, %2\n\ts_nop 0\n\tglobal_load_lds_dwordx4 %1, off nt\n\ts_mov_b32 m0, %0" : "=&s"(sv) : "v"(g), "s"(lds_base) : "memory");
  else asm volatile("s_mov_b32 %0, m0\n\ts_mov_b32 m0, %2\n\ts_nop 0\n\tglobal_load_lds_dwordx4 %1, off\n\ts_mov_b32 m0, %0" : "=&s"(sv) : "v"(g), "s"(lds_base) : "memory");
}
__global__ __launch_bounds__(512, 2) void skel(const char* __restrict__ Q, const char* __restrict__ K, const char* __restrict__ V, char* __restrict__ O, unsigned long long* ws) {
  unsigned long long tstart = __builtin_amdgcn_s_memrealtime();
  extern __shared__ __attribute__((aligned(16))) char lds[];
  const int tid = threadIdx.x, lane = tid & 63;
  const int wid = __builtin_amdgcn_readfirstlane(tid >> 6);
  const int bx = blockIdx.x, x = bx & 7, y = bx >> 3;
  const int hsel = y & 1, pr = (y >> 1) * 8 + x;
  const unsigned lds0 = (unsigned)(uintptr_t)lds;
  const int e = lane >> 5, cl = lane & 31;
  const unsigned rowoff = (4 * wid + e) * 512 + ((cl ^ e) * 16);
  const unsigned voff = (16 * wid + (lane >> 3)) * 512 + (((lane & 7) ^ (((lane >> 4) & 1) << 2)) * 16);
  auto issue = [&](int j) {
    if (j >= 80) return;
    const unsigned dst = lds0 + (j % RING) * SLOT;
    int kind, item, t;
    if (j < 8) { kind = 0; item = 0; t = j; }
    else if (j < 24) { kind = 1; item = 0; t = j - 8; }
    else if (j < 48) { int u = j - 24; if (u % 3 == 2) { kind = 0; item = 1; t = u / 3; } else { kind = 2; item = 0; t = (u / 3) * 2 + (u % 3); } }
    else if (j < 64) { kind = 1; item = 1; t = j - 48; }
    else { kind = 2; item = 1; t = j - 64; }
    const int kvp = item * 128 + pr, bh = kvp * 2 + hsel;
    if (kind == 0) { const char* s = Q + (size_t)bh * 131072 + t * 16384 + rowoff; sglds16<QNT>(s, dst + 2 * wid * 1056); sglds16<QNT>(s + 1024, dst + 2 * wid * 1056 + 1056); }
    else if (kind == 1) { const char* s = K + (size_t)kvp * 262144 + t * 16384 + rowoff; sglds16<KVNT>(s, dst + 2 * wid * 1056); sglds16<KVNT>(s + 1024, dst + 2 * wid * 1056 + 1056); }
    else {
      if (VFULL) { const char* s = V + (size_t)kvp * 262144 + t * 16384 + rowoff; sglds16<KVNT>(s, dst + 2 * wid * 1056); sglds16<KVNT>(s + 1024, dst + 2 * wid * 1056 + 1056); }
      else { const int g = ((t >> 2) + (ROT ? pr : 0)) & 3, kq = t & 3; const char* s = V + (size_t)kvp * 262144 + kq * 65536 + g * 128 + voff; sglds16<KVNT>(s, dst + 2 * wid * 1024); sglds16<KVNT>(s + 4096, dst + 2 * wid * 1024 + 1024); }
    }
  };
  for (int j = 0; j < RING - 1; ++j) issue(j);
  u32x4 w = {(unsigned)tid, 1u, 2u, 3u};
  for (int j = 0; j < 80; ++j) {
    asm volatile("s_waitcnt vmcnt(%0)\n\ts_barrier" :: "i"(2 * (RING - 2)) : "memory");
    issue(j + RING - 1);
    if (DELAY) { for (int d = 0; d < DELAY; ++d) __builtin_amdgcn_s_sleep(8); }
    int item = -1, g = 0;
    if (j >= 24 && j < 48) { int u = j - 24; if (u % 6 == 4) { item = 0; g = u / 6; } }
    else if (j >= 64) { int t = j - 64; if ((t & 3) == 3) { item = 1; g = t >> 2; } }
    if (item >= 0) {
      if (ROT) g = (g + pr) & 3;
      const int bh = (item * 128 + pr) * 2 + hsel;
      char* ob = O + (size_t)bh * 131072 + wid * 16384 + g * 128;
      if (OMODE == 0) { for (int i = 0; i < 4; ++i) *(u32x4*)(ob + (lane & 31) * 512 + i * 32 + (lane >> 5) * 16) = w; }
      else if (OMODE == 1) { for (int i = 0; i < 4; ++i) *(u32x4*)(ob + (i * 8 + (lane >> 3)) * 512 + (lane & 7) * 16) = w; }
      else if (OMODE == 2) { for (int i = 0; i < 4; ++i) __builtin_nontemporal_store(w, (u32x4*)(ob + (i * 8 + (lane >> 3)) * 512 + (lane & 7) * 16)); }
      else if (OMODE == 3) { for (int i = 0; i < 4; ++i) *(u32x4*)(ob + ((i & 1) * 16 + (lane & 15)) * 512 + (i >> 1) * 64 + (lane >> 4) * 16) = w; }
      else if (OMODE == 4) { for (int i = 0; i < 4; ++i) __builtin_nontemporal_store(w, (u32x4*)(ob + ((i & 1) * 16 + (lane & 15)) * 512 + (i >> 1) * 64 + (lane >> 4) * 16)); }
    }
  }
  asm volatile("s_waitcnt vmcnt(0)" ::: "memory"); __syncthreads();
  if (tid == 0) { unsigned long long tend = __builtin_amdgcn_s_memrealtime(); atomicMax(&ws[0], ~tstart); atomicMax(&ws[1], tend); }
}

__global__ void spin10(unsigned long long* ws) {
  unsigned long long T = ws[1] - (~ws[0]);
  unsigned long long t0 = __builtin_amdgcn_s_memrealtime();
  if (T > 100000ull) T = 100000ull;
  while (__builtin_amdgcn_s_memrealtime() - t0 < 10 * T) __builtin_amdgcn_s_sleep(2);
}
extern "C" void kernel_launch(void* const* d_in, const int* in_sizes, int n_in, void* d_out, int out_size, void* d_ws, size_t ws_size, hipStream_t stream) {
  hipFuncSetAttribute((const void*)attn_v0, hipFuncAttributeMaxDynamicSharedMemorySize, LDS_BYTES);
  hipFuncSetAttribute((const void*)skel, hipFuncAttributeMaxDynamicSharedMemorySize, SK_LDS);
  hipMemsetAsync(d_ws, 0, 16, stream);
  skel<<<256, 512, SK_LDS, stream>>>((const char*)d_in[0], (const char*)d_in[1], (const char*)d_in[2], (char*)d_out, (unsigned long long*)d_ws);
  spin10<<<1, 64, 0, stream>>>((unsigned long long*)d_ws);
  attn_v0<<<512, 512, LDS_BYTES, stream>>>((const unsigned short*)d_in[0], (const unsigned short*)d_in[1], (const unsigned short*)d_in[2], (unsigned short*)d_out);
}
```

```cpp
#define WGSEL 5
#define MODE 0
#include <hip/hip_runtime.h>
#include <hip/hip_bf16.h>
typedef short bf16x8 __attribute__((ext_vector_type(8)));
typedef short s16x4 __attribute__((ext_vector_type(4)));
typedef float f32x16 __attribute__((ext_vector_type(16)));
typedef unsigned u32x4 __attribute__((ext_vector_type(4)));
typedef unsigned u32x2 __attribute__((ext_vector_type(2)));
typedef __attribute__((address_space(3))) const char* lds_cptr;
typedef short v4i16_t __attribute__((ext_vector_type(4)));
constexpr int D = 256, SQ = 256, SK = 512, KT = 32, NT = SK / KT;
constexpr int KSLOT = 16 * 1056, VSLOT = 16 * 1152, NS = 3;
constexpr int LDS_K = 0, LDS_V = NS * KSLOT, LDS_BYTES = NS * (KSLOT + VSLOT);
constexpr float C2 = 0.0625f * 1.4426950408889634f;
constexpr float THR = 8.f;
#define MFMA(a, b, c) __builtin_amdgcn_mfma_f32_32x32x16_bf16(a, b, c, 0, 0, 0)
#define SBAR() __builtin_amdgcn_sched_barrier(0)
__device__ __forceinline__ unsigned cvtpk(float lo, float hi) { unsigned r; asm("v_cvt_pk_bf16_f32 %0, %1, %2" : "=v"(r) : "v"(lo), "v"(hi)); return r; }
__device__ __forceinline__ void glds16(const void* g, unsigned lds_base) {
  unsigned sv; asm volatile("s_mov_b32 %0, m0\n\ts_mov_b32 m0, %2\n\ts_nop 0\n\tglobal_load_lds_dwordx4 %1, off\n\ts_mov_b32 m0, %0" : "=&s"(sv) : "v"(g), "s"(lds_base) : "memory"); }
__device__ __forceinline__ s16x4 vtr(lds_cptr p) { return __builtin_bit_cast(s16x4, __builtin_amdgcn_ds_read_tr16_b64_v4i16((__attribute__((address_space(3))) v4i16_t*)p)); }
#define MX3(a, b, c) __builtin_fmaxf(__builtin_fmaxf((a), (b)), (c))

__global__ __launch_bounds__(512, 2) void attn_v0(const unsigned short* __restrict__ Q, const unsigned short* __restrict__ K, const unsigned short* __restrict__ V, unsigned short* __restrict__ O, unsigned long long* ws) {
  unsigned long long c0 = __builtin_amdgcn_s_memtime(), r0 = __builtin_amdgcn_s_memrealtime();
  extern __shared__ __attribute__((aligned(16))) char lds[];
  const int tid = threadIdx.x, lane = tid & 63, r32 = lane & 31, hi = lane >> 5;
  const int wid = __builtin_amdgcn_readfirstlane(tid >> 6);
  const int bx = blockIdx.x, x = bx & 7, y = bx >> 3;
  const int kvp = (y >> 1) * 8 + x, hsel = y & 1;
  const int bh = kvp * 2 + hsel;
  const unsigned short* Qw = Q + ((size_t)bh * SQ + wid * 32) * D;
  const unsigned short* Kb = K + (size_t)kvp * SK * D;
  const unsigned short* Vb = V + (size_t)kvp * SK * D;
  const unsigned lds0 = (unsigned)(uintptr_t)lds;
  const int e = lane >> 5, cl = lane & 31;
  const unsigned short* ksrc = Kb + (size_t)(4 * wid + e) * D + ((cl ^ e) * 8);
  const unsigned short* vsrc = Vb + (size_t)(4 * wid + e) * D + ((cl ^ (e << 2)) * 8);
  const unsigned kdst = lds0 + LDS_K + (2 * wid) * 1056, vdst = lds0 + LDS_V + (2 * wid) * 1152;
#define DMA(t, s) do { glds16(ksrc + (size_t)(t) * KT * D, kdst + (s) * KSLOT); glds16(ksrc + (size_t)(t) * KT * D + 2 * D, kdst + (s) * KSLOT + 1056); \
    glds16(vsrc + (size_t)(t) * KT * D, vdst + (s) * VSLOT); glds16(vsrc + (size_t)(t) * KT * D + 2 * D, vdst + (s) * VSLOT + 1152); } while (0)
  DMA(0, 0); DMA(1, 1);
  bf16x8 qr[16];
#pragma unroll
  for (int s = 0; s < 16; ++s) qr[s] = *reinterpret_cast<const bf16x8*>(&Qw[(size_t)r32 * D + s * 16 + hi * 8]);
  const lds_cptr kp0 = (lds_cptr)lds + LDS_K + (r32 >> 1) * 1056 + (r32 & 1) * 512 + 16 * (hi ^ (r32 & 1));
  const int q4 = (lane & 15) >> 2, p4 = lane & 3, blk = (lane >> 4) & 1, vrow = 4 * hi + q4;
  const lds_cptr vp0 = (lds_cptr)lds + LDS_V + (vrow >> 1) * 1152 + (vrow & 1) * 512 + 64 * (0 ^ (vrow & 1)) + 32 * blk + 8 * p4;
  const lds_cptr vp1 = (lds_cptr)lds + LDS_V + (vrow >> 1) * 1152 + (vrow & 1) * 512 + 64 * (1 ^ (vrow & 1)) + 32 * blk + 8 * p4;
  f32x16 o[8];
#pragma unroll
  for (int i = 0; i < 8; ++i) o[i] = f32x16{};
  float mref = -1e30f, lsum = 0.f;
  int sl = 0;
  for (int t = 0; t < NT; ++t) {
    if (t + 1 < NT) asm volatile("s_waitcnt vmcnt(4) lgkmcnt(0)\n\ts_barrier" ::: "memory"); else asm volatile("s_waitcnt vmcnt(0) lgkmcnt(0)\n\ts_barrier" ::: "memory");
    { int s2 = sl + 2; if (s2 >= NS) s2 -= NS; if (t + 2 < NT) DMA(t + 2, s2); }
    f32x16 p = f32x16{};
    const lds_cptr kp = kp0 + sl * KSLOT;
#pragma unroll
    for (int s = 0; s < 16; ++s) { bf16x8 kf = *(const __attribute__((address_space(3))) bf16x8*)(kp + 32 * s); p = MFMA(kf, qr[s], p); }
    float a = MX3(p[0], p[1], p[2]), b = MX3(p[3], p[4], p[5]);
    a = MX3(a, p[6], p[7]); b = MX3(b, p[8], p[9]); a = MX3(a, p[10], p[11]); b = MX3(b, p[12], p[13]); a = MX3(a, p[14], p[15]);
    float m = __builtin_fmaxf(a, b);
    { auto rr = __builtin_amdgcn_permlane32_swap(__float_as_uint(m), __float_as_uint(m), false, false); m = __builtin_fmaxf(__uint_as_float(rr[0]), __uint_as_float(rr[1])); }
    m *= C2;
    if (__builtin_expect(__any(m - mref > THR), 0)) {
      const float mn = __builtin_fmaxf(m, mref); const float f = __builtin_amdgcn_exp2f(mref - mn); mref = mn; lsum *= f;
#pragma unroll
      for (int i = 0; i < 8; ++i)
#pragma unroll
        for (int r = 0; r < 16; ++r) o[i][r] *= f;
    }
    const float nm = -mref; float ps = 0.f;
#pragma unroll
    for (int r = 0; r < 16; ++r) { p[r] = __builtin_amdgcn_exp2f(__builtin_fmaf(p[r], C2, nm)); ps += p[r]; }
    lsum += ps;
    u32x4 w0 = {cvtpk(p[0], p[1]), cvtpk(p[2], p[3]), cvtpk(p[4], p[5]), cvtpk(p[6], p[7])};
    u32x4 w1 = {cvtpk(p[8], p[9]), cvtpk(p[10], p[11]), cvtpk(p[12], p[13]), cvtpk(p[14], p[15])};
    const bf16x8 pb0 = __builtin_bit_cast(bf16x8, w0), pb1 = __builtin_bit_cast(bf16x8, w1);
#pragma unroll
    for (int db = 0; db < 8; ++db) {
      const lds_cptr vp = ((db & 1) ? vp1 : vp0) + sl * VSLOT + 128 * (db >> 1);
      s16x4 l0 = vtr(vp), h0 = vtr(vp + 4 * 1152), l1 = vtr(vp + 8 * 1152), h1 = vtr(vp + 12 * 1152);
      bf16x8 v0 = {l0[0], l0[1], l0[2], l0[3], h0[0], h0[1], h0[2], h0[3]};
      bf16x8 v1 = {l1[0], l1[1], l1[2], l1[3], h1[0], h1[1], h1[2], h1[3]};
      o[db] = MFMA(v0, pb0, o[db]); o[db] = MFMA(v1, pb1, o[db]);
    }
    sl = sl + 1; if (sl >= NS) sl = 0;
  }
  { auto rr = __builtin_amdgcn_permlane32_swap(__float_as_uint(lsum), __float_as_uint(lsum), false, false); lsum = __uint_as_float(rr[0]) + __uint_as_float(rr[1]); }
  const float il = 1.0f / lsum;
  unsigned short* Ow = O + ((size_t)bh * SQ + wid * 32 + r32) * D;
#pragma unroll
  for (int db = 0; db < 8; ++db) {
#pragma unroll
    for (int g = 0; g < 4; g += 2) {
      unsigned a0 = cvtpk(o[db][4 * g] * il, o[db][4 * g + 1] * il), a1 = cvtpk(o[db][4 * g + 2] * il, o[db][4 * g + 3] * il);
      unsigned b0 = cvtpk(o[db][4 * g + 4] * il, o[db][4 * g + 5] * il), b1 = cvtpk(o[db][4 * g + 6] * il, o[db][4 * g + 7] * il);
      auto r0 = __builtin_amdgcn_permlane32_swap(a0, b0, false, false); auto r1 = __builtin_amdgcn_permlane32_swap(a1, b1, false, false);
      u32x4 w = {r0[0], r1[0], r0[1], r1[1]};
      *(u32x4*)(Ow + db * 32 + g * 8 + hi * 8) = w;
    }
  }
  if (blockIdx.x == WGSEL && threadIdx.x == 0) { unsigned long long c1 = __builtin_amdgcn_s_memtime(), r1 = __builtin_amdgcn_s_memrealtime(); ws[0] = c1 - c0; ws[1] = r1 - r0; }
}

__global__ void spinf(unsigned long long* ws) {
  unsigned long long T = MODE ? ws[1] * 10 : ws[0] * 1000ull / ws[1];
  if (T > 100000ull) T = 100000ull;
  unsigned long long t0 = __builtin_amdgcn_s_memrealtime();
  while (__builtin_amdgcn_s_memrealtime() - t0 < T) __builtin_amdgcn_s_sleep(2);
}
extern "C" void kernel_launch(void* const* d_in, const int* in_sizes, int n_in, void* d_out, int out_size, void* d_ws, size_t ws_size, hipStream_t stream) {
  hipFuncSetAttribute((const void*)attn_v0, hipFuncAttributeMaxDynamicSharedMemorySize, LDS_BYTES);
  attn_v0<<<512, 512, LDS_BYTES, stream>>>((const unsigned short*)d_in[0], (const unsigned short*)d_in[1], (const unsigned short*)d_in[2], (unsigned short*)d_out, (unsigned long long*)d_ws);
  spinf<<<1, 64, 0, stream>>>((unsigned long long*)d_ws);
}
```

```cpp
#include <hip/hip_runtime.h>
typedef short bf16x8 __attribute__((ext_vector_type(8)));
typedef float f32x16 __attribute__((ext_vector_type(16)));
typedef unsigned u32x4 __attribute__((ext_vector_type(4)));
constexpr int D = 256, SQ = 256, SK = 512;
constexpr int LDS_BYTES = 163840;
constexpr float C2 = 0.0625f * 1.4426950408889634f;
#define MFMA(a, b, c) __builtin_amdgcn_mfma_f32_32x32x16_bf16(a, b, c, 0, 0, 0)
__device__ __forceinline__ unsigned cvtpk(float lo, float hi) { unsigned r; asm("v_cvt_pk_bf16_f32 %0, %1, %2" : "=v"(r) : "v"(lo), "v"(hi)); return r; }

__device__ __forceinline__ void slow_rows(const unsigned short* __restrict__ Qw, const unsigned short* __restrict__ Kb, const unsigned short* __restrict__ Vb,
                                          unsigned short* __restrict__ Ow, int lane) {
  const int r32 = lane & 31, hi = lane >> 5;
  bf16x8 qr[16];
#pragma unroll
  for (int s = 0; s < 16; ++s) qr[s] = *reinterpret_cast<const bf16x8*>(&Qw[(size_t)r32 * D + s * 16 + hi * 8]);
  f32x16 o[8];
#pragma unroll
  for (int i = 0; i < 8; ++i) o[i] = f32x16{};
  float mref = -3.0e38f, lsum = 0.f;
#pragma unroll 1
  for (int t = 0; t < SK / 32; ++t) {
    f32x16 p = f32x16{};
    const unsigned short* kp = Kb + (size_t)(t * 32 + r32) * D + hi * 8;
#pragma unroll
    for (int s = 0; s < 16; ++s) { bf16x8 kf = *reinterpret_cast<const bf16x8*>(kp + 16 * s); p = MFMA(kf, qr[s], p); }
    float m = p[0];
#pragma unroll
    for (int r = 1; r < 16; ++r) m = __builtin_fmaxf(m, p[r]);
    { auto rr = __builtin_amdgcn_permlane32_swap(__float_as_uint(m), __float_as_uint(m), false, false); m = __builtin_fmaxf(__uint_as_float(rr[0]), __uint_as_float(rr[1])); }
    const float mn = __builtin_fmaxf(m, mref);
    const float f = __builtin_amdgcn_exp2f((mref - mn) * C2);
    mref = mn; lsum *= f;
#pragma unroll
    for (int i = 0; i < 8; ++i)
#pragma unroll
      for (int r = 0; r < 16; ++r) o[i][r] *= f;
    float ps = 0.f;
#pragma unroll
    for (int r = 0; r < 16; ++r) { p[r] = __builtin_amdgcn_exp2f((p[r] - mref) * C2); ps += p[r]; }
    lsum += ps;
    u32x4 w0 = {cvtpk(p[0], p[1]), cvtpk(p[2], p[3]), cvtpk(p[4], p[5]), cvtpk(p[6], p[7])};
    u32x4 w1 = {cvtpk(p[8], p[9]), cvtpk(p[10], p[11]), cvtpk(p[12], p[13]), cvtpk(p[14], p[15])};
    const bf16x8 pb0 = __builtin_bit_cast(bf16x8, w0), pb1 = __builtin_bit_cast(bf16x8, w1);
    const unsigned short* vp = Vb + (size_t)(t * 32 + 4 * hi) * D + r32;
#pragma unroll
    for (int db = 0; db < 8; ++db) {
      bf16x8 v0, v1;
#pragma unroll
      for (int i = 0; i < 8; ++i) {
        const int kr = (i & 3) + 8 * (i >> 2);
        v0[i] = (short)vp[(size_t)kr * D + db * 32];
        v1[i] = (short)vp[(size_t)(16 + kr) * D + db * 32];
      }
      o[db] = MFMA(v0, pb0, o[db]); o[db] = MFMA(v1, pb1, o[db]);
    }
  }
  { auto rr = __builtin_amdgcn_permlane32_swap(__float_as_uint(lsum), __float_as_uint(lsum), false, false); lsum = __uint_as_float(rr[0]) + __uint_as_float(rr[1]); }
  const float il = 1.0f / lsum;
  unsigned* op = reinterpret_cast<unsigned*>(Ow + (size_t)r32 * D + 4 * hi);
#pragma unroll
  for (int db = 0; db < 8; ++db)
#pragma unroll
    for (int g = 0; g < 4; ++g) {
      op[db * 16 + g * 4 + 0] = cvtpk(o[db][4 * g] * il, o[db][4 * g + 1] * il);
      op[db * 16 + g * 4 + 1] = cvtpk(o[db][4 * g + 2] * il, o[db][4 * g + 3] * il);
    }
}

__global__ __launch_bounds__(256, 1) void attn_c64(const unsigned short* __restrict__ Q, const unsigned short* __restrict__ K, const unsigned short* __restrict__ V, unsigned short* __restrict__ O) {
  extern __shared__ __attribute__((aligned(1024))) char lds[];
  const int wid = __builtin_amdgcn_readfirstlane(threadIdx.x >> 6);
  const unsigned bx = blockIdx.x, x = bx & 7, y = bx >> 3;
  const unsigned hsel = y & 1, pair = (y >> 1) * 8 + x;
  const unsigned lds0 = (unsigned)(uintptr_t)lds;
  unsigned flags;
  asm volatile(
    "s_load_dwordx2 s[40:41], %1, 0x8\n\t"
    "s_load_dwordx2 s[52:53], %1, 0x0\n\t"
    "s_load_dwordx2 s[48:49], %1, 0x10\n\t"
    "s_load_dwordx2 s[56:57], %1, 0x18\n\t"
    "s_lshr_b32 s60, %6, 4\n\t"
    "s_lshl_b32 s60, s60, 3\n\t"
    "s_and_b32 s61, %6, 7\n\t"
    "s_or_b32 s60, s60, s61\n\t"
    "s_lshl_b32 s60, s60, 18\n\t"
    "s_lshl_b32 s61, %6, 14\n\t"
    "s_and_b32 s61, s61, 0x20000\n\t"
    "s_or_b32 s61, s61, s60\n\t"
    "s_lshl_b32 s62, %6, 3\n\t"
    "s_and_b32 s62, s62, 0x180\n\t"
    "s_mov_b32 s82, %5\n\t"
    "s_mov_b32 s54, 0x4000000\n\t"
    "s_mov_b32 s55, 0x20000\n\t"
    "s_mov_b32 s42, 0x4000000\n\t"
    "s_mov_b32 s43, 0x20000\n\t"
    "s_mov_b32 s50, 0x4000000\n\t"
    "s_mov_b32 s51, 0x20000\n\t"
    "s_mov_b32 s58, 0x4000000\n\t"
    "s_mov_b32 s59, 0x20000\n\t"
    "s_lshl_b32 s71, s82, 15\n\t"
    "s_add_u32 s67, s61, s71\n\t"
    "s_add_u32 s67, s67, s62\n\t"
    "s_lshl_b32 s71, s82, 7\n\t"
    "s_xor_b32 s71, s71, s62\n\t"
    "s_add_u32 s64, s60, s71\n\t"
    "s_lshl_b32 s75, s82, 12\n\t"
    "s_add_u32 s70, %8, s75\n\t"
    "v_mbcnt_lo_u32_b32 v0, -1, 0\n\t"
    "v_mbcnt_hi_u32_b32 v0, -1, v0\n\t"
    "v_lshrrev_b32 v1, 3, v0\n\t"
    "v_and_b32 v2, 7, v0\n\t"
    "v_lshrrev_b32 v4, 1, v1\n\t"
    "v_and_b32 v3, 1, v1\n\t"
    "v_lshl_or_b32 v3, v4, 3, v3\n\t"
    "v_lshlrev_b32 v3, 9, v3\n\t"
    "v_xor_b32 v5, v2, v4\n\t"
    "v_lshl_add_u32 v232, v5, 4, v3\n\t"
    "v_xor_b32 v233, 64, v232\n\t"
    "s_getpc_b64 s[80:81]\n\t"
    "s_lshr_b32 s71, %6, 1\n\t"
    "s_and_b32 s71, s71, 0xc\n\t"
    "s_or_b32 s71, s71, s82\n\t"
    "s_lshl_b32 s71, s71, 12\n\t"
    "v_lshlrev_b32 v6, 6, v0\n\t"
    "v_add_u32 v6, s71, v6\n\t"
    "v_min_u32 v6, 50212, v6\n\t"
    "global_load_dword v7, v6, s[80:81]\n\t"
    "s_waitcnt lgkmcnt(0)\n\t"
    "s_load_dword s63, s[40:41], s64\n\t"
    "s_load_dword s63, s[52:53], s67\n\t"
    "s_and_b32 s41, s41, 0xffff\n\t"
    "s_and_b32 s53, s53, 0xffff\n\t"
    "s_xor_b32 s71, s64, 0x0\n\t"
    "s_add_u32 m0, s70, 0x0\n\t"
    "s_nop 0\n\t"
    "buffer_load_dwordx4 v232, s[40:43], s71 offen offset:0 nt lds\n\t"
    "buffer_load_dwordx4 v233, s[40:43], s71 offen offset:1024 nt lds\n\t"
    "buffer_load_dwordx4 v232, s[40:43], s71 offen offset:2048 nt lds\n\t"
    "buffer_load_dwordx4 v233, s[40:43], s71 offen offset:3072 nt lds\n\t"
    "s_xor_b32 s71, s67, 0x0\n\t"
    "s_add_u32 m0, s70, 0x10000\n\t"
    "s_nop 0\n\t"
    "buffer_load_dwordx4 v232, s[52:55], s71 offen offset:0 nt lds\n\t"
    "buffer_load_dwordx4 v233, s[52:55], s71 offen offset:1024 nt lds\n\t"
    "s_xor_b32 s71, s67, 0x80\n\t"
    "s_add_u32 m0, s70, 0x10800\n\t"
    "s_nop 0\n\t"
    "buffer_load_dwordx4 v232, s[52:55], s71 offen offset:0 nt lds\n\t"
    "buffer_load_dwordx4 v233, s[52:55], s71 offen offset:1024 nt lds\n\t"
    "s_xor_b32 s71, s67, 0x100\n\t"
    "s_add_u32 m0, s70, 0x14000\n\t"
    "s_nop 0\n\t"
    "buffer_load_dwordx4 v232, s[52:55], s71 offen offset:0 nt lds\n\t"
    "buffer_load_dwordx4 v233, s[52:55], s71 offen offset:1024 nt lds\n\t"
    "s_xor_b32 s71, s67, 0x180\n\t"
    "s_add_u32 m0, s70, 0x14800\n\t"
    "s_nop 0\n\t"
    "buffer_load_dwordx4 v232, s[52:55], s71 offen offset:0 nt lds\n\t"
    "buffer_load_dwordx4 v233, s[52:55], s71 offen offset:1024 nt lds\n\t"
    "s_add_u32 s65, s64, 0x2000000\n\t"
    "s_lshl_b32 s71, s82, 14\n\t"
    "s_add_u32 s66, s60, s71\n\t"
    "s_add_u32 s66, s66, s62\n\t"
    "s_sub_u32 s68, s67, 0x2000000\n\t"
    "s_mov_b32 s83, 0\n\t"
    "s_mov_b32 s58, 0\n\t"
    "s_lshr_b32 s71, s61, 2\n\t"
    "s_and_b32 s71, s71, 0x8000\n\t"
    "s_add_u32 s81, s65, s71\n\t"
    "s_lshl_b32 s71, s71, 2\n\t"
    "s_lshl_b32 s34, s82, 15\n\t"
    "s_add_u32 s71, s71, s34\n\t"
    "s_add_u32 s35, s60, s71\n\t"
    "s_add_u32 s35, s35, s62\n\t"
    "s_add_u32 s35, s35, 0x2000000\n\t"
    "s_mov_b32 s80, 0x4000000\n\t"
    "s_mov_b32 s72, 0x3db8aa3b\n\t"
    "s_mov_b32 s79, 0xbdb8aa3b\n\t"
    "s_mov_b32 s73, 1\n\t"
    "s_mov_b32 s74, 0\n\t"
    "s_mov_b64 s[76:77], 0\n\t"
    "s_mov_b32 s78, 0x49800000\n\t"
    "v_lshlrev_b32 v238, 9, v0\n\t"
    "v_lshlrev_b32 v5, 1, v4\n\t"
    "v_xor_b32 v5, v2, v5\n\t"
    "v_lshl_add_u32 v234, v5, 4, v3\n\t"
    "v_lshl_add_u32 v237, v2, 4, v3\n\t"
    "v_and_b32 v8, 15, v0\n\t"
    "v_lshrrev_b32 v9, 4, v0\n\t"
    "v_lshrrev_b32 v10, 1, v8\n\t"
    "v_xor_b32 v11, v9, v10\n\t"
    "v_lshlrev_b32 v12, 7, v8\n\t"
    "v_add_u32 v12, %8, v12\n\t"
    "v_lshl_add_u32 v224, v11, 4, v12\n\t"
    "v_xor_b32 v11, 4, v11\n\t"
    "v_lshl_add_u32 v225, v11, 4, v12\n\t"
    "v_add_u32 v226, s75, v224\n\t"
    "v_add_u32 v227, s75, v225\n\t"
    "v_lshrrev_b32 v13, 2, v8\n\t"
    "v_and_b32 v14, 3, v0\n\t"
    "v_and_b32 v15, 1, v9\n\t"
    "v_lshrrev_b32 v16, 1, v13\n\t"
    "v_lshl_or_b32 v16, v15, 1, v16\n\t"
    "v_lshl_add_u32 v17, v9, 2, v13\n\t"
    "v_lshlrev_b32 v17, 7, v17\n\t"
    "v_lshl_add_u32 v17, v14, 3, v17\n\t"
    "v_add_u32 v17, %8, v17\n\t"
    "v_add_u32 v17, 0x10000, v17\n\t"
    "v_xor_b32 v18, 0, v16\n\t"
    "v_lshl_add_u32 v228, v18, 5, v17\n\t"
    "v_xor_b32 v18, 1, v16\n\t"
    "v_lshl_add_u32 v229, v18, 5, v17\n\t"
    "v_xor_b32 v18, 2, v16\n\t"
    "v_lshl_add_u32 v230, v18, 5, v17\n\t"
    "v_xor_b32 v18, 3, v16\n\t"
    "v_lshl_add_u32 v231, v18, 5, v17\n\t"
    "s_add_u32 s34, s70, 0x20000\n\t"
    "v_lshrrev_b32 v18, 1, v9\n\t"
    "v_and_b32 v20, 7, v8\n\t"
    "v_lshlrev_b32 v21, 2, v18\n\t"
    "v_xor_b32 v20, v20, v21\n\t"
    "v_lshlrev_b32 v18, 11, v18\n\t"
    "v_lshl_add_u32 v18, v20, 4, v18\n\t"
    "v_lshl_add_u32 v18, v15, 3, v18\n\t"
    "v_lshrrev_b32 v20, 3, v8\n\t"
    "v_lshl_add_u32 v18, v20, 8, v18\n\t"
    "v_add_u32 v235, s34, v18\n\t"
    "v_and_b32 v20, 1, v2\n\t"
    "v_lshlrev_b32 v21, 2, v20\n\t"
    "v_xor_b32 v21, v1, v21\n\t"
    "v_lshlrev_b32 v18, 11, v20\n\t"
    "v_lshl_add_u32 v18, v21, 4, v18\n\t"
    "v_and_b32 v20, 2, v2\n\t"
    "v_lshl_add_u32 v18, v20, 6, v18\n\t"
    "v_and_b32 v20, 4, v2\n\t"
    "v_lshl_add_u32 v18, v20, 12, v18\n\t"
    "v_add_u32 v236, s34, v18\n\t"
    "s_mov_b32 vcc_lo, 0x11111111\n\t"
    "s_nop 1\n\t"
    "s_mov_b32 vcc_hi, 0x11111111\n\t"
    "s_nop 1\n\t"
    "v_mov_b32 v63, 0x3f803f80\n\t"
    "v_cndmask_b32 v48, 0, v63, vcc\n\t"
    "s_lshl_b64 vcc, vcc, 1\n\t"
    "s_nop 1\n\t"
    "v_mov_b32 v49, v48\n\t"
    "v_mov_b32 v50, v48\n\t"
    "v_mov_b32 v51, v48\n\t"
    "v_cndmask_b32 v52, 0, v63, vcc\n\t"
    "s_lshl_b64 vcc, vcc, 1\n\t"
    "s_nop 1\n\t"
    "v_mov_b32 v53, v52\n\t"
    "v_mov_b32 v54, v52\n\t"
    "v_mov_b32 v55, v52\n\t"
    "v_cndmask_b32 v56, 0, v63, vcc\n\t"
    "s_lshl_b64 vcc, vcc, 1\n\t"
    "s_nop 1\n\t"
    "v_mov_b32 v57, v56\n\t"
    "v_mov_b32 v58, v56\n\t"
    "v_mov_b32 v59, v56\n\t"
    "v_cndmask_b32 v60, 0, v63, vcc\n\t"
    "v_mov_b32 v61, v60\n\t"
    "v_mov_b32 v62, v60\n\t"
    "v_mov_b32 v63, v60\n\t"
    "s_mov_b32 s80, 0\n\t"
    "s_and_b32 s49, s49, 0xffff\n\t"
    "s_and_b32 s57, s57, 0xffff\n\t"
    "s_mov_b32 s44, s40\n\t"
    "s_mov_b32 s45, s41\n\t"
    "s_mov_b32 s46, s42\n\t"
    "s_mov_b32 s47, s43\n\t"
    "s_xor_b32 s71, s67, 0x0\n\t"
    "s_add_u32 m0, s70, 0x17800\n\t"
    "s_nop 0\n\t"
    "buffer_load_dwordx4 v232, s[52:55], s71 offen offset:2048 nt lds\n\t"
    "buffer_load_dwordx4 v233, s[52:55], s71 offen offset:3072 nt lds\n\t"
    "s_xor_b32 s71, s67, 0x80\n\t"
    "s_add_u32 m0, s70, 0x18000\n\t"
    "s_nop 0\n\t"
    "buffer_load_dwordx4 v232, s[52:55], s71 offen offset:2048 nt lds\n\t"
    "buffer_load_dwordx4 v233, s[52:55], s71 offen offset:3072 nt lds\n\t"
    "s_xor_b32 s71, s67, 0x100\n\t"
    "s_add_u32 m0, s70, 0x1b800\n\t"
    "s_nop 0\n\t"
    "buffer_load_dwordx4 v232, s[52:55], s71 offen offset:2048 nt lds\n\t"
    "buffer_load_dwordx4 v233, s[52:55], s71 offen offset:3072 nt lds\n\t"
    "s_xor_b32 s71, s67, 0x180\n\t"
    "s_add_u32 m0, s70, 0x1c000\n\t"
    "s_nop 0\n\t"
    "buffer_load_dwordx4 v232, s[52:55], s71 offen offset:2048 nt lds\n\t"
    "buffer_load_dwordx4 v233, s[52:55], s71 offen offset:3072 nt lds\n\t"
    "s_xor_b32 s71, s64, 0x4000\n\t"
    "s_add_u32 m0, s70, 0x4000\n\t"
    "s_nop 0\n\t"
    "buffer_load_dwordx4 v232, s[40:43], s71 offen offset:0 nt lds\n\t"
    "buffer_load_dwordx4 v233, s[40:43], s71 offen offset:1024 nt lds\n\t"
    "buffer_load_dwordx4 v232, s[40:43], s71 offen offset:2048 nt lds\n\t"
    "buffer_load_dwordx4 v233, s[40:43], s71 offen offset:3072 nt lds\n\t"
    "s_waitcnt vmcnt(18)\n\t"
    "v_add_u32 v226, 0x10000, v226\n\t"
    "ds_read_b128 a[0:3], v226 offset:0\n\t"
    "v_add_u32 v227, 0x10000, v227\n\t"
    "ds_read_b128 a[4:7], v227 offset:0\n\t"
    "s_waitcnt vmcnt(16)\n\t"
    "ds_read_b128 a[32:35], v226 offset:2048\n\t"
    "ds_read_b128 a[36:39], v227 offset:2048\n\t"
    "s_waitcnt vmcnt(14)\n\t"
    "ds_read_b128 a[64:67], v226 offset:16384\n\t"
    "ds_read_b128 a[68:71], v227 offset:16384\n\t"
    "s_waitcnt vmcnt(12)\n\t"
    "ds_read_b128 a[96:99], v226 offset:18432\n\t"
    "ds_read_b128 a[100:103], v227 offset:18432\n\t"
    "s_xor_b32 s71, s64, 0x8000\n\t"
    "s_add_u32 m0, s70, 0x8000\n\t"
    "s_nop 0\n\t"
    "buffer_load_dwordx4 v232, s[40:43], s71 offen offset:0 nt lds\n\t"
    "buffer_load_dwordx4 v233, s[40:43], s71 offen offset:1024 nt lds\n\t"
    "buffer_load_dwordx4 v232, s[40:43], s71 offen offset:2048 nt lds\n\t"
    "buffer_load_dwordx4 v233, s[40:43], s71 offen offset:3072 nt lds\n\t"
    "s_waitcnt vmcnt(24)\n\t"
    "s_barrier\n\t"
    "ds_read_b128 v[96:99], v224 offset:0\n\t"
    "ds_read_b128 v[100:103], v225 offset:0\n\t"
    "ds_read_b128 v[104:107], v224 offset:4096\n\t"
    "ds_read_b128 v[108:111], v225 offset:4096\n\t"
    "ds_read_b128 v[112:115], v224 offset:8192\n\t"
    "ds_read_b128 v[116:119], v225 offset:8192\n\t"
    "ds_read_b128 v[120:123], v224 offset:12288\n\t"
    "ds_read_b128 v[124:127], v225 offset:12288\n\t"
    "ds_read_b128 v[128:131], v224 offset:2048\n\t"
    "ds_read_b128 v[132:135], v225 offset:2048\n\t"
    "ds_read_b128 v[136:139], v224 offset:6144\n\t"
    "ds_read_b128 v[140:143], v225 offset:6144\n\t"
    "s_xor_b32 s71, s64, 0xc000\n\t"
    "s_add_u32 m0, s70, 0xc000\n\t"
    "s_nop 0\n\t"
    "buffer_load_dwordx4 v232, s[40:43], s71 offen offset:0 nt lds\n\t"
    "buffer_load_dwordx4 v233, s[40:43], s71 offen offset:1024 nt lds\n\t"
    "buffer_load_dwordx4 v232, s[40:43], s71 offen offset:2048 nt lds\n\t"
    "buffer_load_dwordx4 v233, s[40:43], s71 offen offset:3072 nt lds\n\t"
    "s_waitcnt vmcnt(18)\n\t"
    "ds_read_b128 a[8:11], v226 offset:32768\n\t"
    "ds_read_b128 a[12:15], v227 offset:32768\n\t"
    "s_waitcnt vmcnt(16)\n\t"
    "ds_read_b128 a[40:43], v226 offset:34816\n\t"
    "ds_read_b128 a[44:47], v227 offset:34816\n\t"
    "s_waitcnt vmcnt(14)\n\t"
    "ds_read_b128 a[72:75], v226 offset:49152\n\t"
    "ds_read_b128 a[76:79], v227 offset:49152\n\t"
    "s_waitcnt vmcnt(12)\n\t"
    "ds_read_b128 a[104:107], v226 offset:51200\n\t"
    "ds_read_b128 a[108:111], v227 offset:51200\n\t"
    "s_waitcnt lgkmcnt(15)\n\t"
    "v_mfma_f32_16x16x32_bf16 v[16:19], v[96:99], a[0:3], 0\n\t"
    "s_xor_b32 s71, s64, 0x10000\n\t"
    "s_add_u32 m0, s70, 0x10000\n\t"
    "v_mfma_f32_16x16x32_bf16 v[16:19], v[100:103], a[4:7], v[16:19]\n\t"
    "s_waitcnt lgkmcnt(7)\n\t"
    "v_mfma_f32_16x16x32_bf16 v[20:23], v[96:99], a[8:11], 0\n\t"
    "ds_read_b128 v[144:147], v224 offset:10240\n\t"
    "s_waitcnt lgkmcnt(7)\n\t"
    "v_mfma_f32_16x16x32_bf16 v[20:23], v[100:103], a[12:15], v[20:23]\n\t"
    "v_mfma_f32_16x16x32_bf16 v[16:19], v[104:107], a[32:35], v[16:19]\n\t"
    "buffer_load_dwordx4 v232, s[40:43], s71 offen offset:0 nt lds\n\t"
    "v_mfma_f32_16x16x32_bf16 v[16:19], v[108:111], a[36:39], v[16:19]\n\t"
    "ds_read_b128 v[148:151], v225 offset:10240\n\t"
    "s_waitcnt lgkmcnt(7)\n\t"
    "v_mfma_f32_16x16x32_bf16 v[20:23], v[104:107], a[40:43], v[20:23]\n\t"
    "ds_read_b128 v[152:155], v224 offset:14336\n\t"
    "s_waitcnt lgkmcnt(7)\n\t"
    "v_mfma_f32_16x16x32_bf16 v[20:23], v[108:111], a[44:47], v[20:23]\n\t"
    "v_mfma_f32_16x16x32_bf16 v[16:19], v[112:115], a[64:67], v[16:19]\n\t"
    "s_waitcnt vmcnt(9)\n\t"
    "v_mfma_f32_16x16x32_bf16 v[16:19], v[116:119], a[68:71], v[16:19]\n\t"
    "s_barrier\n\t"
    "ds_read_b128 v[156:159], v225 offset:14336\n\t"
    "s_waitcnt lgkmcnt(7)\n\t"
    "v_mfma_f32_16x16x32_bf16 v[20:23], v[112:115], a[72:75], v[20:23]\n\t"
    "ds_read_b128 v[96:99], v224 offset:16384\n\t"
    "s_waitcnt lgkmcnt(7)\n\t"
    "v_mfma_f32_16x16x32_bf16 v[20:23], v[116:119], a[76:79], v[20:23]\n\t"
    "v_mfma_f32_16x16x32_bf16 v[16:19], v[120:123], a[96:99], v[16:19]\n\t"
    "buffer_load_dwordx4 v233, s[40:43], s71 offen offset:1024 nt lds\n\t"
    "v_mfma_f32_16x16x32_bf16 v[16:19], v[124:127], a[100:103], v[16:19]\n\t"
    "ds_read_b128 v[100:103], v225 offset:16384\n\t"
    "s_waitcnt lgkmcnt(7)\n\t"
    "v_mfma_f32_16x16x32_bf16 v[20:23], v[120:123], a[104:107], v[20:23]\n\t"
    "ds_read_b128 v[104:107], v224 offset:20480\n\t"
    "s_waitcnt lgkmcnt(7)\n\t"
    "v_mfma_f32_16x16x32_bf16 v[20:23], v[124:127], a[108:111], v[20:23]\n\t"
    "v_mfma_f32_16x16x32_bf16 v[32:35], v[128:131], a[0:3], 0\n\t"
    "buffer_load_dwordx4 v232, s[40:43], s71 offen offset:2048 nt lds\n\t"
    "v_mfma_f32_16x16x32_bf16 v[32:35], v[132:135], a[4:7], v[32:35]\n\t"
    "ds_read_b128 v[108:111], v225 offset:20480\n\t"
    "v_max3_f32 v240, v16, v17, v18\n\t"
    "v_mfma_f32_16x16x32_bf16 v[36:39], v[128:131], a[8:11], 0\n\t"
    "ds_read_b128 v[112:115], v224 offset:24576\n\t"
    "v_mfma_f32_16x16x32_bf16 v[36:39], v[132:135], a[12:15], v[36:39]\n\t"
    "v_max3_f32 v241, v20, v21, v22\n\t"
    "v_max_f32 v240, v240, v19\n\t"
    "v_max_f32 v241, v241, v23\n\t"
    "v_mov_b32 v208, v240\n\t"
    "v_mfma_f32_16x16x32_bf16 v[32:35], v[136:139], a[32:35], v[32:35]\n\t"
    "buffer_load_dwordx4 v233, s[40:43], s71 offen offset:3072 nt lds\n\t"
    "v_mov_b32 v209, v241\n\t"
    "v_permlane32_swap_b32 v208, v240\n\t"
    "s_nop 0\n\t"
    "v_permlane32_swap_b32 v209, v241\n\t"
    "v_max_f32 v240, v240, v208\n\t"
    "v_mfma_f32_16x16x32_bf16 v[32:35], v[140:143], a[36:39], v[32:35]\n\t"
    "s_xor_b32 s71, s64, 0x14000\n\t"
    "ds_read_b128 v[116:119], v225 offset:24576\n\t"
    "v_max_f32 v241, v241, v209\n\t"
    "v_mov_b32 v208, v240\n\t"
    "v_mov_b32 v209, v241\n\t"
    "s_nop 0\n\t"
    "v_permlane16_swap_b32 v208, v240\n\t"
    "v_mfma_f32_16x16x32_bf16 v[36:39], v[136:139], a[40:43], v[36:39]\n\t"
    "s_add_u32 m0, s70, 0x14000\n\t"
    "ds_read_b128 v[120:123], v224 offset:28672\n\t"
    "v_permlane16_swap_b32 v209, v241\n\t"
    "v_max_f32 v240, v240, v208\n\t"
    "v_max_f32 v241, v241, v209\n\t"
    "v_mul_f32 v240, s79, v240\n\t"
    "v_mfma_f32_16x16x32_bf16 v[36:39], v[140:143], a[44:47], v[36:39]\n\t"
    "s_waitcnt lgkmcnt(9)\n\t"
    "v_mul_f32 v241, s79, v241\n\t"
    "v_fma_f32 v16, v16, s72, v240\n\t"
    "v_fma_f32 v17, v17, s72, v240\n\t"
    "v_exp_f32 v16, v16\n\t"
    "v_mfma_f32_16x16x32_bf16 v[32:35], v[144:147], a[64:67], v[32:35]\n\t"
    "buffer_load_dwordx4 v232, s[40:43], s71 offen offset:0 nt lds\n\t"
    "v_exp_f32 v17, v17\n\t"
    "v_fma_f32 v18, v18, s72, v240\n\t"
    "v_fma_f32 v19, v19, s72, v240\n\t"
    "v_exp_f32 v18, v18\n\t"
    "v_mfma_f32_16x16x32_bf16 v[32:35], v[148:151], a[68:71], v[32:35]\n\t"
    "ds_read_b128 v[124:127], v225 offset:28672\n\t"
    "v_cvt_pk_bf16_f32 v64, v16, v17\n\t"
    "v_exp_f32 v19, v19\n\t"
    "v_fma_f32 v20, v20, s72, v241\n\t"
    "v_fma_f32 v21, v21, s72, v241\n\t"
    "v_mfma_f32_16x16x32_bf16 v[36:39], v[144:147], a[72:75], v[36:39]\n\t"
    "ds_read_b128 v[128:131], v224 offset:18432\n\t"
    "v_exp_f32 v20, v20\n\t"
    "v_cvt_pk_bf16_f32 v65, v18, v19\n\t"
    "v_mfma_f32_16x16x32_bf16 v[36:39], v[148:151], a[76:79], v[36:39]\n\t"
    "s_waitcnt lgkmcnt(9)\n\t"
    "v_exp_f32 v21, v21\n\t"
    "v_mfma_f32_16x16x32_bf16 v[32:35], v[152:155], a[96:99], v[32:35]\n\t"
    "buffer_load_dwordx4 v233, s[40:43], s71 offen offset:1024 nt lds\n\t"
    "v_mfma_f32_16x16x32_bf16 v[32:35], v[156:159], a[100:103], v[32:35]\n\t"
    "ds_read_b128 v[132:135], v225 offset:18432\n\t"
    "v_fma_f32 v22, v22, s72, v241\n\t"
    "v_mfma_f32_16x16x32_bf16 v[36:39], v[152:155], a[104:107], v[36:39]\n\t"
    "ds_read_b128 v[136:139], v224 offset:22528\n\t"
    "v_fma_f32 v23, v23, s72, v241\n\t"
    "v_mfma_f32_16x16x32_bf16 v[36:39], v[156:159], a[108:111], v[36:39]\n\t"
    "s_waitcnt lgkmcnt(9)\n\t"
    "v_exp_f32 v22, v22\n\t"
    "v_cvt_pk_bf16_f32 v68, v20, v21\n\t"
    "v_mfma_f32_16x16x32_bf16 v[0:3], v[96:99], a[0:3], 0\n\t"
    "buffer_load_dwordx4 v232, s[40:43], s71 offen offset:2048 nt lds\n\t"
    "v_mfma_f32_16x16x32_bf16 v[0:3], v[100:103], a[4:7], v[0:3]\n\t"
    "ds_read_b128 v[140:143], v225 offset:22528\n\t"
    "v_exp_f32 v23, v23\n\t"
    "v_mfma_f32_16x16x32_bf16 v[4:7], v[96:99], a[8:11], 0\n\t"
    "ds_read_b128 v[144:147], v224 offset:26624\n\t"
    "v_cvt_pk_bf16_f32 v69, v22, v23\n\t"
    "v_mfma_f32_16x16x32_bf16 v[4:7], v[100:103], a[12:15], v[4:7]\n\t"
    "s_waitcnt lgkmcnt(9)\n\t"
    "v_fma_f32 v32, v32, s72, v240\n\t"
    "v_fma_f32 v33, v33, s72, v240\n\t"
    "v_exp_f32 v32, v32\n\t"
    "v_exp_f32 v33, v33\n\t"
    "v_mfma_f32_16x16x32_bf16 v[0:3], v[104:107], a[32:35], v[0:3]\n\t"
    "buffer_load_dwordx4 v233, s[40:43], s71 offen offset:3072 nt lds\n\t"
    "v_fma_f32 v34, v34, s72, v240\n\t"
    "v_fma_f32 v35, v35, s72, v240\n\t"
    "v_exp_f32 v34, v34\n\t"
    "v_cvt_pk_bf16_f32 v66, v32, v33\n\t"
    "v_mfma_f32_16x16x32_bf16 v[0:3], v[108:111], a[36:39], v[0:3]\n\t"
    "s_xor_b32 s71, s67, 0x4000\n\t"
    "ds_read_b128 v[148:151], v225 offset:26624\n\t"
    "v_exp_f32 v35, v35\n\t"
    "v_fma_f32 v36, v36, s72, v241\n\t"
    "v_fma_f32 v37, v37, s72, v241\n\t"
    "v_exp_f32 v36, v36\n\t"
    "v_mfma_f32_16x16x32_bf16 v[4:7], v[104:107], a[40:43], v[4:7]\n\t"
    "s_add_u32 m0, s70, 0x18000\n\t"
    "ds_read_b128 v[152:155], v224 offset:30720\n\t"
    "v_cvt_pk_bf16_f32 v67, v34, v35\n\t"
    "v_mfma_f32_16x16x32_bf16 v[4:7], v[108:111], a[44:47], v[4:7]\n\t"
    "s_waitcnt lgkmcnt(9)\n\t"
    "v_exp_f32 v37, v37\n\t"
    "v_fma_f32 v38, v38, s72, v241\n\t"
    "v_mfma_f32_16x16x32_bf16 v[0:3], v[112:115], a[64:67], v[0:3]\n\t"
    "s_waitcnt vmcnt(12)\n\t"
    "buffer_load_dwordx4 v232, s[52:55], s71 offen offset:0 nt lds\n\t"
    "v_mfma_f32_16x16x32_bf16 v[0:3], v[116:119], a[68:71], v[0:3]\n\t"
    "s_barrier\n\t"
    "ds_read_b128 v[156:159], v225 offset:30720\n\t"
    "v_mfma_f32_16x16x32_bf16 v[4:7], v[112:115], a[72:75], v[4:7]\n\t"
    "ds_read_b128 v[96:99], v224 offset:32768\n\t"
    "v_fma_f32 v39, v39, s72, v241\n\t"
    "v_mfma_f32_16x16x32_bf16 v[4:7], v[116:119], a[76:79], v[4:7]\n\t"
    "s_waitcnt lgkmcnt(9)\n\t"
    "v_exp_f32 v38, v38\n\t"
    "v_cvt_pk_bf16_f32 v70, v36, v37\n\t"
    "v_mfma_f32_16x16x32_bf16 v[0:3], v[120:123], a[96:99], v[0:3]\n\t"
    "buffer_load_dwordx4 v233, s[52:55], s71 offen offset:1024 nt lds\n\t"
    "v_mfma_f32_16x16x32_bf16 v[0:3], v[124:127], a[100:103], v[0:3]\n\t"
    "s_xor_b32 s71, s67, 0x4080\n\t"
    "ds_read_b128 v[100:103], v225 offset:32768\n\t"
    "v_mfma_f32_16x16x32_bf16 v[4:7], v[120:123], a[104:107], v[4:7]\n\t"
    "s_add_u32 m0, s70, 0x18800\n\t"
    "ds_read_b128 v[104:107], v224 offset:36864\n\t"
    "v_mfma_f32_16x16x32_bf16 v[4:7], v[124:127], a[108:111], v[4:7]\n\t"
    "s_waitcnt lgkmcnt(9)\n\t"
    "v_exp_f32 v39, v39\n\t"
    "v_mfma_f32_16x16x32_bf16 v[16:19], v[128:131], a[0:3], 0\n\t"
    "buffer_load_dwordx4 v232, s[52:55], s71 offen offset:0 nt lds\n\t"
    "v_mfma_f32_16x16x32_bf16 v[16:19], v[132:135], a[4:7], v[16:19]\n\t"
    "ds_read_b128 v[108:111], v225 offset:36864\n\t"
    "v_cvt_pk_bf16_f32 v71, v38, v39\n\t"
    "v_mfma_f32_16x16x32_bf16 v[20:23], v[128:131], a[8:11], 0\n\t"
    "ds_read_b128 v[112:115], v224 offset:40960\n\t"
    "buffer_load_dwordx4 v233, s[52:55], s71 offen offset:1024 nt lds\n\t"
    "v_mfma_f32_16x16x32_bf16 v[20:23], v[132:135], a[12:15], v[20:23]\n\t"
    "s_waitcnt lgkmcnt(9)\n\t"
    "s_xor_b32 s71, s67, 0x4000\n\t"
    "s_add_u32 m0, s70, 0x1b800\n\t"
    "v_mfma_f32_16x16x32_bf16 v[16:19], v[136:139], a[32:35], v[16:19]\n\t"
    "v_fma_f32 v0, v0, s72, v240\n\t"
    "v_fma_f32 v1, v1, s72, v240\n\t"
    "v_mfma_f32_16x16x32_bf16 v[16:19], v[140:143], a[36:39], v[16:19]\n\t"
    "ds_read_b128 v[116:119], v225 offset:40960\n\t"
    "v_exp_f32 v0, v0\n\t"
    "v_mfma_f32_16x16x32_bf16 v[20:23], v[136:139], a[40:43], v[20:23]\n\t"
    "ds_read_b128 v[120:123], v224 offset:45056\n\t"
    "buffer_load_dwordx4 v232, s[52:55], s71 offen offset:2048 nt lds\n\t"
    "v_mfma_f32_16x16x32_bf16 v[20:23], v[140:143], a[44:47], v[20:23]\n\t"
    "s_waitcnt lgkmcnt(9)\n\t"
    "v_exp_f32 v1, v1\n\t"
    "v_mfma_f32_16x16x32_bf16 v[16:19], v[144:147], a[64:67], v[16:19]\n\t"
    "v_fma_f32 v2, v2, s72, v240\n\t"
    "v_fma_f32 v3, v3, s72, v240\n\t"
    "v_mfma_f32_16x16x32_bf16 v[16:19], v[148:151], a[68:71], v[16:19]\n\t"
    "ds_read_b128 v[124:127], v225 offset:45056\n\t"
    "v_exp_f32 v2, v2\n\t"
    "v_mfma_f32_16x16x32_bf16 v[20:23], v[144:147], a[72:75], v[20:23]\n\t"
    "ds_read_b128 v[128:131], v224 offset:34816\n\t"
    "buffer_load_dwordx4 v233, s[52:55], s71 offen offset:3072 nt lds\n\t"
    "v_cvt_pk_bf16_f32 v80, v0, v1\n\t"
    "v_mfma_f32_16x16x32_bf16 v[20:23], v[148:151], a[76:79], v[20:23]\n\t"
    "s_waitcnt lgkmcnt(9)\n\t"
    "v_exp_f32 v3, v3\n\t"
    "v_mfma_f32_16x16x32_bf16 v[16:19], v[152:155], a[96:99], v[16:19]\n\t"
    "s_xor_b32 s71, s67, 0x4080\n\t"
    "v_fma_f32 v4, v4, s72, v241\n\t"
    "v_fma_f32 v5, v5, s72, v241\n\t"
    "v_mfma_f32_16x16x32_bf16 v[16:19], v[156:159], a[100:103], v[16:19]\n\t"
    "ds_read_b128 v[132:135], v225 offset:34816\n\t"
    "v_exp_f32 v4, v4\n\t"
    "v_mfma_f32_16x16x32_bf16 v[20:23], v[152:155], a[104:107], v[20:23]\n\t"
    "ds_read_b128 v[136:139], v224 offset:38912\n\t"
    "v_cvt_pk_bf16_f32 v81, v2, v3\n\t"
    "v_mfma_f32_16x16x32_bf16 v[20:23], v[156:159], a[108:111], v[20:23]\n\t"
    "s_waitcnt lgkmcnt(9)\n\t"
    "v_exp_f32 v5, v5\n\t"
    "v_mfma_f32_16x16x32_bf16 v[32:35], v[96:99], a[0:3], 0\n\t"
    "s_add_u32 m0, s70, 0x1c000\n\t"
    "v_fma_f32 v6, v6, s72, v241\n\t"
    "v_fma_f32 v7, v7, s72, v241\n\t"
    "v_mfma_f32_16x16x32_bf16 v[32:35], v[100:103], a[4:7], v[32:35]\n\t"
    "ds_read_b128 v[140:143], v225 offset:38912\n\t"
    "buffer_load_dwordx4 v232, s[52:55], s71 offen offset:2048 nt lds\n\t"
    "v_exp_f32 v6, v6\n\t"
    "v_mfma_f32_16x16x32_bf16 v[36:39], v[96:99], a[8:11], 0\n\t"
    "ds_read_b128 v[144:147], v224 offset:43008\n\t"
    "v_cvt_pk_bf16_f32 v84, v4, v5\n\t"
    "v_mfma_f32_16x16x32_bf16 v[36:39], v[100:103], a[12:15], v[36:39]\n\t"
    "s_waitcnt lgkmcnt(9)\n\t"
    "v_exp_f32 v7, v7\n\t"
    "v_mfma_f32_16x16x32_bf16 v[32:35], v[104:107], a[32:35], v[32:35]\n\t"
    "buffer_load_dwordx4 v233, s[52:55], s71 offen offset:3072 nt lds\n\t"
    "v_fma_f32 v16, v16, s72, v240\n\t"
    "v_fma_f32 v17, v17, s72, v240\n\t"
    "v_mfma_f32_16x16x32_bf16 v[32:35], v[108:111], a[36:39], v[32:35]\n\t"
    "s_xor_b32 s71, s67, 0x4100\n\t"
    "s_add_u32 m0, s70, 0x20000\n\t"
    "ds_read_b128 v[148:151], v225 offset:43008\n\t"
    "v_exp_f32 v16, v16\n\t"
    "v_mfma_f32_16x16x32_bf16 v[36:39], v[104:107], a[40:43], v[36:39]\n\t"
    "ds_read_b128 v[152:155], v224 offset:47104\n\t"
    "buffer_load_dwordx4 v232, s[52:55], s71 offen offset:0 nt lds\n\t"
    "v_cvt_pk_bf16_f32 v85, v6, v7\n\t"
    "v_mfma_f32_16x16x32_bf16 v[36:39], v[108:111], a[44:47], v[36:39]\n\t"
    "s_waitcnt lgkmcnt(9)\n\t"
    "v_exp_f32 v17, v17\n\t"
    "v_mfma_f32_16x16x32_bf16 v[32:35], v[112:115], a[64:67], v[32:35]\n\t"
    "s_waitcnt vmcnt(17)\n\t"
    "v_fma_f32 v18, v18, s72, v240\n\t"
    "v_fma_f32 v19, v19, s72, v240\n\t"
    "v_mfma_f32_16x16x32_bf16 v[32:35], v[116:119], a[68:71], v[32:35]\n\t"
    "s_barrier\n\t"
    "ds_read_b128 v[156:159], v225 offset:47104\n\t"
    "v_exp_f32 v18, v18\n\t"
    "v_mfma_f32_16x16x32_bf16 v[36:39], v[112:115], a[72:75], v[36:39]\n\t"
    "ds_read_b128 v[96:99], v224 offset:49152\n\t"
    "buffer_load_dwordx4 v233, s[52:55], s71 offen offset:1024 nt lds\n\t"
    "v_cvt_pk_bf16_f32 v82, v16, v17\n\t"
    "v_mfma_f32_16x16x32_bf16 v[36:39], v[116:119], a[76:79], v[36:39]\n\t"
    "s_waitcnt lgkmcnt(9)\n\t"
    "v_exp_f32 v19, v19\n\t"
    "v_mfma_f32_16x16x32_bf16 v[32:35], v[120:123], a[96:99], v[32:35]\n\t"
    "s_xor_b32 s71, s67, 0x4180\n\t"
    "v_fma_f32 v20, v20, s72, v241\n\t"
    "v_fma_f32 v21, v21, s72, v241\n\t"
    "v_mfma_f32_16x16x32_bf16 v[32:35], v[124:127], a[100:103], v[32:35]\n\t"
    "ds_read_b128 v[100:103], v225 offset:49152\n\t"
    "v_exp_f32 v20, v20\n\t"
    "v_mfma_f32_16x16x32_bf16 v[36:39], v[120:123], a[104:107], v[36:39]\n\t"
    "ds_read_b128 v[104:107], v224 offset:53248\n\t"
    "v_cvt_pk_bf16_f32 v83, v18, v19\n\t"
    "v_mfma_f32_16x16x32_bf16 v[36:39], v[124:127], a[108:111], v[36:39]\n\t"
    "s_waitcnt lgkmcnt(9)\n\t"
    "v_exp_f32 v21, v21\n\t"
    "v_mfma_f32_16x16x32_bf16 v[0:3], v[128:131], a[0:3], 0\n\t"
    "s_add_u32 m0, s70, 0x20800\n\t"
    "v_fma_f32 v22, v22, s72, v241\n\t"
    "v_fma_f32 v23, v23, s72, v241\n\t"
    "v_mfma_f32_16x16x32_bf16 v[0:3], v[132:135], a[4:7], v[0:3]\n\t"
    "ds_read_b128 v[108:111], v225 offset:53248\n\t"
    "v_exp_f32 v22, v22\n\t"
    "v_mfma_f32_16x16x32_bf16 v[4:7], v[128:131], a[8:11], 0\n\t"
    "ds_read_b128 v[112:115], v224 offset:57344\n\t"
    "buffer_load_dwordx4 v232, s[52:55], s71 offen offset:0 nt lds\n\t"
    "v_cvt_pk_bf16_f32 v86, v20, v21\n\t"
    "v_mfma_f32_16x16x32_bf16 v[4:7], v[132:135], a[12:15], v[4:7]\n\t"
    "s_waitcnt lgkmcnt(9)\n\t"
    "v_exp_f32 v23, v23\n\t"
    "v_mfma_f32_16x16x32_bf16 v[252:255], v[48:51], v[80:83], 0\n\t"
    "buffer_load_dwordx4 v233, s[52:55], s71 offen offset:1024 nt lds\n\t"
    "v_mfma_f32_16x16x32_bf16 v[0:3], v[136:139], a[32:35], v[0:3]\n\t"
    "s_xor_b32 s71, s67, 0x4100\n\t"
    "v_fma_f32 v32, v32, s72, v240\n\t"
    "v_fma_f32 v33, v33, s72, v240\n\t"
    "v_mfma_f32_16x16x32_bf16 v[0:3], v[140:143], a[36:39], v[0:3]\n\t"
    "ds_read_b128 v[116:119], v225 offset:57344\n\t"
    "v_exp_f32 v32, v32\n\t"
    "v_mfma_f32_16x16x32_bf16 v[4:7], v[136:139], a[40:43], v[4:7]\n\t"
    "ds_read_b128 v[120:123], v224 offset:61440\n\t"
    "v_cvt_pk_bf16_f32 v87, v22, v23\n\t"
    "v_mfma_f32_16x16x32_bf16 v[4:7], v[140:143], a[44:47], v[4:7]\n\t"
    "s_waitcnt lgkmcnt(9)\n\t"
    "v_exp_f32 v33, v33\n\t"
    "v_mfma_f32_16x16x32_bf16 v[0:3], v[144:147], a[64:67], v[0:3]\n\t"
    "s_add_u32 m0, s70, 0x23800\n\t"
    "v_fma_f32 v34, v34, s72, v240\n\t"
    "v_fma_f32 v35, v35, s72, v240\n\t"
    "v_mfma_f32_16x16x32_bf16 v[0:3], v[148:151], a[68:71], v[0:3]\n\t"
    "ds_read_b128 v[124:127], v225 offset:61440\n\t"
    "buffer_load_dwordx4 v232, s[52:55], s71 offen offset:2048 nt lds\n\t"
    "v_exp_f32 v34, v34\n\t"
    "v_mfma_f32_16x16x32_bf16 v[4:7], v[144:147], a[72:75], v[4:7]\n\t"
    "ds_read_b128 v[128:131], v224 offset:51200\n\t"
    "v_cvt_pk_bf16_f32 v32, v32, v33\n\t"
    "v_accvgpr_write_b32 a128, v32\n\t"
    "v_mfma_f32_16x16x32_bf16 v[4:7], v[148:151], a[76:79], v[4:7]\n\t"
    "s_waitcnt lgkmcnt(9)\n\t"
    "v_exp_f32 v35, v35\n\t"
    "v_mfma_f32_16x16x32_bf16 v[252:255], v[52:55], v[84:87], v[252:255]\n\t"
    "buffer_load_dwordx4 v233, s[52:55], s71 offen offset:3072 nt lds\n\t"
    "v_mfma_f32_16x16x32_bf16 v[0:3], v[152:155], a[96:99], v[0:3]\n\t"
    "s_xor_b32 s71, s67, 0x4180\n\t"
    "v_fma_f32 v36, v36, s72, v241\n\t"
    "v_fma_f32 v37, v37, s72, v241\n\t"
    "v_mfma_f32_16x16x32_bf16 v[0:3], v[156:159], a[100:103], v[0:3]\n\t"
    "ds_read_b128 v[132:135], v225 offset:51200\n\t"
    "v_exp_f32 v36, v36\n\t"
    "v_mfma_f32_16x16x32_bf16 v[4:7], v[152:155], a[104:107], v[4:7]\n\t"
    "ds_read_b128 v[136:139], v224 offset:55296\n\t"
    "v_cvt_pk_bf16_f32 v34, v34, v35\n\t"
    "v_accvgpr_write_b32 a129, v34\n\t"
    "v_mfma_f32_16x16x32_bf16 v[4:7], v[156:159], a[108:111], v[4:7]\n\t"
    "s_waitcnt lgkmcnt(9)\n\t"
    "v_exp_f32 v37, v37\n\t"
    "v_mfma_f32_16x16x32_bf16 v[16:19], v[96:99], a[0:3], 0\n\t"
    "s_add_u32 m0, s70, 0x24000\n\t"
    "v_fma_f32 v38, v38, s72, v241\n\t"
    "v_fma_f32 v39, v39, s72, v241\n\t"
    "v_mfma_f32_16x16x32_bf16 v[16:19], v[100:103], a[4:7], v[16:19]\n\t"
    "ds_read_b128 v[140:143], v225 offset:55296\n\t"
    "buffer_load_dwordx4 v232, s[52:55], s71 offen offset:2048 nt lds\n\t"
    "v_exp_f32 v38, v38\n\t"
    "v_mfma_f32_16x16x32_bf16 v[20:23], v[96:99], a[8:11], 0\n\t"
    "ds_read_b128 v[144:147], v224 offset:59392\n\t"
    "v_cvt_pk_bf16_f32 v36, v36, v37\n\t"
    "v_accvgpr_write_b32 a132, v36\n\t"
    "v_mfma_f32_16x16x32_bf16 v[20:23], v[100:103], a[12:15], v[20:23]\n\t"
    "s_waitcnt lgkmcnt(9)\n\t"
    "v_exp_f32 v39, v39\n\t"
    "v_mfma_f32_16x16x32_bf16 v[16:19], v[104:107], a[32:35], v[16:19]\n\t"
    "v_fma_f32 v0, v0, s72, v240\n\t"
    "v_fma_f32 v1, v1, s72, v240\n\t"
    "v_mfma_f32_16x16x32_bf16 v[16:19], v[108:111], a[36:39], v[16:19]\n\t"
    "ds_read_b128 v[148:151], v225 offset:59392\n\t"
    "buffer_load_dwordx4 v233, s[52:55], s71 offen offset:3072 nt lds\n\t"
    "v_exp_f32 v0, v0\n\t"
    "v_mfma_f32_16x16x32_bf16 v[20:23], v[104:107], a[40:43], v[20:23]\n\t"
    "ds_read_b128 v[152:155], v224 offset:63488\n\t"
    "v_cvt_pk_bf16_f32 v38, v38, v39\n\t"
    "v_accvgpr_write_b32 a133, v38\n\t"
    "v_mfma_f32_16x16x32_bf16 v[20:23], v[108:111], a[44:47], v[20:23]\n\t"
    "s_waitcnt lgkmcnt(9)\n\t"
    "v_exp_f32 v1, v1\n\t"
    "v_mfma_f32_16x16x32_bf16 v[252:255], v[48:51], v[64:67], v[252:255]\n\t"
    "v_mfma_f32_16x16x32_bf16 v[16:19], v[112:115], a[64:67], v[16:19]\n\t"
    "s_waitcnt vmcnt(20)\n\t"
    "v_fma_f32 v2, v2, s72, v240\n\t"
    "v_fma_f32 v3, v3, s72, v240\n\t"
    "v_mfma_f32_16x16x32_bf16 v[16:19], v[116:119], a[68:71], v[16:19]\n\t"
    "s_barrier\n\t"
    "ds_read_b128 v[156:159], v225 offset:63488\n\t"
    "v_add_u32 v224, 0x10000, v224\n\t"
    "v_exp_f32 v2, v2\n\t"
    "v_mfma_f32_16x16x32_bf16 v[20:23], v[112:115], a[72:75], v[20:23]\n\t"
    "ds_read_b128 v[96:99], v224 offset:0\n\t"
    "v_cvt_pk_bf16_f32 v0, v0, v1\n\t"
    "v_accvgpr_write_b32 a130, v0\n\t"
    "v_mfma_f32_16x16x32_bf16 v[20:23], v[116:119], a[76:79], v[20:23]\n\t"
    "s_waitcnt lgkmcnt(9)\n\t"
    "v_exp_f32 v3, v3\n\t"
    "v_mfma_f32_16x16x32_bf16 v[16:19], v[120:123], a[96:99], v[16:19]\n\t"
    "v_add_u32 v225, 0x10000, v225\n\t"
    "v_fma_f32 v4, v4, s72, v241\n\t"
    "v_fma_f32 v5, v5, s72, v241\n\t"
    "v_mfma_f32_16x16x32_bf16 v[16:19], v[124:127], a[100:103], v[16:19]\n\t"
    "ds_read_b128 v[100:103], v225 offset:0\n\t"
    "v_exp_f32 v4, v4\n\t"
    "v_mfma_f32_16x16x32_bf16 v[20:23], v[120:123], a[104:107], v[20:23]\n\t"
    "ds_read_b128 v[104:107], v224 offset:4096\n\t"
    "v_cvt_pk_bf16_f32 v2, v2, v3\n\t"
    "v_accvgpr_write_b32 a131, v2\n\t"
    "v_mfma_f32_16x16x32_bf16 v[20:23], v[124:127], a[108:111], v[20:23]\n\t"
    "s_waitcnt lgkmcnt(9)\n\t"
    "v_exp_f32 v5, v5\n\t"
    "v_mfma_f32_16x16x32_bf16 v[252:255], v[52:55], v[68:71], v[252:255]\n\t"
    "v_mfma_f32_16x16x32_bf16 v[32:35], v[128:131], a[0:3], 0\n\t"
    "v_fma_f32 v6, v6, s72, v241\n\t"
    "v_fma_f32 v7, v7, s72, v241\n\t"
    "v_mfma_f32_16x16x32_bf16 v[32:35], v[132:135], a[4:7], v[32:35]\n\t"
    "ds_read_b128 v[108:111], v225 offset:4096\n\t"
    "v_exp_f32 v6, v6\n\t"
    "v_mfma_f32_16x16x32_bf16 v[36:39], v[128:131], a[8:11], 0\n\t"
    "ds_read_b128 v[112:115], v224 offset:8192\n\t"
    "v_cvt_pk_bf16_f32 v4, v4, v5\n\t"
    "v_accvgpr_write_b32 a134, v4\n\t"
    "v_mfma_f32_16x16x32_bf16 v[36:39], v[132:135], a[12:15], v[36:39]\n\t"
    "s_waitcnt lgkmcnt(9)\n\t"
    "v_exp_f32 v7, v7\n\t"
    "v_mfma_f32_16x16x32_bf16 v[252:255], v[48:51], a[128:131], v[252:255]\n\t"
    "v_mfma_f32_16x16x32_bf16 v[32:35], v[136:139], a[32:35], v[32:35]\n\t"
    "v_fma_f32 v16, v16, s72, v240\n\t"
    "v_fma_f32 v17, v17, s72, v240\n\t"
    "v_mfma_f32_16x16x32_bf16 v[32:35], v[140:143], a[36:39], v[32:35]\n\t"
    "ds_read_b128 v[116:119], v225 offset:8192\n\t"
    "v_exp_f32 v16, v16\n\t"
    "v_mfma_f32_16x16x32_bf16 v[36:39], v[136:139], a[40:43], v[36:39]\n\t"
    "ds_read_b128 v[120:123], v224 offset:12288\n\t"
    "v_cvt_pk_bf16_f32 v6, v6, v7\n\t"
    "v_accvgpr_write_b32 a135, v6\n\t"
    "v_mfma_f32_16x16x32_bf16 v[36:39], v[140:143], a[44:47], v[36:39]\n\t"
    "s_waitcnt lgkmcnt(9)\n\t"
    "v_exp_f32 v17, v17\n\t"
    "v_mfma_f32_16x16x32_bf16 v[32:35], v[144:147], a[64:67], v[32:35]\n\t"
    "v_fma_f32 v18, v18, s72, v240\n\t"
    "v_fma_f32 v19, v19, s72, v240\n\t"
    "v_mfma_f32_16x16x32_bf16 v[32:35], v[148:151], a[68:71], v[32:35]\n\t"
    "ds_read_b128 v[124:127], v225 offset:12288\n\t"
    "v_exp_f32 v18, v18\n\t"
    "v_mfma_f32_16x16x32_bf16 v[36:39], v[144:147], a[72:75], v[36:39]\n\t"
    "ds_read_b128 v[128:131], v224 offset:2048\n\t"
    "v_cvt_pk_bf16_f32 v16, v16, v17\n\t"
    "v_accvgpr_write_b32 a144, v16\n\t"
    "v_mfma_f32_16x16x32_bf16 v[36:39], v[148:151], a[76:79], v[36:39]\n\t"
    "s_waitcnt lgkmcnt(9)\n\t"
    "v_exp_f32 v19, v19\n\t"
    "v_mfma_f32_16x16x32_bf16 v[252:255], v[52:55], a[132:135], v[252:255]\n\t"
    "v_mfma_f32_16x16x32_bf16 v[32:35], v[152:155], a[96:99], v[32:35]\n\t"
    "v_fma_f32 v20, v20, s72, v241\n\t"
    "v_fma_f32 v21, v21, s72, v241\n\t"
    "v_mfma_f32_16x16x32_bf16 v[32:35], v[156:159], a[100:103], v[32:35]\n\t"
    "ds_read_b128 v[132:135], v225 offset:2048\n\t"
    "v_exp_f32 v20, v20\n\t"
    "v_mfma_f32_16x16x32_bf16 v[36:39], v[152:155], a[104:107], v[36:39]\n\t"
    "ds_read_b128 v[136:139], v224 offset:6144\n\t"
    "v_cvt_pk_bf16_f32 v18, v18, v19\n\t"
    "v_accvgpr_write_b32 a145, v18\n\t"
    "v_mfma_f32_16x16x32_bf16 v[36:39], v[156:159], a[108:111], v[36:39]\n\t"
    "s_waitcnt lgkmcnt(9)\n\t"
    "v_exp_f32 v21, v21\n\t"
    "v_mfma_f32_16x16x32_bf16 v[0:3], v[96:99], a[0:3], 0\n\t"
    "v_fma_f32 v22, v22, s72, v241\n\t"
    "v_fma_f32 v23, v23, s72, v241\n\t"
    "v_mfma_f32_16x16x32_bf16 v[0:3], v[100:103], a[4:7], v[0:3]\n\t"
    "ds_read_b128 v[140:143], v225 offset:6144\n\t"
    "v_exp_f32 v22, v22\n\t"
    "v_mfma_f32_16x16x32_bf16 v[4:7], v[96:99], a[8:11], 0\n\t"
    "ds_read_b128 v[144:147], v224 offset:10240\n\t"
    "v_cvt_pk_bf16_f32 v20, v20, v21\n\t"
    "v_accvgpr_write_b32 a148, v20\n\t"
    "v_mfma_f32_16x16x32_bf16 v[4:7], v[100:103], a[12:15], v[4:7]\n\t"
    "s_waitcnt lgkmcnt(9)\n\t"
    "v_exp_f32 v23, v23\n\t"
    "v_mfma_f32_16x16x32_bf16 v[0:3], v[104:107], a[32:35], v[0:3]\n\t"
    "v_fma_f32 v32, v32, s72, v240\n\t"
    "v_fma_f32 v33, v33, s72, v240\n\t"
    "v_mfma_f32_16x16x32_bf16 v[0:3], v[108:111], a[36:39], v[0:3]\n\t"
    "ds_read_b128 v[148:151], v225 offset:10240\n\t"
    "v_exp_f32 v32, v32\n\t"
    "v_mfma_f32_16x16x32_bf16 v[4:7], v[104:107], a[40:43], v[4:7]\n\t"
    "ds_read_b128 v[152:155], v224 offset:14336\n\t"
    "v_cvt_pk_bf16_f32 v22, v22, v23\n\t"
    "v_accvgpr_write_b32 a149, v22\n\t"
    "v_mfma_f32_16x16x32_bf16 v[4:7], v[108:111], a[44:47], v[4:7]\n\t"
    "s_waitcnt lgkmcnt(9)\n\t"
    "v_exp_f32 v33, v33\n\t"
    "v_mfma_f32_16x16x32_bf16 v[0:3], v[112:115], a[64:67], v[0:3]\n\t"
    "s_waitcnt vmcnt(16)\n\t"
    "v_fma_f32 v34, v34, s72, v240\n\t"
    "v_fma_f32 v35, v35, s72, v240\n\t"
    "v_mfma_f32_16x16x32_bf16 v[0:3], v[116:119], a[68:71], v[0:3]\n\t"
    "s_barrier\n\t"
    "ds_read_b128 v[156:159], v225 offset:14336\n\t"
    "v_exp_f32 v34, v34\n\t"
    "v_mfma_f32_16x16x32_bf16 v[4:7], v[112:115], a[72:75], v[4:7]\n\t"
    "ds_read_b128 v[96:99], v224 offset:16384\n\t"
    "v_cvt_pk_bf16_f32 v32, v32, v33\n\t"
    "v_accvgpr_write_b32 a146, v32\n\t"
    "v_mfma_f32_16x16x32_bf16 v[4:7], v[116:119], a[76:79], v[4:7]\n\t"
    "s_waitcnt lgkmcnt(9)\n\t"
    "v_exp_f32 v35, v35\n\t"
    "v_mfma_f32_16x16x32_bf16 v[0:3], v[120:123], a[96:99], v[0:3]\n\t"
    "v_fma_f32 v36, v36, s72, v241\n\t"
    "v_fma_f32 v37, v37, s72, v241\n\t"
    "v_mfma_f32_16x16x32_bf16 v[0:3], v[124:127], a[100:103], v[0:3]\n\t"
    "ds_read_b128 v[100:103], v225 offset:16384\n\t"
    "v_exp_f32 v36, v36\n\t"
    "v_mfma_f32_16x16x32_bf16 v[4:7], v[120:123], a[104:107], v[4:7]\n\t"
    "ds_read_b128 v[104:107], v224 offset:20480\n\t"
    "v_cvt_pk_bf16_f32 v34, v34, v35\n\t"
    "v_accvgpr_write_b32 a147, v34\n\t"
    "v_mfma_f32_16x16x32_bf16 v[4:7], v[124:127], a[108:111], v[4:7]\n\t"
    "s_waitcnt lgkmcnt(9)\n\t"
    "v_exp_f32 v37, v37\n\t"
    "v_mfma_f32_16x16x32_bf16 v[16:19], v[128:131], a[0:3], 0\n\t"
    "v_fma_f32 v38, v38, s72, v241\n\t"
    "v_fma_f32 v39, v39, s72, v241\n\t"
    "v_mfma_f32_16x16x32_bf16 v[16:19], v[132:135], a[4:7], v[16:19]\n\t"
    "ds_read_b128 v[108:111], v225 offset:20480\n\t"
    "v_exp_f32 v38, v38\n\t"
    "v_mfma_f32_16x16x32_bf16 v[20:23], v[128:131], a[8:11], 0\n\t"
    "ds_read_b128 v[112:115], v224 offset:24576\n\t"
    "v_cvt_pk_bf16_f32 v36, v36, v37\n\t"
    "v_accvgpr_write_b32 a150, v36\n\t"
    "v_mfma_f32_16x16x32_bf16 v[20:23], v[132:135], a[12:15], v[20:23]\n\t"
    "s_waitcnt lgkmcnt(9)\n\t"
    "v_exp_f32 v39, v39\n\t"
    "v_mfma_f32_16x16x32_bf16 v[252:255], v[48:51], a[144:147], v[252:255]\n\t"
    "v_mfma_f32_16x16x32_bf16 v[16:19], v[136:139], a[32:35], v[16:19]\n\t"
    "v_fma_f32 v0, v0, s72, v240\n\t"
    "v_fma_f32 v1, v1, s72, v240\n\t"
    "v_mfma_f32_16x16x32_bf16 v[16:19], v[140:143], a[36:39], v[16:19]\n\t"
    "ds_read_b128 v[116:119], v225 offset:24576\n\t"
    "v_exp_f32 v0, v0\n\t"
    "v_mfma_f32_16x16x32_bf16 v[20:23], v[136:139], a[40:43], v[20:23]\n\t"
    "ds_read_b128 v[120:123], v224 offset:28672\n\t"
    "v_cvt_pk_bf16_f32 v38, v38, v39\n\t"
    "v_accvgpr_write_b32 a151, v38\n\t"
    "v_mfma_f32_16x16x32_bf16 v[20:23], v[140:143], a[44:47], v[20:23]\n\t"
    "s_waitcnt lgkmcnt(9)\n\t"
    "v_exp_f32 v1, v1\n\t"
    "v_mfma_f32_16x16x32_bf16 v[16:19], v[144:147], a[64:67], v[16:19]\n\t"
    "v_fma_f32 v2, v2, s72, v240\n\t"
    "v_fma_f32 v3, v3, s72, v240\n\t"
    "v_mfma_f32_16x16x32_bf16 v[16:19], v[148:151], a[68:71], v[16:19]\n\t"
    "ds_read_b128 v[124:127], v225 offset:28672\n\t"
    "v_exp_f32 v2, v2\n\t"
    "v_mfma_f32_16x16x32_bf16 v[20:23], v[144:147], a[72:75], v[20:23]\n\t"
    "ds_read_b128 v[128:131], v224 offset:18432\n\t"
    "v_cvt_pk_bf16_f32 v0, v0, v1\n\t"
    "v_accvgpr_write_b32 a160, v0\n\t"
    "v_mfma_f32_16x16x32_bf16 v[20:23], v[148:151], a[76:79], v[20:23]\n\t"
    "s_waitcnt lgkmcnt(9)\n\t"
    "v_exp_f32 v3, v3\n\t"
    "v_mfma_f32_16x16x32_bf16 v[252:255], v[52:55], a[148:151], v[252:255]\n\t"
    "v_mfma_f32_16x16x32_bf16 v[16:19], v[152:155], a[96:99], v[16:19]\n\t"
    "v_fma_f32 v4, v4, s72, v241\n\t"
    "v_fma_f32 v5, v5, s72, v241\n\t"
    "v_mfma_f32_16x16x32_bf16 v[16:19], v[156:159], a[100:103], v[16:19]\n\t"
    "ds_read_b128 v[132:135], v225 offset:18432\n\t"
    "v_exp_f32 v4, v4\n\t"
    "v_mfma_f32_16x16x32_bf16 v[20:23], v[152:155], a[104:107], v[20:23]\n\t"
    "ds_read_b128 v[136:139], v224 offset:22528\n\t"
    "v_cvt_pk_bf16_f32 v2, v2, v3\n\t"
    "v_accvgpr_write_b32 a161, v2\n\t"
    "v_mfma_f32_16x16x32_bf16 v[20:23], v[156:159], a[108:111], v[20:23]\n\t"
    "s_waitcnt lgkmcnt(9)\n\t"
    "v_exp_f32 v5, v5\n\t"
    "v_mfma_f32_16x16x32_bf16 v[32:35], v[96:99], a[0:3], 0\n\t"
    "v_fma_f32 v6, v6, s72, v241\n\t"
    "v_fma_f32 v7, v7, s72, v241\n\t"
    "v_mfma_f32_16x16x32_bf16 v[32:35], v[100:103], a[4:7], v[32:35]\n\t"
    "ds_read_b128 v[140:143], v225 offset:22528\n\t"
    "v_exp_f32 v6, v6\n\t"
    "v_mfma_f32_16x16x32_bf16 v[36:39], v[96:99], a[8:11], 0\n\t"
    "ds_read_b128 v[144:147], v224 offset:26624\n\t"
    "v_cvt_pk_bf16_f32 v4, v4, v5\n\t"
    "v_accvgpr_write_b32 a164, v4\n\t"
    "v_mfma_f32_16x16x32_bf16 v[36:39], v[100:103], a[12:15], v[36:39]\n\t"
    "s_waitcnt lgkmcnt(9)\n\t"
    "v_exp_f32 v7, v7\n\t"
    "v_mfma_f32_16x16x32_bf16 v[32:35], v[104:107], a[32:35], v[32:35]\n\t"
    "v_fma_f32 v16, v16, s72, v240\n\t"
    "v_fma_f32 v17, v17, s72, v240\n\t"
    "v_mfma_f32_16x16x32_bf16 v[32:35], v[108:111], a[36:39], v[32:35]\n\t"
    "ds_read_b128 v[148:151], v225 offset:26624\n\t"
    "v_exp_f32 v16, v16\n\t"
    "v_mfma_f32_16x16x32_bf16 v[36:39], v[104:107], a[40:43], v[36:39]\n\t"
    "ds_read_b128 v[152:155], v224 offset:30720\n\t"
    "v_cvt_pk_bf16_f32 v6, v6, v7\n\t"
    "v_accvgpr_write_b32 a165, v6\n\t"
    "v_mfma_f32_16x16x32_bf16 v[36:39], v[108:111], a[44:47], v[36:39]\n\t"
    "s_waitcnt lgkmcnt(9)\n\t"
    "v_exp_f32 v17, v17\n\t"
    "v_mfma_f32_16x16x32_bf16 v[32:35], v[112:115], a[64:67], v[32:35]\n\t"
    "v_fma_f32 v18, v18, s72, v240\n\t"
    "v_fma_f32 v19, v19, s72, v240\n\t"
    "v_mfma_f32_16x16x32_bf16 v[32:35], v[116:119], a[68:71], v[32:35]\n\t"
    "ds_read_b128 v[156:159], v225 offset:30720\n\t"
    "v_add_u32 v224, 0xffff0000, v224\n\t"
    "v_exp_f32 v18, v18\n\t"
    "v_mfma_f32_16x16x32_bf16 v[36:39], v[112:115], a[72:75], v[36:39]\n\t"
    "ds_read_b128 v[96:99], v224 offset:0\n\t"
    "v_cvt_pk_bf16_f32 v16, v16, v17\n\t"
    "v_accvgpr_write_b32 a162, v16\n\t"
    "v_mfma_f32_16x16x32_bf16 v[36:39], v[116:119], a[76:79], v[36:39]\n\t"
    "s_waitcnt lgkmcnt(9)\n\t"
    "v_exp_f32 v19, v19\n\t"
    "v_mfma_f32_16x16x32_bf16 v[32:35], v[120:123], a[96:99], v[32:35]\n\t"
    "v_add_u32 v225, 0xffff0000, v225\n\t"
    "v_fma_f32 v20, v20, s72, v241\n\t"
    "v_fma_f32 v21, v21, s72, v241\n\t"
    "v_mfma_f32_16x16x32_bf16 v[32:35], v[124:127], a[100:103], v[32:35]\n\t"
    "ds_read_b128 v[100:103], v225 offset:0\n\t"
    "v_exp_f32 v20, v20\n\t"
    "v_mfma_f32_16x16x32_bf16 v[36:39], v[120:123], a[104:107], v[36:39]\n\t"
    "ds_read_b128 v[104:107], v224 offset:4096\n\t"
    "v_cvt_pk_bf16_f32 v18, v18, v19\n\t"
    "v_accvgpr_write_b32 a163, v18\n\t"
    "v_mfma_f32_16x16x32_bf16 v[36:39], v[124:127], a[108:111], v[36:39]\n\t"
    "s_waitcnt lgkmcnt(9)\n\t"
    "v_exp_f32 v21, v21\n\t"
    "v_mfma_f32_16x16x32_bf16 v[0:3], v[128:131], a[0:3], 0\n\t"
    "v_fma_f32 v22, v22, s72, v241\n\t"
    "v_fma_f32 v23, v23, s72, v241\n\t"
    "v_mfma_f32_16x16x32_bf16 v[0:3], v[132:135], a[4:7], v[0:3]\n\t"
    "ds_read_b128 v[108:111], v225 offset:4096\n\t"
    "v_exp_f32 v22, v22\n\t"
    "v_mfma_f32_16x16x32_bf16 v[4:7], v[128:131], a[8:11], 0\n\t"
    "ds_read_b128 v[112:115], v224 offset:2048\n\t"
    "v_cvt_pk_bf16_f32 v20, v20, v21\n\t"
    "v_accvgpr_write_b32 a166, v20\n\t"
    "v_mfma_f32_16x16x32_bf16 v[4:7], v[132:135], a[12:15], v[4:7]\n\t"
    "s_waitcnt lgkmcnt(9)\n\t"
    "v_exp_f32 v23, v23\n\t"
    "v_mfma_f32_16x16x32_bf16 v[252:255], v[48:51], a[160:163], v[252:255]\n\t"
    "v_mfma_f32_16x16x32_bf16 v[0:3], v[136:139], a[32:35], v[0:3]\n\t"
    "v_fma_f32 v32, v32, s72, v240\n\t"
    "v_fma_f32 v33, v33, s72, v240\n\t"
    "v_mfma_f32_16x16x32_bf16 v[0:3], v[140:143], a[36:39], v[0:3]\n\t"
    "ds_read_b128 v[116:119], v225 offset:2048\n\t"
    "v_exp_f32 v32, v32\n\t"
    "v_mfma_f32_16x16x32_bf16 v[4:7], v[136:139], a[40:43], v[4:7]\n\t"
    "ds_read_b128 v[120:123], v224 offset:6144\n\t"
    "v_cvt_pk_bf16_f32 v22, v22, v23\n\t"
    "v_accvgpr_write_b32 a167, v22\n\t"
    "v_mfma_f32_16x16x32_bf16 v[4:7], v[140:143], a[44:47], v[4:7]\n\t"
    "s_waitcnt lgkmcnt(9)\n\t"
    "v_exp_f32 v33, v33\n\t"
    "v_mfma_f32_16x16x32_bf16 v[0:3], v[144:147], a[64:67], v[0:3]\n\t"
    "v_fma_f32 v34, v34, s72, v240\n\t"
    "v_fma_f32 v35, v35, s72, v240\n\t"
    "s_waitcnt vmcnt(14)\n\t"
    "ds_read_b128 a[16:19], v226 offset:32768\n\t"
    "ds_read_b128 a[20:23], v227 offset:32768\n\t"
    "s_waitcnt vmcnt(12)\n\t"
    "ds_read_b128 a[48:51], v226 offset:34816\n\t"
    "ds_read_b128 a[52:55], v227 offset:34816\n\t"
    "s_waitcnt vmcnt(10)\n\t"
    "ds_read_b128 a[24:27], v226 offset:49152\n\t"
    "ds_read_b128 a[28:31], v227 offset:49152\n\t"
    "s_waitcnt vmcnt(8)\n\t"
    "ds_read_b128 a[56:59], v226 offset:51200\n\t"
    "ds_read_b128 a[60:63], v227 offset:51200\n\t"
    "v_mfma_f32_16x16x32_bf16 v[0:3], v[148:151], a[68:71], v[0:3]\n\t"
    "ds_read_b128 v[124:127], v225 offset:6144\n\t"
    "v_exp_f32 v34, v34\n\t"
    "v_mfma_f32_16x16x32_bf16 v[4:7], v[144:147], a[72:75], v[4:7]\n\t"
    "ds_read_b128 v[128:131], v224 offset:16384\n\t"
    "v_cvt_pk_bf16_f32 v32, v32, v33\n\t"
    "v_accvgpr_write_b32 a176, v32\n\t"
    "v_mfma_f32_16x16x32_bf16 v[4:7], v[148:151], a[76:79], v[4:7]\n\t"
    "s_waitcnt lgkmcnt(15)\n\t"
    "v_exp_f32 v35, v35\n\t"
    "v_mfma_f32_16x16x32_bf16 v[252:255], v[52:55], a[164:167], v[252:255]\n\t"
    "v_mfma_f32_16x16x32_bf16 v[0:3], v[152:155], a[96:99], v[0:3]\n\t"
    "v_fma_f32 v36, v36, s72, v241\n\t"
    "v_fma_f32 v37, v37, s72, v241\n\t"
    "v_mfma_f32_16x16x32_bf16 v[0:3], v[156:159], a[100:103], v[0:3]\n\t"
    "ds_read_b128 v[132:135], v225 offset:16384\n\t"
    "v_exp_f32 v36, v36\n\t"
    "v_mfma_f32_16x16x32_bf16 v[4:7], v[152:155], a[104:107], v[4:7]\n\t"
    "ds_read_b128 v[136:139], v224 offset:20480\n\t"
    "v_cvt_pk_bf16_f32 v34, v34, v35\n\t"
    "v_accvgpr_write_b32 a177, v34\n\t"
    "v_mfma_f32_16x16x32_bf16 v[4:7], v[156:159], a[108:111], v[4:7]\n\t"
    "v_exp_f32 v37, v37\n\t"
    "s_waitcnt lgkmcnt(11)\n\t"
    "v_mfma_f32_16x16x32_bf16 v[160:163], v[96:99], a[16:19], 0\n\t"
    "s_xor_b32 s71, s64, 0x18000\n\t"
    "v_fma_f32 v38, v38, s72, v241\n\t"
    "v_fma_f32 v39, v39, s72, v241\n\t"
    "s_nop 3\n\t"
    "s_waitcnt lgkmcnt(10)\n\t"
    "v_mfma_f32_16x16x32_bf16 v[160:163], v[100:103], a[20:23], v[160:163]\n\t"
    "ds_read_b128 v[140:143], v225 offset:20480\n\t"
    "v_exp_f32 v38, v38\n\t"
    "s_waitcnt lgkmcnt(8)\n\t"
    "v_mfma_f32_16x16x32_bf16 v[164:167], v[96:99], a[24:27], 0\n\t"
    "ds_read_b128 v[144:147], v224 offset:18432\n\t"
    "v_cvt_pk_bf16_f32 v36, v36, v37\n\t"
    "v_accvgpr_write_b32 a180, v36\n\t"
    "s_nop 3\n\t"
    "s_waitcnt lgkmcnt(8)\n\t"
    "v_mfma_f32_16x16x32_bf16 v[164:167], v[100:103], a[28:31], v[164:167]\n\t"
    "v_exp_f32 v39, v39\n\t"
    "v_mfma_f32_16x16x32_bf16 v[160:163], v[104:107], a[48:51], v[160:163]\n\t"
    "s_add_u32 m0, s70, 0x18000\n\t"
    "s_nop 3\n\t"
    "v_mfma_f32_16x16x32_bf16 v[160:163], v[108:111], a[52:55], v[160:163]\n\t"
    "ds_read_b128 v[148:151], v225 offset:18432\n\t"
    "s_waitcnt lgkmcnt(8)\n\t"
    "v_mfma_f32_16x16x32_bf16 v[164:167], v[104:107], a[56:59], v[164:167]\n\t"
    "ds_read_b128 v[152:155], v224 offset:22528\n\t"
    "v_cvt_pk_bf16_f32 v38, v38, v39\n\t"
    "v_accvgpr_write_b32 a181, v38\n\t"
    "s_nop 3\n\t"
    "s_waitcnt lgkmcnt(8)\n\t"
    "v_mfma_f32_16x16x32_bf16 v[164:167], v[108:111], a[60:63], v[164:167]\n\t"
    "v_mfma_f32_16x16x32_bf16 v[168:171], v[112:115], a[16:19], 0\n\t"
    "buffer_load_dwordx4 v232, s[40:43], s71 offen offset:0 nt lds\n\t"
    "s_nop 3\n\t"
    "v_mfma_f32_16x16x32_bf16 v[168:171], v[116:119], a[20:23], v[168:171]\n\t"
    "ds_read_b128 v[156:159], v225 offset:22528\n\t"
    "v_mfma_f32_16x16x32_bf16 v[172:175], v[112:115], a[24:27], 0\n\t"
    "ds_read_b128 v[96:99], v224 offset:32768\n\t"
    "s_nop 3\n\t"
    "v_mfma_f32_16x16x32_bf16 v[172:175], v[116:119], a[28:31], v[172:175]\n\t"
    "s_waitcnt lgkmcnt(9)\n\t"
    "v_mfma_f32_16x16x32_bf16 v[168:171], v[120:123], a[48:51], v[168:171]\n\t"
    "s_nop 3\n\t"
    "v_mfma_f32_16x16x32_bf16 v[168:171], v[124:127], a[52:55], v[168:171]\n\t"
    "ds_read_b128 v[100:103], v225 offset:32768\n\t"
    "v_mfma_f32_16x16x32_bf16 v[172:175], v[120:123], a[56:59], v[172:175]\n\t"
    "ds_read_b128 v[104:107], v224 offset:36864\n\t"
    "s_nop 3\n\t"
    "v_mfma_f32_16x16x32_bf16 v[172:175], v[124:127], a[60:63], v[172:175]\n\t"
    "s_waitcnt lgkmcnt(9)\n\t"
    "v_mfma_f32_16x16x32_bf16 v[176:179], v[128:131], a[16:19], 0\n\t"
    "s_nop 3\n\t"
    "v_mfma_f32_16x16x32_bf16 v[176:179], v[132:135], a[20:23], v[176:179]\n\t"
    "ds_read_b128 v[108:111], v225 offset:36864\n\t"
    "v_mfma_f32_16x16x32_bf16 v[180:183], v[128:131], a[24:27], 0\n\t"
    "ds_read_b128 v[112:115], v224 offset:34816\n\t"
    "s_nop 3\n\t"
    "v_mfma_f32_16x16x32_bf16 v[180:183], v[132:135], a[28:31], v[180:183]\n\t"
    "s_waitcnt lgkmcnt(9)\n\t"
    "v_mfma_f32_16x16x32_bf16 v[176:179], v[136:139], a[48:51], v[176:179]\n\t"
    "buffer_load_dwordx4 v233, s[40:43], s71 offen offset:1024 nt lds\n\t"
    "s_nop 3\n\t"
    "v_mfma_f32_16x16x32_bf16 v[176:179], v[140:143], a[52:55], v[176:179]\n\t"
    "ds_read_b128 v[116:119], v225 offset:34816\n\t"
    "v_mfma_f32_16x16x32_bf16 v[180:183], v[136:139], a[56:59], v[180:183]\n\t"
    "ds_read_b128 v[120:123], v224 offset:38912\n\t"
    "s_nop 3\n\t"
    "v_mfma_f32_16x16x32_bf16 v[180:183], v[140:143], a[60:63], v[180:183]\n\t"
    "s_waitcnt lgkmcnt(9)\n\t"
    "v_mfma_f32_16x16x32_bf16 v[184:187], v[144:147], a[16:19], 0\n\t"
    "s_nop 3\n\t"
    "v_mfma_f32_16x16x32_bf16 v[184:187], v[148:151], a[20:23], v[184:187]\n\t"
    "ds_read_b128 v[124:127], v225 offset:38912\n\t"
    "v_mfma_f32_16x16x32_bf16 v[188:191], v[144:147], a[24:27], 0\n\t"
    "ds_read_b128 v[128:131], v224 offset:49152\n\t"
    "s_nop 3\n\t"
    "v_mfma_f32_16x16x32_bf16 v[188:191], v[148:151], a[28:31], v[188:191]\n\t"
    "s_waitcnt lgkmcnt(9)\n\t"
    "v_mfma_f32_16x16x32_bf16 v[184:187], v[152:155], a[48:51], v[184:187]\n\t"
    "buffer_load_dwordx4 v232, s[40:43], s71 offen offset:2048 nt lds\n\t"
    "s_nop 3\n\t"
    "v_mfma_f32_16x16x32_bf16 v[184:187], v[156:159], a[52:55], v[184:187]\n\t"
    "ds_read_b128 v[132:135], v225 offset:49152\n\t"
    "v_mfma_f32_16x16x32_bf16 v[188:191], v[152:155], a[56:59], v[188:191]\n\t"
    "ds_read_b128 v[136:139], v224 offset:53248\n\t"
    "s_nop 3\n\t"
    "v_mfma_f32_16x16x32_bf16 v[188:191], v[156:159], a[60:63], v[188:191]\n\t"
    "s_waitcnt lgkmcnt(9)\n\t"
    "v_mfma_f32_16x16x32_bf16 v[192:195], v[96:99], a[16:19], 0\n\t"
    "s_nop 3\n\t"
    "v_mfma_f32_16x16x32_bf16 v[192:195], v[100:103], a[20:23], v[192:195]\n\t"
    "ds_read_b128 v[140:143], v225 offset:53248\n\t"
    "v_mfma_f32_16x16x32_bf16 v[196:199], v[96:99], a[24:27], 0\n\t"
    "ds_read_b128 v[144:147], v224 offset:51200\n\t"
    "s_nop 3\n\t"
    "v_mfma_f32_16x16x32_bf16 v[196:199], v[100:103], a[28:31], v[196:199]\n\t"
    "s_waitcnt lgkmcnt(9)\n\t"
    "v_mfma_f32_16x16x32_bf16 v[192:195], v[104:107], a[48:51], v[192:195]\n\t"
    "buffer_load_dwordx4 v233, s[40:43], s71 offen offset:3072 nt lds\n\t"
    "s_nop 3\n\t"
    "v_mfma_f32_16x16x32_bf16 v[192:195], v[108:111], a[52:55], v[192:195]\n\t"
    "s_xor_b32 s71, s64, 0x1c000\n\t"
    "ds_read_b128 v[148:151], v225 offset:51200\n\t"
    "v_mfma_f32_16x16x32_bf16 v[196:199], v[104:107], a[56:59], v[196:199]\n\t"
    "s_add_u32 m0, s70, 0x1c000\n\t"
    "ds_read_b128 v[152:155], v224 offset:55296\n\t"
    "s_nop 3\n\t"
    "v_mfma_f32_16x16x32_bf16 v[196:199], v[108:111], a[60:63], v[196:199]\n\t"
    "s_waitcnt lgkmcnt(9)\n\t"
    "v_mfma_f32_16x16x32_bf16 v[200:203], v[112:115], a[16:19], 0\n\t"
    "s_nop 3\n\t"
    "v_mfma_f32_16x16x32_bf16 v[200:203], v[116:119], a[20:23], v[200:203]\n\t"
    "ds_read_b128 v[156:159], v225 offset:55296\n\t"
    "v_add_u32 v224, 0x10000, v224\n\t"
    "v_mfma_f32_16x16x32_bf16 v[204:207], v[112:115], a[24:27], 0\n\t"
    "ds_read_b128 v[96:99], v224 offset:0\n\t"
    "s_nop 3\n\t"
    "v_mfma_f32_16x16x32_bf16 v[204:207], v[116:119], a[28:31], v[204:207]\n\t"
    "s_waitcnt lgkmcnt(9)\n\t"
    "v_mfma_f32_16x16x32_bf16 v[200:203], v[120:123], a[48:51], v[200:203]\n\t"
    "v_add_u32 v225, 0x10000, v225\n\t"
    "s_nop 3\n\t"
    "v_mfma_f32_16x16x32_bf16 v[200:203], v[124:127], a[52:55], v[200:203]\n\t"
    "ds_read_b128 v[100:103], v225 offset:0\n\t"
    "v_mfma_f32_16x16x32_bf16 v[204:207], v[120:123], a[56:59], v[204:207]\n\t"
    "ds_read_b128 v[104:107], v224 offset:4096\n\t"
    "s_nop 3\n\t"
    "v_mfma_f32_16x16x32_bf16 v[204:207], v[124:127], a[60:63], v[204:207]\n\t"
    "s_waitcnt lgkmcnt(9)\n\t"
    "v_mfma_f32_16x16x32_bf16 v[212:215], v[128:131], a[16:19], 0\n\t"
    "buffer_load_dwordx4 v232, s[40:43], s71 offen offset:0 nt lds\n\t"
    "s_nop 3\n\t"
    "v_mfma_f32_16x16x32_bf16 v[212:215], v[132:135], a[20:23], v[212:215]\n\t"
    "ds_read_b128 v[108:111], v225 offset:4096\n\t"
    "v_mfma_f32_16x16x32_bf16 v[216:219], v[128:131], a[24:27], 0\n\t"
    "ds_read_b128 v[112:115], v224 offset:2048\n\t"
    "s_nop 3\n\t"
    "v_mfma_f32_16x16x32_bf16 v[216:219], v[132:135], a[28:31], v[216:219]\n\t"
    "s_waitcnt lgkmcnt(9)\n\t"
    "v_mfma_f32_16x16x32_bf16 v[212:215], v[136:139], a[48:51], v[212:215]\n\t"
    "s_nop 3\n\t"
    "v_mfma_f32_16x16x32_bf16 v[212:215], v[140:143], a[52:55], v[212:215]\n\t"
    "ds_read_b128 v[116:119], v225 offset:2048\n\t"
    "v_mfma_f32_16x16x32_bf16 v[216:219], v[136:139], a[56:59], v[216:219]\n\t"
    "ds_read_b128 v[120:123], v224 offset:6144\n\t"
    "s_nop 3\n\t"
    "v_mfma_f32_16x16x32_bf16 v[216:219], v[140:143], a[60:63], v[216:219]\n\t"
    "s_waitcnt lgkmcnt(9)\n\t"
    "v_mfma_f32_16x16x32_bf16 v[244:247], v[144:147], a[16:19], 0\n\t"
    "buffer_load_dwordx4 v233, s[40:43], s71 offen offset:1024 nt lds\n\t"
    "s_nop 3\n\t"
    "v_mfma_f32_16x16x32_bf16 v[244:247], v[148:151], a[20:23], v[244:247]\n\t"
    "ds_read_b128 v[124:127], v225 offset:6144\n\t"
    "v_mfma_f32_16x16x32_bf16 v[248:251], v[144:147], a[24:27], 0\n\t"
    "ds_read_b128 v[128:131], v224 offset:16384\n\t"
    "s_nop 3\n\t"
    "v_mfma_f32_16x16x32_bf16 v[248:251], v[148:151], a[28:31], v[248:251]\n\t"
    "s_waitcnt lgkmcnt(9)\n\t"
    "v_mfma_f32_16x16x32_bf16 v[244:247], v[152:155], a[48:51], v[244:247]\n\t"
    "s_nop 3\n\t"
    "v_mfma_f32_16x16x32_bf16 v[244:247], v[156:159], a[52:55], v[244:247]\n\t"
    "ds_read_b128 v[132:135], v225 offset:16384\n\t"
    "v_mfma_f32_16x16x32_bf16 v[248:251], v[152:155], a[56:59], v[248:251]\n\t"
    "ds_read_b128 v[136:139], v224 offset:20480\n\t"
    "s_nop 3\n\t"
    "v_mfma_f32_16x16x32_bf16 v[248:251], v[156:159], a[60:63], v[248:251]\n\t"
    "s_waitcnt lgkmcnt(9)\n\t"
    "v_mfma_f32_16x16x32_bf16 v[16:19], v[96:99], a[16:19], 0\n\t"
    "buffer_load_dwordx4 v232, s[40:43], s71 offen offset:2048 nt lds\n\t"
    "s_nop 3\n\t"
    "v_mfma_f32_16x16x32_bf16 v[16:19], v[100:103], a[20:23], v[16:19]\n\t"
    "ds_read_b128 v[140:143], v225 offset:20480\n\t"
    "v_mfma_f32_16x16x32_bf16 v[20:23], v[96:99], a[24:27], 0\n\t"
    "ds_read_b128 v[144:147], v224 offset:18432\n\t"
    "s_nop 3\n\t"
    "v_mfma_f32_16x16x32_bf16 v[20:23], v[100:103], a[28:31], v[20:23]\n\t"
    "s_waitcnt lgkmcnt(9)\n\t"
    "v_mfma_f32_16x16x32_bf16 v[16:19], v[104:107], a[48:51], v[16:19]\n\t"
    "s_nop 3\n\t"
    "v_mfma_f32_16x16x32_bf16 v[16:19], v[108:111], a[52:55], v[16:19]\n\t"
    "ds_read_b128 v[148:151], v225 offset:18432\n\t"
    "v_mfma_f32_16x16x32_bf16 v[20:23], v[104:107], a[56:59], v[20:23]\n\t"
    "ds_read_b128 v[152:155], v224 offset:22528\n\t"
    "s_nop 3\n\t"
    "v_mfma_f32_16x16x32_bf16 v[20:23], v[108:111], a[60:63], v[20:23]\n\t"
    "s_waitcnt lgkmcnt(9)\n\t"
    "v_mfma_f32_16x16x32_bf16 v[24:27], v[112:115], a[16:19], 0\n\t"
    "buffer_load_dwordx4 v233, s[40:43], s71 offen offset:3072 nt lds\n\t"
    "s_nop 3\n\t"
    "v_mfma_f32_16x16x32_bf16 v[24:27], v[116:119], a[20:23], v[24:27]\n\t"
    "ds_read_b128 v[156:159], v225 offset:22528\n\t"
    "v_add_u32 v224, 0xffff0000, v224\n\t"
    "v_mfma_f32_16x16x32_bf16 v[28:31], v[112:115], a[24:27], 0\n\t"
    "ds_read_b128 v[96:99], v224 offset:8192\n\t"
    "s_nop 3\n\t"
    "v_mfma_f32_16x16x32_bf16 v[28:31], v[116:119], a[28:31], v[28:31]\n\t"
    "s_waitcnt lgkmcnt(9)\n\t"
    "v_mfma_f32_16x16x32_bf16 v[24:27], v[120:123], a[48:51], v[24:27]\n\t"
    "v_add_u32 v225, 0xffff0000, v225\n\t"
    "s_nop 3\n\t"
    "v_mfma_f32_16x16x32_bf16 v[24:27], v[124:127], a[52:55], v[24:27]\n\t"
    "ds_read_b128 v[100:103], v225 offset:8192\n\t"
    "v_mfma_f32_16x16x32_bf16 v[28:31], v[120:123], a[56:59], v[28:31]\n\t"
    "ds_read_b128 v[104:107], v224 offset:12288\n\t"
    "s_nop 3\n\t"
    "v_mfma_f32_16x16x32_bf16 v[28:31], v[124:127], a[60:63], v[28:31]\n\t"
    "s_waitcnt lgkmcnt(9)\n\t"
    "v_mfma_f32_16x16x32_bf16 v[40:43], v[128:131], a[16:19], 0\n\t"
    "s_nop 3\n\t"
    "v_mfma_f32_16x16x32_bf16 v[40:43], v[132:135], a[20:23], v[40:43]\n\t"
    "ds_read_b128 v[108:111], v225 offset:12288\n\t"
    "v_mfma_f32_16x16x32_bf16 v[44:47], v[128:131], a[24:27], 0\n\t"
    "ds_read_b128 v[112:115], v224 offset:10240\n\t"
    "s_nop 3\n\t"
    "v_mfma_f32_16x16x32_bf16 v[44:47], v[132:135], a[28:31], v[44:47]\n\t"
    "s_waitcnt lgkmcnt(9)\n\t"
    "v_mfma_f32_16x16x32_bf16 v[40:43], v[136:139], a[48:51], v[40:43]\n\t"
    "s_nop 3\n\t"
    "v_mfma_f32_16x16x32_bf16 v[40:43], v[140:143], a[52:55], v[40:43]\n\t"
    "ds_read_b128 v[116:119], v225 offset:10240\n\t"
    "v_mfma_f32_16x16x32_bf16 v[44:47], v[136:139], a[56:59], v[44:47]\n\t"
    "ds_read_b128 v[120:123], v224 offset:14336\n\t"
    "s_nop 3\n\t"
    "v_mfma_f32_16x16x32_bf16 v[44:47], v[140:143], a[60:63], v[44:47]\n\t"
    "s_waitcnt lgkmcnt(9)\n\t"
    "s_waitcnt vmcnt(14)\n\t"
    "v_add_u32 v226, 0x10000, v226\n\t"
    "ds_read_b128 a[80:83], v226 offset:0\n\t"
    "v_add_u32 v227, 0x10000, v227\n\t"
    "ds_read_b128 a[84:87], v227 offset:0\n\t"
    "s_waitcnt vmcnt(12)\n\t"
    "ds_read_b128 a[112:115], v226 offset:2048\n\t"
    "ds_read_b128 a[116:119], v227 offset:2048\n\t"
    "s_waitcnt vmcnt(10)\n\t"
    "ds_read_b128 a[88:91], v226 offset:16384\n\t"
    "ds_read_b128 a[92:95], v227 offset:16384\n\t"
    "s_waitcnt vmcnt(8)\n\t"
    "ds_read_b128 a[120:123], v226 offset:18432\n\t"
    "ds_read_b128 a[124:127], v227 offset:18432\n\t"
    "v_add_u32 v226, 0xffff0000, v226\n\t"
    "v_add_u32 v227, 0xffff0000, v227\n\t"
    "s_add_u32 s67, s67, 0x2000000\n\t"
    "v_mfma_f32_16x16x32_bf16 v[8:11], v[144:147], a[16:19], 0\n\t"
    "s_nop 3\n\t"
    "v_mfma_f32_16x16x32_bf16 v[8:11], v[148:151], a[20:23], v[8:11]\n\t"
    "ds_read_b128 v[124:127], v225 offset:14336\n\t"
    "v_mfma_f32_16x16x32_bf16 v[12:15], v[144:147], a[24:27], 0\n\t"
    "ds_read_b128 v[128:131], v224 offset:24576\n\t"
    "s_nop 3\n\t"
    "v_mfma_f32_16x16x32_bf16 v[12:15], v[148:151], a[28:31], v[12:15]\n\t"
    "s_waitcnt lgkmcnt(15)\n\t"
    "v_mfma_f32_16x16x32_bf16 v[8:11], v[152:155], a[48:51], v[8:11]\n\t"
    "s_nop 3\n\t"
    "v_mfma_f32_16x16x32_bf16 v[8:11], v[156:159], a[52:55], v[8:11]\n\t"
    "ds_read_b128 v[132:135], v225 offset:24576\n\t"
    "v_mfma_f32_16x16x32_bf16 v[12:15], v[152:155], a[56:59], v[12:15]\n\t"
    "ds_read_b128 v[136:139], v224 offset:28672\n\t"
    "s_nop 3\n\t"
    "v_mfma_f32_16x16x32_bf16 v[12:15], v[156:159], a[60:63], v[12:15]\n\t"
    "s_waitcnt lgkmcnt(11)\n\t"
    "v_mfma_f32_16x16x32_bf16 v[160:163], v[96:99], a[80:83], v[160:163]\n\t"
    "s_xor_b32 s71, s64, 0x20000\n\t"
    "s_add_u32 m0, s70, 0x20000\n\t"
    "s_waitcnt lgkmcnt(10)\n\t"
    "v_mfma_f32_16x16x32_bf16 v[160:163], v[100:103], a[84:87], v[160:163]\n\t"
    "ds_read_b128 v[140:143], v225 offset:28672\n\t"
    "s_waitcnt lgkmcnt(8)\n\t"
    "v_mfma_f32_16x16x32_bf16 v[164:167], v[96:99], a[88:91], v[164:167]\n\t"
    "ds_read_b128 v[144:147], v224 offset:26624\n\t"
    "s_waitcnt lgkmcnt(8)\n\t"
    "v_mfma_f32_16x16x32_bf16 v[164:167], v[100:103], a[92:95], v[164:167]\n\t"
    "v_mfma_f32_16x16x32_bf16 v[160:163], v[104:107], a[112:115], v[160:163]\n\t"
    "v_mfma_f32_16x16x32_bf16 v[160:163], v[108:111], a[116:119], v[160:163]\n\t"
    "ds_read_b128 v[148:151], v225 offset:26624\n\t"
    "s_waitcnt lgkmcnt(8)\n\t"
    "v_mfma_f32_16x16x32_bf16 v[164:167], v[104:107], a[120:123], v[164:167]\n\t"
    "ds_read_b128 v[152:155], v224 offset:30720\n\t"
    "s_waitcnt lgkmcnt(8)\n\t"
    "v_mfma_f32_16x16x32_bf16 v[164:167], v[108:111], a[124:127], v[164:167]\n\t"
    "v_mfma_f32_16x16x32_bf16 v[168:171], v[112:115], a[80:83], v[168:171]\n\t"
    "buffer_load_dwordx4 v232, s[40:43], s71 offen offset:0 nt lds\n\t"
    "v_mfma_f32_16x16x32_bf16 v[168:171], v[116:119], a[84:87], v[168:171]\n\t"
    "ds_read_b128 v[156:159], v225 offset:30720\n\t"
    "v_max3_f32 v242, v160, v161, v162\n\t"
    "v_mfma_f32_16x16x32_bf16 v[172:175], v[112:115], a[88:91], v[172:175]\n\t"
    "ds_read_b128 v[96:99], v224 offset:40960\n\t"
    "v_mfma_f32_16x16x32_bf16 v[172:175], v[116:119], a[92:95], v[172:175]\n\t"
    "s_waitcnt lgkmcnt(9)\n\t"
    "v_max3_f32 v243, v164, v165, v166\n\t"
    "v_max_f32 v242, v242, v163\n\t"
    "v_max_f32 v243, v243, v167\n\t"
    "v_mov_b32 v210, v242\n\t"
    "v_mov_b32 v211, v243\n\t"
    "v_mfma_f32_16x16x32_bf16 v[168:171], v[120:123], a[112:115], v[168:171]\n\t"
    "v_permlane32_swap_b32 v210, v242\n\t"
    "v_permlane32_swap_b32 v211, v243\n\t"
    "v_max_f32 v242, v242, v210\n\t"
    "v_max_f32 v243, v243, v211\n\t"
    "v_mov_b32 v210, v242\n\t"
    "v_mfma_f32_16x16x32_bf16 v[168:171], v[124:127], a[116:119], v[168:171]\n\t"
    "ds_read_b128 v[100:103], v225 offset:40960\n\t"
    "v_mov_b32 v211, v243\n\t"
    "v_permlane16_swap_b32 v210, v242\n\t"
    "s_nop 0\n\t"
    "v_permlane16_swap_b32 v211, v243\n\t"
    "v_max_f32 v242, v242, v210\n\t"
    "v_max_f32 v243, v243, v211\n\t"
    "v_mfma_f32_16x16x32_bf16 v[172:175], v[120:123], a[120:123], v[172:175]\n\t"
    "ds_read_b128 v[104:107], v224 offset:45056\n\t"
    "v_mul_f32 v242, s79, v242\n\t"
    "v_mul_f32 v243, s79, v243\n\t"
    "v_fma_f32 v160, v160, s72, v242\n\t"
    "v_fma_f32 v161, v161, s72, v242\n\t"
    "v_exp_f32 v160, v160\n\t"
    "v_mfma_f32_16x16x32_bf16 v[172:175], v[124:127], a[124:127], v[172:175]\n\t"
    "s_waitcnt lgkmcnt(9)\n\t"
    "v_exp_f32 v161, v161\n\t"
    "v_fma_f32 v162, v162, s72, v242\n\t"
    "v_fma_f32 v163, v163, s72, v242\n\t"
    "v_exp_f32 v162, v162\n\t"
    "v_cvt_pk_bf16_f32 v72, v160, v161\n\t"
    "v_mfma_f32_16x16x32_bf16 v[176:179], v[128:131], a[80:83], v[176:179]\n\t"
    "buffer_load_dwordx4 v233, s[40:43], s71 offen offset:1024 nt lds\n\t"
    "v_exp_f32 v163, v163\n\t"
    "v_fma_f32 v164, v164, s72, v243\n\t"
    "v_fma_f32 v165, v165, s72, v243\n\t"
    "v_exp_f32 v164, v164\n\t"
    "v_cvt_pk_bf16_f32 v73, v162, v163\n\t"
    "v_mfma_f32_16x16x32_bf16 v[176:179], v[132:135], a[84:87], v[176:179]\n\t"
    "ds_read_b128 v[108:111], v225 offset:45056\n\t"
    "v_exp_f32 v165, v165\n\t"
    "v_fma_f32 v166, v166, s72, v243\n\t"
    "v_fma_f32 v167, v167, s72, v243\n\t"
    "v_exp_f32 v166, v166\n\t"
    "v_cvt_pk_bf16_f32 v76, v164, v165\n\t"
    "v_mfma_f32_16x16x32_bf16 v[180:183], v[128:131], a[88:91], v[180:183]\n\t"
    "ds_read_b128 v[112:115], v224 offset:43008\n\t"
    "v_exp_f32 v167, v167\n\t"
    "v_mfma_f32_16x16x32_bf16 v[180:183], v[132:135], a[92:95], v[180:183]\n\t"
    "s_waitcnt lgkmcnt(9)\n\t"
    "v_cvt_pk_bf16_f32 v77, v166, v167\n\t"
    "v_fma_f32 v168, v168, s72, v242\n\t"
    "v_fma_f32 v169, v169, s72, v242\n\t"
    "v_exp_f32 v168, v168\n\t"
    "v_exp_f32 v169, v169\n\t"
    "v_mfma_f32_16x16x32_bf16 v[176:179], v[136:139], a[112:115], v[176:179]\n\t"
    "v_fma_f32 v170, v170, s72, v242\n\t"
    "v_fma_f32 v171, v171, s72, v242\n\t"
    "v_exp_f32 v170, v170\n\t"
    "v_cvt_pk_bf16_f32 v74, v168, v169\n\t"
    "v_exp_f32 v171, v171\n\t"
    "v_mfma_f32_16x16x32_bf16 v[176:179], v[140:143], a[116:119], v[176:179]\n\t"
    "ds_read_b128 v[116:119], v225 offset:43008\n\t"
    "v_fma_f32 v172, v172, s72, v243\n\t"
    "v_fma_f32 v173, v173, s72, v243\n\t"
    "v_exp_f32 v172, v172\n\t"
    "v_cvt_pk_bf16_f32 v75, v170, v171\n\t"
    "v_exp_f32 v173, v173\n\t"
    "v_mfma_f32_16x16x32_bf16 v[180:183], v[136:139], a[120:123], v[180:183]\n\t"
    "ds_read_b128 v[120:123], v224 offset:47104\n\t"
    "v_fma_f32 v174, v174, s72, v243\n\t"
    "v_fma_f32 v175, v175, s72, v243\n\t"
    "v_exp_f32 v174, v174\n\t"
    "v_cvt_pk_bf16_f32 v78, v172, v173\n\t"
    "v_mfma_f32_16x16x32_bf16 v[180:183], v[140:143], a[124:127], v[180:183]\n\t"
    "s_waitcnt lgkmcnt(9)\n\t"
    "v_exp_f32 v175, v175\n\t"
    "v_mfma_f32_16x16x32_bf16 v[184:187], v[144:147], a[80:83], v[184:187]\n\t"
    "buffer_load_dwordx4 v232, s[40:43], s71 offen offset:2048 nt lds\n\t"
    "v_cvt_pk_bf16_f32 v79, v174, v175\n\t"
    "v_mfma_f32_16x16x32_bf16 v[184:187], v[148:151], a[84:87], v[184:187]\n\t"
    "ds_read_b128 v[124:127], v225 offset:47104\n\t"
    "v_fma_f32 v176, v176, s72, v242\n\t"
    "v_fma_f32 v177, v177, s72, v242\n\t"
    "v_exp_f32 v176, v176\n\t"
    "v_exp_f32 v177, v177\n\t"
    "v_fma_f32 v178, v178, s72, v242\n\t"
    "v_mfma_f32_16x16x32_bf16 v[188:191], v[144:147], a[88:91], v[188:191]\n\t"
    "ds_read_b128 v[128:131], v224 offset:57344\n\t"
    "v_fma_f32 v179, v179, s72, v242\n\t"
    "v_exp_f32 v178, v178\n\t"
    "v_cvt_pk_bf16_f32 v88, v176, v177\n\t"
    "v_exp_f32 v179, v179\n\t"
    "v_mfma_f32_16x16x32_bf16 v[188:191], v[148:151], a[92:95], v[188:191]\n\t"
    "s_waitcnt lgkmcnt(9)\n\t"
    "v_fma_f32 v180, v180, s72, v243\n\t"
    "v_fma_f32 v181, v181, s72, v243\n\t"
    "v_exp_f32 v180, v180\n\t"
    "v_cvt_pk_bf16_f32 v89, v178, v179\n\t"
    "v_exp_f32 v181, v181\n\t"
    "v_mfma_f32_16x16x32_bf16 v[184:187], v[152:155], a[112:115], v[184:187]\n\t"
    "v_fma_f32 v182, v182, s72, v243\n\t"
    "v_fma_f32 v183, v183, s72, v243\n\t"
    "v_mfma_f32_16x16x32_bf16 v[184:187], v[156:159], a[116:119], v[184:187]\n\t"
    "ds_read_b128 v[132:135], v225 offset:57344\n\t"
    "v_exp_f32 v182, v182\n\t"
    "v_mfma_f32_16x16x32_bf16 v[188:191], v[152:155], a[120:123], v[188:191]\n\t"
    "ds_read_b128 v[136:139], v224 offset:61440\n\t"
    "v_cvt_pk_bf16_f32 v92, v180, v181\n\t"
    "v_mfma_f32_16x16x32_bf16 v[188:191], v[156:159], a[124:127], v[188:191]\n\t"
    "s_waitcnt lgkmcnt(9)\n\t"
    "v_exp_f32 v183, v183\n\t"
    "v_mfma_f32_16x16x32_bf16 v[192:195], v[96:99], a[80:83], v[192:195]\n\t"
    "buffer_load_dwordx4 v233, s[40:43], s71 offen offset:3072 nt lds\n\t"
    "v_cvt_pk_bf16_f32 v93, v182, v183\n\t"
    "v_mfma_f32_16x16x32_bf16 v[192:195], v[100:103], a[84:87], v[192:195]\n\t"
    "s_xor_b32 s71, s64, 0x24000\n\t"
    "ds_read_b128 v[140:143], v225 offset:61440\n\t"
    "v_fma_f32 v184, v184, s72, v242\n\t"
    "v_fma_f32 v185, v185, s72, v242\n\t"
    "v_exp_f32 v184, v184\n\t"
    "v_exp_f32 v185, v185\n\t"
    "v_fma_f32 v186, v186, s72, v242\n\t"
    "v_mfma_f32_16x16x32_bf16 v[196:199], v[96:99], a[88:91], v[196:199]\n\t"
    "s_add_u32 m0, s70, 0x24000\n\t"
    "ds_read_b128 v[144:147], v224 offset:59392\n\t"
    "v_fma_f32 v187, v187, s72, v242\n\t"
    "v_exp_f32 v186, v186\n\t"
    "v_cvt_pk_bf16_f32 v90, v184, v185\n\t"
    "v_exp_f32 v187, v187\n\t"
    "v_mfma_f32_16x16x32_bf16 v[196:199], v[100:103], a[92:95], v[196:199]\n\t"
    "s_waitcnt lgkmcnt(9)\n\t"
    "v_fma_f32 v188, v188, s72, v243\n\t"
    "v_fma_f32 v189, v189, s72, v243\n\t"
    "v_exp_f32 v188, v188\n\t"
    "v_cvt_pk_bf16_f32 v91, v186, v187\n\t"
    "v_exp_f32 v189, v189\n\t"
    "v_mfma_f32_16x16x32_bf16 v[192:195], v[104:107], a[112:115], v[192:195]\n\t"
    "v_fma_f32 v190, v190, s72, v243\n\t"
    "v_fma_f32 v191, v191, s72, v243\n\t"
    "v_mfma_f32_16x16x32_bf16 v[192:195], v[108:111], a[116:119], v[192:195]\n\t"
    "ds_read_b128 v[148:151], v225 offset:59392\n\t"
    "v_exp_f32 v190, v190\n\t"
    "v_mfma_f32_16x16x32_bf16 v[196:199], v[104:107], a[120:123], v[196:199]\n\t"
    "ds_read_b128 v[152:155], v224 offset:63488\n\t"
    "v_cvt_pk_bf16_f32 v94, v188, v189\n\t"
    "v_mfma_f32_16x16x32_bf16 v[196:199], v[108:111], a[124:127], v[196:199]\n\t"
    "s_waitcnt lgkmcnt(9)\n\t"
    "v_exp_f32 v191, v191\n\t"
    "v_mfma_f32_16x16x32_bf16 v[200:203], v[112:115], a[80:83], v[200:203]\n\t"
    "buffer_load_dwordx4 v232, s[40:43], s71 offen offset:0 nt lds\n\t"
    "v_cvt_pk_bf16_f32 v95, v190, v191\n\t"
    "v_mfma_f32_16x16x32_bf16 v[200:203], v[116:119], a[84:87], v[200:203]\n\t"
    "ds_read_b128 v[156:159], v225 offset:63488\n\t"
    "v_add_u32 v224, 0x10000, v224\n\t"
    "v_fma_f32 v192, v192, s72, v242\n\t"
    "v_fma_f32 v193, v193, s72, v242\n\t"
    "v_exp_f32 v192, v192\n\t"
    "v_exp_f32 v193, v193\n\t"
    "v_fma_f32 v194, v194, s72, v242\n\t"
    "v_mfma_f32_16x16x32_bf16 v[204:207], v[112:115], a[88:91], v[204:207]\n\t"
    "ds_read_b128 v[96:99], v224 offset:8192\n\t"
    "v_fma_f32 v195, v195, s72, v242\n\t"
    "v_exp_f32 v194, v194\n\t"
    "v_cvt_pk_bf16_f32 v192, v192, v193\n\t"
    "v_accvgpr_write_b32 a136, v192\n\t"
    "v_exp_f32 v195, v195\n\t"
    "v_mfma_f32_16x16x32_bf16 v[204:207], v[116:119], a[92:95], v[204:207]\n\t"
    "s_waitcnt lgkmcnt(9)\n\t"
    "v_fma_f32 v196, v196, s72, v243\n\t"
    "v_fma_f32 v197, v197, s72, v243\n\t"
    "v_exp_f32 v196, v196\n\t"
    "v_cvt_pk_bf16_f32 v194, v194, v195\n\t"
    "v_accvgpr_write_b32 a137, v194\n\t"
    "v_mfma_f32_16x16x32_bf16 v[252:255], v[56:59], v[88:91], v[252:255]\n\t"
    "v_exp_f32 v197, v197\n\t"
    "v_fma_f32 v198, v198, s72, v243\n\t"
    "v_fma_f32 v199, v199, s72, v243\n\t"
    "v_mfma_f32_16x16x32_bf16 v[200:203], v[120:123], a[112:115], v[200:203]\n\t"
    "v_add_u32 v225, 0x10000, v225\n\t"
    "v_exp_f32 v198, v198\n\t"
    "v_mfma_f32_16x16x32_bf16 v[200:203], v[124:127], a[116:119], v[200:203]\n\t"
    "ds_read_b128 v[100:103], v225 offset:8192\n\t"
    "v_cvt_pk_bf16_f32 v196, v196, v197\n\t"
    "v_mfma_f32_16x16x32_bf16 v[204:207], v[120:123], a[120:123], v[204:207]\n\t"
    "ds_read_b128 v[104:107], v224 offset:12288\n\t"
    "v_accvgpr_write_b32 a140, v196\n\t"
    "v_mfma_f32_16x16x32_bf16 v[204:207], v[124:127], a[124:127], v[204:207]\n\t"
    "s_waitcnt lgkmcnt(9)\n\t"
    "v_exp_f32 v199, v199\n\t"
    "v_mfma_f32_16x16x32_bf16 v[252:255], v[60:63], v[92:95], v[252:255]\n\t"
    "buffer_load_dwordx4 v233, s[40:43], s71 offen offset:1024 nt lds\n\t"
    "v_mfma_f32_16x16x32_bf16 v[212:215], v[128:131], a[80:83], v[212:215]\n\t"
    "v_cvt_pk_bf16_f32 v198, v198, v199\n\t"
    "v_accvgpr_write_b32 a141, v198\n\t"
    "v_mfma_f32_16x16x32_bf16 v[212:215], v[132:135], a[84:87], v[212:215]\n\t"
    "ds_read_b128 v[108:111], v225 offset:12288\n\t"
    "v_fma_f32 v200, v200, s72, v242\n\t"
    "v_fma_f32 v201, v201, s72, v242\n\t"
    "v_exp_f32 v200, v200\n\t"
    "v_exp_f32 v201, v201\n\t"
    "v_fma_f32 v202, v202, s72, v242\n\t"
    "v_mfma_f32_16x16x32_bf16 v[216:219], v[128:131], a[88:91], v[216:219]\n\t"
    "ds_read_b128 v[112:115], v224 offset:10240\n\t"
    "v_fma_f32 v203, v203, s72, v242\n\t"
    "v_exp_f32 v202, v202\n\t"
    "v_cvt_pk_bf16_f32 v200, v200, v201\n\t"
    "v_accvgpr_write_b32 a138, v200\n\t"
    "v_exp_f32 v203, v203\n\t"
    "v_mfma_f32_16x16x32_bf16 v[216:219], v[132:135], a[92:95], v[216:219]\n\t"
    "s_waitcnt lgkmcnt(9)\n\t"
    "v_fma_f32 v204, v204, s72, v243\n\t"
    "v_fma_f32 v205, v205, s72, v243\n\t"
    "v_exp_f32 v204, v204\n\t"
    "v_cvt_pk_bf16_f32 v202, v202, v203\n\t"
    "v_accvgpr_write_b32 a139, v202\n\t"
    "v_mfma_f32_16x16x32_bf16 v[252:255], v[56:59], v[72:75], v[252:255]\n\t"
    "v_exp_f32 v205, v205\n\t"
    "v_fma_f32 v206, v206, s72, v243\n\t"
    "v_mfma_f32_16x16x32_bf16 v[212:215], v[136:139], a[112:115], v[212:215]\n\t"
    "buffer_load_dwordx4 v232, s[40:43], s71 offen offset:2048 nt lds\n\t"
    "v_mfma_f32_16x16x32_bf16 v[212:215], v[140:143], a[116:119], v[212:215]\n\t"
    "ds_read_b128 v[116:119], v225 offset:10240\n\t"
    "v_fma_f32 v207, v207, s72, v243\n\t"
    "v_mfma_f32_16x16x32_bf16 v[216:219], v[136:139], a[120:123], v[216:219]\n\t"
    "ds_read_b128 v[120:123], v224 offset:14336\n\t"
    "v_exp_f32 v206, v206\n\t"
    "v_mfma_f32_16x16x32_bf16 v[216:219], v[140:143], a[124:127], v[216:219]\n\t"
    "s_waitcnt lgkmcnt(9)\n\t"
    "v_cvt_pk_bf16_f32 v204, v204, v205\n\t"
    "v_accvgpr_write_b32 a142, v204\n\t"
    "v_mfma_f32_16x16x32_bf16 v[252:255], v[60:63], v[76:79], v[252:255]\n\t"
    "v_exp_f32 v207, v207\n\t"
    "v_mfma_f32_16x16x32_bf16 v[244:247], v[144:147], a[80:83], v[244:247]\n\t"
    "v_cvt_pk_bf16_f32 v206, v206, v207\n\t"
    "v_accvgpr_write_b32 a143, v206\n\t"
    "v_mfma_f32_16x16x32_bf16 v[244:247], v[148:151], a[84:87], v[244:247]\n\t"
    "ds_read_b128 v[124:127], v225 offset:14336\n\t"
    "v_fma_f32 v212, v212, s72, v242\n\t"
    "v_fma_f32 v213, v213, s72, v242\n\t"
    "v_exp_f32 v212, v212\n\t"
    "v_exp_f32 v213, v213\n\t"
    "v_fma_f32 v214, v214, s72, v242\n\t"
    "v_mfma_f32_16x16x32_bf16 v[248:251], v[144:147], a[88:91], v[248:251]\n\t"
    "ds_read_b128 v[128:131], v224 offset:24576\n\t"
    "v_fma_f32 v215, v215, s72, v242\n\t"
    "v_exp_f32 v214, v214\n\t"
    "v_cvt_pk_bf16_f32 v212, v212, v213\n\t"
    "v_accvgpr_write_b32 a152, v212\n\t"
    "v_exp_f32 v215, v215\n\t"
    "v_mfma_f32_16x16x32_bf16 v[248:251], v[148:151], a[92:95], v[248:251]\n\t"
    "s_waitcnt lgkmcnt(9)\n\t"
    "v_fma_f32 v216, v216, s72, v243\n\t"
    "v_fma_f32 v217, v217, s72, v243\n\t"
    "v_exp_f32 v216, v216\n\t"
    "v_cvt_pk_bf16_f32 v214, v214, v215\n\t"
    "v_accvgpr_write_b32 a153, v214\n\t"
    "v_mfma_f32_16x16x32_bf16 v[252:255], v[56:59], a[136:139], v[252:255]\n\t"
    "buffer_load_dwordx4 v233, s[40:43], s71 offen offset:3072 nt lds\n\t"
    "v_mfma_f32_16x16x32_bf16 v[244:247], v[152:155], a[112:115], v[244:247]\n\t"
    "v_exp_f32 v217, v217\n\t"
    "v_fma_f32 v218, v218, s72, v243\n\t"
    "v_mfma_f32_16x16x32_bf16 v[244:247], v[156:159], a[116:119], v[244:247]\n\t"
    "ds_read_b128 v[132:135], v225 offset:24576\n\t"
    "v_fma_f32 v219, v219, s72, v243\n\t"
    "v_mfma_f32_16x16x32_bf16 v[248:251], v[152:155], a[120:123], v[248:251]\n\t"
    "ds_read_b128 v[136:139], v224 offset:28672\n\t"
    "v_exp_f32 v218, v218\n\t"
    "v_mfma_f32_16x16x32_bf16 v[248:251], v[156:159], a[124:127], v[248:251]\n\t"
    "s_waitcnt lgkmcnt(9)\n\t"
    "v_cvt_pk_bf16_f32 v216, v216, v217\n\t"
    "v_accvgpr_write_b32 a156, v216\n\t"
    "v_mfma_f32_16x16x32_bf16 v[252:255], v[60:63], a[140:143], v[252:255]\n\t"
    "v_exp_f32 v219, v219\n\t"
    "v_mfma_f32_16x16x32_bf16 v[16:19], v[96:99], a[80:83], v[16:19]\n\t"
    "v_cvt_pk_bf16_f32 v218, v218, v219\n\t"
    "v_accvgpr_write_b32 a157, v218\n\t"
    "v_mfma_f32_16x16x32_bf16 v[16:19], v[100:103], a[84:87], v[16:19]\n\t"
    "ds_read_b128 v[140:143], v225 offset:28672\n\t"
    "v_fma_f32 v244, v244, s72, v242\n\t"
    "v_fma_f32 v245, v245, s72, v242\n\t"
    "v_exp_f32 v244, v244\n\t"
    "v_exp_f32 v245, v245\n\t"
    "v_fma_f32 v246, v246, s72, v242\n\t"
    "v_mfma_f32_16x16x32_bf16 v[20:23], v[96:99], a[88:91], v[20:23]\n\t"
    "ds_read_b128 v[144:147], v224 offset:26624\n\t"
    "v_fma_f32 v247, v247, s72, v242\n\t"
    "v_exp_f32 v246, v246\n\t"
    "v_cvt_pk_bf16_f32 v244, v244, v245\n\t"
    "v_accvgpr_write_b32 a154, v244\n\t"
    "v_exp_f32 v247, v247\n\t"
    "v_mfma_f32_16x16x32_bf16 v[20:23], v[100:103], a[92:95], v[20:23]\n\t"
    "s_waitcnt lgkmcnt(9)\n\t"
    "v_fma_f32 v248, v248, s72, v243\n\t"
    "v_fma_f32 v249, v249, s72, v243\n\t"
    "v_exp_f32 v248, v248\n\t"
    "v_cvt_pk_bf16_f32 v246, v246, v247\n\t"
    "v_accvgpr_write_b32 a155, v246\n\t"
    "v_mfma_f32_16x16x32_bf16 v[16:19], v[104:107], a[112:115], v[16:19]\n\t"
    "v_exp_f32 v249, v249\n\t"
    "v_fma_f32 v250, v250, s72, v243\n\t"
    "v_fma_f32 v251, v251, s72, v243\n\t"
    "v_exp_f32 v250, v250\n\t"
    "v_cvt_pk_bf16_f32 v248, v248, v249\n\t"
    "v_mfma_f32_16x16x32_bf16 v[16:19], v[108:111], a[116:119], v[16:19]\n\t"
    "ds_read_b128 v[148:151], v225 offset:26624\n\t"
    "v_accvgpr_write_b32 a158, v248\n\t"
    "v_exp_f32 v251, v251\n\t"
    "v_mfma_f32_16x16x32_bf16 v[20:23], v[104:107], a[120:123], v[20:23]\n\t"
    "ds_read_b128 v[152:155], v224 offset:30720\n\t"
    "v_cvt_pk_bf16_f32 v250, v250, v251\n\t"
    "v_mfma_f32_16x16x32_bf16 v[20:23], v[108:111], a[124:127], v[20:23]\n\t"
    "s_waitcnt lgkmcnt(9)\n\t"
    "s_waitcnt vmcnt(12)\n\t"
    "v_accvgpr_write_b32 a159, v250\n\t"
    "v_mfma_f32_16x16x32_bf16 v[24:27], v[112:115], a[80:83], v[24:27]\n\t"
    "s_barrier\n\t"
    "v_mfma_f32_16x16x32_bf16 v[24:27], v[116:119], a[84:87], v[24:27]\n\t"
    "ds_read_b128 v[156:159], v225 offset:30720\n\t"
    "v_fma_f32 v16, v16, s72, v242\n\t"
    "v_fma_f32 v17, v17, s72, v242\n\t"
    "v_exp_f32 v16, v16\n\t"
    "v_exp_f32 v17, v17\n\t"
    "v_fma_f32 v18, v18, s72, v242\n\t"
    "v_mfma_f32_16x16x32_bf16 v[28:31], v[112:115], a[88:91], v[28:31]\n\t"
    "ds_read_b128 a[224:227], v224 offset:32768\n\t"
    "v_fma_f32 v19, v19, s72, v242\n\t"
    "v_exp_f32 v18, v18\n\t"
    "v_cvt_pk_bf16_f32 v16, v16, v17\n\t"
    "v_accvgpr_write_b32 a168, v16\n\t"
    "v_exp_f32 v19, v19\n\t"
    "v_mfma_f32_16x16x32_bf16 v[28:31], v[116:119], a[92:95], v[28:31]\n\t"
    "s_waitcnt lgkmcnt(9)\n\t"
    "ds_read_b128 a[228:231], v225 offset:32768\n\t"
    "v_fma_f32 v20, v20, s72, v243\n\t"
    "v_fma_f32 v21, v21, s72, v243\n\t"
    "v_exp_f32 v20, v20\n\t"
    "v_cvt_pk_bf16_f32 v18, v18, v19\n\t"
    "v_accvgpr_write_b32 a169, v18\n\t"
    "v_mfma_f32_16x16x32_bf16 v[252:255], v[56:59], a[152:155], v[252:255]\n\t"
    "ds_read_b128 a[232:235], v224 offset:36864\n\t"
    "v_exp_f32 v21, v21\n\t"
    "v_mfma_f32_16x16x32_bf16 v[24:27], v[120:123], a[112:115], v[24:27]\n\t"
    "ds_read_b128 a[236:239], v225 offset:36864\n\t"
    "v_fma_f32 v22, v22, s72, v243\n\t"
    "v_mfma_f32_16x16x32_bf16 v[24:27], v[124:127], a[116:119], v[24:27]\n\t"
    "ds_read_b128 a[240:243], v224 offset:40960\n\t"
    "v_fma_f32 v23, v23, s72, v243\n\t"
    "v_mfma_f32_16x16x32_bf16 v[28:31], v[120:123], a[120:123], v[28:31]\n\t"
    "v_exp_f32 v22, v22\n\t"
    "v_mfma_f32_16x16x32_bf16 v[28:31], v[124:127], a[124:127], v[28:31]\n\t"
    "s_waitcnt lgkmcnt(11)\n\t"
    "v_cvt_pk_bf16_f32 v20, v20, v21\n\t"
    "v_accvgpr_write_b32 a172, v20\n\t"
    "v_mfma_f32_16x16x32_bf16 v[252:255], v[60:63], a[156:159], v[252:255]\n\t"
    "v_exp_f32 v23, v23\n\t"
    "v_mfma_f32_16x16x32_bf16 v[40:43], v[128:131], a[80:83], v[40:43]\n\t"
    "v_cvt_pk_bf16_f32 v22, v22, v23\n\t"
    "v_accvgpr_write_b32 a173, v22\n\t"
    "v_mfma_f32_16x16x32_bf16 v[40:43], v[132:135], a[84:87], v[40:43]\n\t"
    "v_fma_f32 v24, v24, s72, v242\n\t"
    "v_fma_f32 v25, v25, s72, v242\n\t"
    "v_exp_f32 v24, v24\n\t"
    "v_exp_f32 v25, v25\n\t"
    "v_fma_f32 v26, v26, s72, v242\n\t"
    "v_mfma_f32_16x16x32_bf16 v[44:47], v[128:131], a[88:91], v[44:47]\n\t"
    "v_fma_f32 v27, v27, s72, v242\n\t"
    "v_exp_f32 v26, v26\n\t"
    "v_cvt_pk_bf16_f32 v24, v24, v25\n\t"
    "v_accvgpr_write_b32 a170, v24\n\t"
    "v_exp_f32 v27, v27\n\t"
    "v_mfma_f32_16x16x32_bf16 v[44:47], v[132:135], a[92:95], v[44:47]\n\t"
    "s_waitcnt lgkmcnt(9)\n\t"
    "v_fma_f32 v28, v28, s72, v243\n\t"
    "v_fma_f32 v29, v29, s72, v243\n\t"
    "v_exp_f32 v28, v28\n\t"
    "v_cvt_pk_bf16_f32 v26, v26, v27\n\t"
    "v_accvgpr_write_b32 a171, v26\n\t"
    "v_mfma_f32_16x16x32_bf16 v[40:43], v[136:139], a[112:115], v[40:43]\n\t"
    "v_exp_f32 v29, v29\n\t"
    "v_fma_f32 v30, v30, s72, v243\n\t"
    "v_fma_f32 v31, v31, s72, v243\n\t"
    "v_mfma_f32_16x16x32_bf16 v[40:43], v[140:143], a[116:119], v[40:43]\n\t"
    "v_exp_f32 v30, v30\n\t"
    "v_mfma_f32_16x16x32_bf16 v[44:47], v[136:139], a[120:123], v[44:47]\n\t"
    "v_cvt_pk_bf16_f32 v28, v28, v29\n\t"
    "v_accvgpr_write_b32 a174, v28\n\t"
    "v_mfma_f32_16x16x32_bf16 v[44:47], v[140:143], a[124:127], v[44:47]\n\t"
    "s_waitcnt lgkmcnt(7)\n\t"
    "v_exp_f32 v31, v31\n\t"
    "v_mfma_f32_16x16x32_bf16 v[8:11], v[144:147], a[80:83], v[8:11]\n\t"
    "v_cvt_pk_bf16_f32 v30, v30, v31\n\t"
    "v_accvgpr_write_b32 a175, v30\n\t"
    "v_mfma_f32_16x16x32_bf16 v[8:11], v[148:151], a[84:87], v[8:11]\n\t"
    "v_fma_f32 v40, v40, s72, v242\n\t"
    "v_fma_f32 v41, v41, s72, v242\n\t"
    "v_exp_f32 v40, v40\n\t"
    "v_exp_f32 v41, v41\n\t"
    "v_fma_f32 v42, v42, s72, v242\n\t"
    "v_mfma_f32_16x16x32_bf16 v[12:15], v[144:147], a[88:91], v[12:15]\n\t"
    "v_fma_f32 v43, v43, s72, v242\n\t"
    "v_exp_f32 v42, v42\n\t"
    "v_cvt_pk_bf16_f32 v40, v40, v41\n\t"
    "v_accvgpr_write_b32 a184, v40\n\t"
    "v_exp_f32 v43, v43\n\t"
    "v_mfma_f32_16x16x32_bf16 v[12:15], v[148:151], a[92:95], v[12:15]\n\t"
    "s_waitcnt lgkmcnt(5)\n\t"
    "v_fma_f32 v44, v44, s72, v243\n\t"
    "v_fma_f32 v45, v45, s72, v243\n\t"
    "v_exp_f32 v44, v44\n\t"
    "v_mfma_f32_16x16x32_bf16 v[252:255], v[56:59], a[168:171], v[252:255]\n\t"
    "v_cvt_pk_bf16_f32 v42, v42, v43\n\t"
    "v_accvgpr_write_b32 a185, v42\n\t"
    "v_mfma_f32_16x16x32_bf16 v[8:11], v[152:155], a[112:115], v[8:11]\n\t"
    "v_exp_f32 v45, v45\n\t"
    "v_mfma_f32_16x16x32_bf16 v[8:11], v[156:159], a[116:119], v[8:11]\n\t"
    "v_fma_f32 v46, v46, s72, v243\n\t"
    "v_fma_f32 v47, v47, s72, v243\n\t"
    "v_mfma_f32_16x16x32_bf16 v[12:15], v[152:155], a[120:123], v[12:15]\n\t"
    "v_exp_f32 v46, v46\n\t"
    "v_mfma_f32_16x16x32_bf16 v[12:15], v[156:159], a[124:127], v[12:15]\n\t"
    "v_cvt_pk_bf16_f32 v44, v44, v45\n\t"
    "v_accvgpr_write_b32 a188, v44\n\t"
    "v_mfma_f32_16x16x32_bf16 v[252:255], v[60:63], a[172:175], v[252:255]\n\t"
    "v_exp_f32 v47, v47\n\t"
    "s_waitcnt lgkmcnt(1)\n\t"
    "s_nop 7\n\t"
    "s_branch 2f\n\t"
    "1:\n\t"
    "v_mfma_f32_16x16x32_bf16 v[16:19], a[224:227], a[0:3], 0\n\t"
    "v_cvt_pk_bf16_f32 v41, v42, v43\n\t"
    "v_mul_f32 v44, v255, v44\n\t"
    "v_mfma_f32_16x16x32_bf16 v[16:19], a[228:231], a[4:7], v[16:19]\n\t"
    "s_mov_b32 s46, s80\n\t"
    "s_mov_b32 s80, 0\n\t"
    "v_mul_f32 v45, v255, v45\n\t"
    "v_mfma_f32_16x16x32_bf16 v[16:19], a[232:235], a[32:35], v[16:19]\n\t"
    "s_waitcnt lgkmcnt(2)\n\t"
    "ds_read_b128 a[244:247], v225 offset:8192\n\t"
    "v_mfma_f32_16x16x32_bf16 v[16:19], a[236:239], a[36:39], v[16:19]\n\t"
    "ds_write_b64 v235, v[40:41] offset:17408\n\t"
    "v_mfma_f32_16x16x32_bf16 v[20:23], a[236:239], a[44:47], 0\n\t"
    "v_mul_f32 v46, v255, v46\n\t"
    "v_mul_f32 v47, v255, v47\n\t"
    "v_mfma_f32_16x16x32_bf16 v[20:23], a[232:235], a[40:43], v[20:23]\n\t"
    "v_cvt_pk_bf16_f32 v44, v44, v45\n\t"
    "v_cvt_pk_bf16_f32 v45, v46, v47\n\t"
    "v_mfma_f32_16x16x32_bf16 v[20:23], a[228:231], a[12:15], v[20:23]\n\t"
    "ds_read_b128 a[248:251], v224 offset:12288\n\t"
    "v_mfma_f32_16x16x32_bf16 v[20:23], a[224:227], a[8:11], v[20:23]\n\t"
    "ds_write_b64 v235, v[44:45] offset:17920\n\t"
    "v_mfma_f32_16x16x32_bf16 v[24:27], a[224:227], a[16:19], 0\n\t"
    "s_xor_b32 s71, s64, 0x10000\n\t"
    "v_mul_f32 v8, v254, v8\n\t"
    "v_mul_f32 v9, v254, v9\n\t"
    "v_mfma_f32_16x16x32_bf16 v[24:27], a[228:231], a[20:23], v[24:27]\n\t"
    "s_add_u32 m0, s70, 0x10000\n\t"
    "v_mul_f32 v10, v254, v10\n\t"
    "v_mul_f32 v11, v254, v11\n\t"
    "v_mfma_f32_16x16x32_bf16 v[24:27], a[232:235], a[48:51], v[24:27]\n\t"
    "ds_read_b128 a[252:255], v225 offset:12288\n\t"
    "v_cvt_pk_bf16_f32 v8, v8, v9\n\t"
    "v_mfma_f32_16x16x32_bf16 v[24:27], a[236:239], a[52:55], v[24:27]\n\t"
    "buffer_load_dwordx4 v232, s[40:43], s71 offen offset:0 nt lds\n\t"
    "v_mfma_f32_16x16x32_bf16 v[28:31], a[236:239], a[60:63], 0\n\t"
    "v_cvt_pk_bf16_f32 v9, v10, v11\n\t"
    "v_mul_f32 v12, v255, v12\n\t"
    "v_mfma_f32_16x16x32_bf16 v[28:31], a[232:235], a[56:59], v[28:31]\n\t"
    "buffer_store_dwordx4 v[248:251], v237, s[56:59], s83 offen offset:3072 sc1\n\t"
    "v_mul_f32 v13, v255, v13\n\t"
    "v_mfma_f32_16x16x32_bf16 v[28:31], a[228:231], a[28:31], v[28:31]\n\t"
    "s_waitcnt lgkmcnt(2)\n\t"
    "ds_read_b128 a[236:239], v224 offset:2048\n\t"
    "v_mfma_f32_16x16x32_bf16 v[28:31], a[224:227], a[24:27], v[28:31]\n\t"
    "ds_write_b64 v235, v[8:9] offset:1024\n\t"
    "v_mfma_f32_16x16x32_bf16 v[16:19], a[240:243], a[64:67], v[16:19]\n\t"
    "v_mul_f32 v14, v255, v14\n\t"
    "v_mul_f32 v15, v255, v15\n\t"
    "v_mfma_f32_16x16x32_bf16 v[16:19], a[244:247], a[68:71], v[16:19]\n\t"
    "v_cvt_pk_bf16_f32 v12, v12, v13\n\t"
    "v_cvt_pk_bf16_f32 v13, v14, v15\n\t"
    "v_mfma_f32_16x16x32_bf16 v[16:19], a[248:251], a[96:99], v[16:19]\n\t"
    "s_waitcnt lgkmcnt(2)\n\t"
    "ds_read_b128 a[232:235], v225 offset:2048\n\t"
    "v_mfma_f32_16x16x32_bf16 v[16:19], a[252:255], a[100:103], v[16:19]\n\t"
    "ds_write_b64 v235, v[12:13] offset:1536\n\t"
    "v_mfma_f32_16x16x32_bf16 v[20:23], a[252:255], a[108:111], v[20:23]\n\t"
    "v_mul_f32 v48, v252, v48\n\t"
    "v_mul_f32 v49, v252, v49\n\t"
    "v_mfma_f32_16x16x32_bf16 v[20:23], a[248:251], a[104:107], v[20:23]\n\t"
    "v_mul_f32 v50, v252, v50\n\t"
    "v_mul_f32 v51, v252, v51\n\t"
    "v_mfma_f32_16x16x32_bf16 v[20:23], a[244:247], a[76:79], v[20:23]\n\t"
    "ds_read_b128 a[228:231], v224 offset:6144\n\t"
    "v_cvt_pk_bf16_f32 v48, v48, v49\n\t"
    "v_mfma_f32_16x16x32_bf16 v[20:23], a[240:243], a[72:75], v[20:23]\n\t"
    "v_cvt_pk_bf16_f32 v49, v50, v51\n\t"
    "v_mul_f32 v52, v253, v52\n\t"
    "v_mfma_f32_16x16x32_bf16 v[24:27], a[240:243], a[80:83], v[24:27]\n\t"
    "v_mul_f32 v53, v253, v53\n\t"
    "v_mfma_f32_16x16x32_bf16 v[24:27], a[244:247], a[84:87], v[24:27]\n\t"
    "ds_write_b64 v235, v[48:49] offset:16512\n\t"
    "v_mfma_f32_16x16x32_bf16 v[24:27], a[248:251], a[112:115], v[24:27]\n\t"
    "ds_read_b128 a[224:227], v225 offset:6144\n\t"
    "v_mul_f32 v54, v253, v54\n\t"
    "v_mfma_f32_16x16x32_bf16 v[24:27], a[252:255], a[116:119], v[24:27]\n\t"
    "buffer_load_dwordx4 v233, s[40:43], s71 offen offset:1024 nt lds\n\t"
    "v_mfma_f32_16x16x32_bf16 v[28:31], a[252:255], a[124:127], v[28:31]\n\t"
    "v_mul_f32 v55, v253, v55\n\t"
    "v_cvt_pk_bf16_f32 v52, v52, v53\n\t"
    "v_max3_f32 v240, v16, v17, v18\n\t"
    "v_max3_f32 v241, v20, v21, v22\n\t"
    "v_mfma_f32_16x16x32_bf16 v[28:31], a[248:251], a[120:123], v[28:31]\n\t"
    "v_cvt_pk_bf16_f32 v53, v54, v55\n\t"
    "v_mul_f32 v56, v254, v56\n\t"
    "v_mfma_f32_16x16x32_bf16 v[28:31], a[244:247], a[92:95], v[28:31]\n\t"
    "ds_read_b128 a[252:255], v224 offset:10240\n\t"
    "v_mul_f32 v57, v254, v57\n\t"
    "v_mfma_f32_16x16x32_bf16 v[28:31], a[240:243], a[88:91], v[28:31]\n\t"
    "s_waitcnt lgkmcnt(3)\n\t"
    "ds_write_b64 v235, v[52:53] offset:17024\n\t"
    "v_max3_f32 v242, v24, v25, v26\n\t"
    "v_mfma_f32_16x16x32_bf16 v[32:35], a[236:239], a[0:3], 0\n\t"
    "v_mul_f32 v58, v254, v58\n\t"
    "v_mul_f32 v59, v254, v59\n\t"
    "v_mfma_f32_16x16x32_bf16 v[32:35], a[232:235], a[4:7], v[32:35]\n\t"
    "s_waitcnt lgkmcnt(2)\n\t"
    "v_cvt_pk_bf16_f32 v56, v56, v57\n\t"
    "v_cvt_pk_bf16_f32 v57, v58, v59\n\t"
    "v_mfma_f32_16x16x32_bf16 v[32:35], a[228:231], a[32:35], v[32:35]\n\t"
    "ds_read_b128 a[248:251], v225 offset:10240\n\t"
    "v_mul_f32 v60, v255, v60\n\t"
    "v_mfma_f32_16x16x32_bf16 v[32:35], a[224:227], a[36:39], v[32:35]\n\t"
    "ds_read_b128 v[244:247], v236 offset:0\n\t"
    "v_mul_f32 v61, v255, v61\n\t"
    "v_max3_f32 v243, v28, v29, v30\n\t"
    "v_max_f32 v240, v240, v19\n\t"
    "v_max_f32 v241, v241, v23\n\t"
    "v_max_f32 v242, v242, v27\n\t"
    "v_mfma_f32_16x16x32_bf16 v[36:39], a[224:227], a[44:47], 0\n\t"
    "ds_write_b64 v235, v[56:57] offset:17536\n\t"
    "v_max_f32 v243, v243, v31\n\t"
    "v_mov_b32 v208, v240\n\t"
    "v_mov_b32 v209, v241\n\t"
    "v_mov_b32 v210, v242\n\t"
    "v_mfma_f32_16x16x32_bf16 v[36:39], a[228:231], a[40:43], v[36:39]\n\t"
    "v_mul_f32 v62, v255, v62\n\t"
    "v_mul_f32 v63, v255, v63\n\t"
    "v_mov_b32 v211, v243\n\t"
    "v_permlane32_swap_b32 v208, v240\n\t"
    "v_permlane32_swap_b32 v209, v241\n\t"
    "v_permlane32_swap_b32 v210, v242\n\t"
    "v_mfma_f32_16x16x32_bf16 v[36:39], a[232:235], a[12:15], v[36:39]\n\t"
    "ds_read_b128 a[244:247], v224 offset:14336\n\t"
    "v_cvt_pk_bf16_f32 v60, v60, v61\n\t"
    "v_permlane32_swap_b32 v211, v243\n\t"
    "v_max_f32 v240, v240, v208\n\t"
    "v_max_f32 v241, v241, v209\n\t"
    "v_max_f32 v242, v242, v210\n\t"
    "v_mfma_f32_16x16x32_bf16 v[36:39], a[236:239], a[8:11], v[36:39]\n\t"
    "s_xor_b32 s83, s68, 0x180\n\t"
    "v_cvt_pk_bf16_f32 v61, v62, v63\n\t"
    "v_max_f32 v243, v243, v211\n\t"
    "v_mov_b32 v208, v240\n\t"
    "v_mov_b32 v209, v241\n\t"
    "v_mov_b32 v210, v242\n\t"
    "v_mfma_f32_16x16x32_bf16 v[40:43], a[236:239], a[16:19], 0\n\t"
    "s_waitcnt lgkmcnt(2)\n\t"
    "ds_write_b64 v235, v[60:61] offset:18048\n\t"
    "v_mov_b32 v211, v243\n\t"
    "v_permlane16_swap_b32 v208, v240\n\t"
    "v_permlane16_swap_b32 v209, v241\n\t"
    "v_permlane16_swap_b32 v210, v242\n\t"
    "v_mfma_f32_16x16x32_bf16 v[40:43], a[232:235], a[20:23], v[40:43]\n\t"
    "buffer_store_dwordx4 v[244:247], v237, s[56:59], s83 offen offset:0 sc1\n\t"
    "v_permlane16_swap_b32 v211, v243\n\t"
    "v_max_f32 v240, v240, v208\n\t"
    "v_max_f32 v241, v241, v209\n\t"
    "v_max_f32 v242, v242, v210\n\t"
    "v_mfma_f32_16x16x32_bf16 v[40:43], a[228:231], a[48:51], v[40:43]\n\t"
    "ds_read_b128 a[240:243], v225 offset:14336\n\t"
    "v_max_f32 v243, v243, v211\n\t"
    "v_mul_f32 v240, s79, v240\n\t"
    "v_mul_f32 v241, s79, v241\n\t"
    "v_mul_f32 v242, s79, v242\n\t"
    "v_mfma_f32_16x16x32_bf16 v[40:43], a[224:227], a[52:55], v[40:43]\n\t"
    "s_mov_b32 vcc_lo, 0x11111111\n\t"
    "v_mul_f32 v243, s79, v243\n\t"
    "v_fma_f32 v16, v16, s72, v240\n\t"
    "v_fma_f32 v17, v17, s72, v240\n\t"
    "v_exp_f32 v16, v16\n\t"
    "v_mfma_f32_16x16x32_bf16 v[44:47], a[224:227], a[60:63], 0\n\t"
    "buffer_load_dwordx4 v232, s[40:43], s71 offen offset:2048 nt lds\n\t"
    "v_exp_f32 v17, v17\n\t"
    "v_fma_f32 v18, v18, s72, v240\n\t"
    "v_fma_f32 v19, v19, s72, v240\n\t"
    "v_exp_f32 v18, v18\n\t"
    "v_mfma_f32_16x16x32_bf16 v[44:47], a[228:231], a[56:59], v[44:47]\n\t"
    "s_mov_b32 vcc_hi, 0x11111111\n\t"
    "v_cvt_pk_bf16_f32 v64, v16, v17\n\t"
    "v_exp_f32 v19, v19\n\t"
    "v_fma_f32 v20, v20, s72, v241\n\t"
    "v_fma_f32 v21, v21, s72, v241\n\t"
    "v_mov_b32 v63, 0x3f803f80\n\t"
    "v_mfma_f32_16x16x32_bf16 v[44:47], a[232:235], a[28:31], v[44:47]\n\t"
    "v_exp_f32 v20, v20\n\t"
    "v_cvt_pk_bf16_f32 v65, v18, v19\n\t"
    "v_exp_f32 v21, v21\n\t"
    "v_fma_f32 v22, v22, s72, v241\n\t"
    "v_cndmask_b32 v48, 0, v63, vcc\n\t"
    "v_mfma_f32_16x16x32_bf16 v[44:47], a[236:239], a[24:27], v[44:47]\n\t"
    "s_waitcnt lgkmcnt(2)\n\t"
    "v_fma_f32 v23, v23, s72, v241\n\t"
    "v_exp_f32 v22, v22\n\t"
    "v_cvt_pk_bf16_f32 v68, v20, v21\n\t"
    "v_exp_f32 v23, v23\n\t"
    "v_mfma_f32_16x16x32_bf16 v[32:35], a[252:255], a[64:67], v[32:35]\n\t"
    "s_lshl_b64 vcc, vcc, 1\n\t"
    "v_fma_f32 v24, v24, s72, v242\n\t"
    "v_fma_f32 v25, v25, s72, v242\n\t"
    "v_mov_b32 v49, v48\n\t"
    "v_mov_b32 v50, v48\n\t"
    "v_mov_b32 v51, v48\n\t"
    "v_mfma_f32_16x16x32_bf16 v[32:35], a[248:251], a[68:71], v[32:35]\n\t"
    "s_waitcnt lgkmcnt(0)\n\t"
    "v_exp_f32 v24, v24\n\t"
    "v_cvt_pk_bf16_f32 v69, v22, v23\n\t"
    "v_cndmask_b32 v52, 0, v63, vcc\n\t"
    "v_mfma_f32_16x16x32_bf16 v[32:35], a[244:247], a[96:99], v[32:35]\n\t"
    "s_waitcnt vmcnt(17)\n\t"
    "v_exp_f32 v25, v25\n\t"
    "v_fma_f32 v26, v26, s72, v242\n\t"
    "v_mfma_f32_16x16x32_bf16 v[32:35], a[240:243], a[100:103], v[32:35]\n\t"
    "s_barrier\n\t"
    "v_mfma_f32_16x16x32_bf16 v[36:39], a[240:243], a[108:111], v[36:39]\n\t"
    "ds_read_b128 a[224:227], v224 offset:16384\n\t"
    "v_fma_f32 v27, v27, s72, v242\n\t"
    "v_mfma_f32_16x16x32_bf16 v[36:39], a[244:247], a[104:107], v[36:39]\n\t"
    "ds_read_b128 a[228:231], v225 offset:16384\n\t"
    "v_exp_f32 v26, v26\n\t"
    "v_mfma_f32_16x16x32_bf16 v[36:39], a[248:251], a[76:79], v[36:39]\n\t"
    "ds_read_b128 a[232:235], v224 offset:20480\n\t"
    "v_cvt_pk_bf16_f32 v72, v24, v25\n\t"
    "v_mfma_f32_16x16x32_bf16 v[36:39], a[252:255], a[72:75], v[36:39]\n\t"
    "s_lshl_b64 vcc, vcc, 1\n\t"
    "v_exp_f32 v27, v27\n\t"
    "v_mov_b32 v53, v52\n\t"
    "v_mov_b32 v54, v52\n\t"
    "v_mov_b32 v55, v52\n\t"
    "v_mfma_f32_16x16x32_bf16 v[40:43], a[252:255], a[80:83], v[40:43]\n\t"
    "v_fma_f32 v28, v28, s72, v243\n\t"
    "v_fma_f32 v29, v29, s72, v243\n\t"
    "v_cndmask_b32 v56, 0, v63, vcc\n\t"
    "v_mfma_f32_16x16x32_bf16 v[40:43], a[248:251], a[84:87], v[40:43]\n\t"
    "s_lshl_b64 vcc, vcc, 1\n\t"
    "v_exp_f32 v28, v28\n\t"
    "v_mov_b32 v57, v56\n\t"
    "v_mov_b32 v58, v56\n\t"
    "v_mov_b32 v59, v56\n\t"
    "v_mfma_f32_16x16x32_bf16 v[40:43], a[244:247], a[112:115], v[40:43]\n\t"
    "ds_read_b128 a[236:239], v225 offset:20480\n\t"
    "v_cvt_pk_bf16_f32 v73, v26, v27\n\t"
    "v_cndmask_b32 v60, 0, v63, vcc\n\t"
    "v_mov_b32 v61, v60\n\t"
    "v_mov_b32 v62, v60\n\t"
    "v_mov_b32 v63, v60\n\t"
    "v_mfma_f32_16x16x32_bf16 v[40:43], a[240:243], a[116:119], v[40:43]\n\t"
    "v_exp_f32 v29, v29\n\t"
    "v_mfma_f32_16x16x32_bf16 v[44:47], a[240:243], a[124:127], v[44:47]\n\t"
    "v_fma_f32 v30, v30, s72, v243\n\t"
    "v_fma_f32 v31, v31, s72, v243\n\t"
    "v_mfma_f32_16x16x32_bf16 v[44:47], a[244:247], a[120:123], v[44:47]\n\t"
    "buffer_load_dwordx4 v233, s[40:43], s71 offen offset:3072 nt lds\n\t"
    "v_mfma_f32_16x16x32_bf16 v[44:47], a[248:251], a[92:95], v[44:47]\n\t"
    "ds_read_b128 a[240:243], v224 offset:24576\n\t"
    "ds_read_b128 v[248:251], v236 offset:256\n\t"
    "v_mfma_f32_16x16x32_bf16 v[44:47], a[252:255], a[88:91], v[44:47]\n\t"
    "s_waitcnt lgkmcnt(3)\n\t"
    "v_exp_f32 v30, v30\n\t"
    "v_cvt_pk_bf16_f32 v76, v28, v29\n\t"
    "v_mfma_f32_16x16x32_bf16 v[0:3], a[224:227], a[0:3], 0\n\t"
    "v_exp_f32 v31, v31\n\t"
    "v_mfma_f32_16x16x32_bf16 v[0:3], a[228:231], a[4:7], v[0:3]\n\t"
    "v_cvt_pk_bf16_f32 v77, v30, v31\n\t"
    "v_fma_f32 v32, v32, s72, v240\n\t"
    "v_fma_f32 v33, v33, s72, v240\n\t"
    "v_exp_f32 v32, v32\n\t"
    "v_mfma_f32_16x16x32_bf16 v[0:3], a[232:235], a[32:35], v[0:3]\n\t"
    "s_waitcnt lgkmcnt(2)\n\t"
    "ds_read_b128 a[244:247], v225 offset:24576\n\t"
    "v_exp_f32 v33, v33\n\t"
    "v_mfma_f32_16x16x32_bf16 v[0:3], a[236:239], a[36:39], v[0:3]\n\t"
    "s_waitcnt lgkmcnt(1)\n\t"
    "v_fma_f32 v34, v34, s72, v240\n\t"
    "v_fma_f32 v35, v35, s72, v240\n\t"
    "v_mfma_f32_16x16x32_bf16 v[4:7], a[236:239], a[44:47], 0\n\t"
    "buffer_store_dwordx4 v[248:251], v237, s[56:59], s83 offen offset:1024 sc1\n\t"
    "v_mfma_f32_16x16x32_bf16 v[4:7], a[232:235], a[40:43], v[4:7]\n\t"
    "v_exp_f32 v34, v34\n\t"
    "v_cvt_pk_bf16_f32 v66, v32, v33\n\t"
    "v_mfma_f32_16x16x32_bf16 v[4:7], a[228:231], a[12:15], v[4:7]\n\t"
    "ds_read_b128 a[248:251], v224 offset:28672\n\t"
    "v_exp_f32 v35, v35\n\t"
    "v_mfma_f32_16x16x32_bf16 v[4:7], a[224:227], a[8:11], v[4:7]\n\t"
    "v_fma_f32 v36, v36, s72, v241\n\t"
    "v_fma_f32 v37, v37, s72, v241\n\t"
    "v_mfma_f32_16x16x32_bf16 v[8:11], a[224:227], a[16:19], 0\n\t"
    "v_exp_f32 v36, v36\n\t"
    "v_cvt_pk_bf16_f32 v67, v34, v35\n\t"
    "v_mfma_f32_16x16x32_bf16 v[8:11], a[228:231], a[20:23], v[8:11]\n\t"
    "v_exp_f32 v37, v37\n\t"
    "v_fma_f32 v38, v38, s72, v241\n\t"
    "v_mfma_f32_16x16x32_bf16 v[8:11], a[232:235], a[48:51], v[8:11]\n\t"
    "ds_read_b128 a[252:255], v225 offset:28672\n\t"
    "v_fma_f32 v39, v39, s72, v241\n\t"
    "v_mfma_f32_16x16x32_bf16 v[8:11], a[236:239], a[52:55], v[8:11]\n\t"
    "v_exp_f32 v38, v38\n\t"
    "v_mfma_f32_16x16x32_bf16 v[12:15], a[236:239], a[60:63], 0\n\t"
    "s_xor_b32 s71, s64, 0x14000\n\t"
    "s_add_u32 m0, s70, 0x14000\n\t"
    "v_mfma_f32_16x16x32_bf16 v[12:15], a[232:235], a[56:59], v[12:15]\n\t"
    "buffer_load_dwordx4 v232, s[40:43], s71 offen offset:0 nt lds\n\t"
    "v_mfma_f32_16x16x32_bf16 v[12:15], a[228:231], a[28:31], v[12:15]\n\t"
    "ds_read_b128 a[236:239], v224 offset:18432\n\t"
    "v_cvt_pk_bf16_f32 v70, v36, v37\n\t"
    "v_mfma_f32_16x16x32_bf16 v[12:15], a[224:227], a[24:27], v[12:15]\n\t"
    "s_waitcnt lgkmcnt(2)\n\t"
    "v_exp_f32 v39, v39\n\t"
    "v_mfma_f32_16x16x32_bf16 v[0:3], a[240:243], a[64:67], v[0:3]\n\t"
    "v_fma_f32 v40, v40, s72, v242\n\t"
    "v_fma_f32 v41, v41, s72, v242\n\t"
    "v_mfma_f32_16x16x32_bf16 v[0:3], a[244:247], a[68:71], v[0:3]\n\t"
    "v_exp_f32 v40, v40\n\t"
    "v_cvt_pk_bf16_f32 v71, v38, v39\n\t"
    "v_mfma_f32_16x16x32_bf16 v[0:3], a[248:251], a[96:99], v[0:3]\n\t"
    "s_waitcnt lgkmcnt(1)\n\t"
    "ds_read_b128 a[232:235], v225 offset:18432\n\t"
    "v_mfma_f32_16x16x32_bf16 v[0:3], a[252:255], a[100:103], v[0:3]\n\t"
    "v_exp_f32 v41, v41\n\t"
    "v_mfma_f32_16x16x32_bf16 v[4:7], a[252:255], a[108:111], v[4:7]\n\t"
    "v_fma_f32 v42, v42, s72, v242\n\t"
    "v_fma_f32 v43, v43, s72, v242\n\t"
    "v_mfma_f32_16x16x32_bf16 v[4:7], a[248:251], a[104:107], v[4:7]\n\t"
    "v_exp_f32 v42, v42\n\t"
    "v_cvt_pk_bf16_f32 v74, v40, v41\n\t"
    "v_mfma_f32_16x16x32_bf16 v[4:7], a[244:247], a[76:79], v[4:7]\n\t"
    "ds_read_b128 a[228:231], v224 offset:22528\n\t"
    "v_exp_f32 v43, v43\n\t"
    "v_mfma_f32_16x16x32_bf16 v[4:7], a[240:243], a[72:75], v[4:7]\n\t"
    "v_fma_f32 v44, v44, s72, v243\n\t"
    "v_fma_f32 v45, v45, s72, v243\n\t"
    "v_mfma_f32_16x16x32_bf16 v[8:11], a[240:243], a[80:83], v[8:11]\n\t"
    "v_exp_f32 v44, v44\n\t"
    "v_mfma_f32_16x16x32_bf16 v[8:11], a[244:247], a[84:87], v[8:11]\n\t"
    "ds_read_b128 v[244:247], v236 offset:512\n\t"
    "v_cvt_pk_bf16_f32 v75, v42, v43\n\t"
    "v_mfma_f32_16x16x32_bf16 v[8:11], a[248:251], a[112:115], v[8:11]\n\t"
    "ds_read_b128 a[224:227], v225 offset:22528\n\t"
    "v_exp_f32 v45, v45\n\t"
    "v_mfma_f32_16x16x32_bf16 v[8:11], a[252:255], a[116:119], v[8:11]\n\t"
    "v_fma_f32 v46, v46, s72, v243\n\t"
    "v_fma_f32 v47, v47, s72, v243\n\t"
    "v_mfma_f32_16x16x32_bf16 v[12:15], a[252:255], a[124:127], v[12:15]\n\t"
    "v_exp_f32 v46, v46\n\t"
    "v_cvt_pk_bf16_f32 v78, v44, v45\n\t"
    "v_mfma_f32_16x16x32_bf16 v[12:15], a[248:251], a[120:123], v[12:15]\n\t"
    "v_exp_f32 v47, v47\n\t"
    "v_mfma_f32_16x16x32_bf16 v[12:15], a[244:247], a[92:95], v[12:15]\n\t"
    "s_waitcnt lgkmcnt(2)\n\t"
    "ds_read_b128 a[252:255], v224 offset:26624\n\t"
    "v_mfma_f32_16x16x32_bf16 v[12:15], a[240:243], a[88:91], v[12:15]\n\t"
    "buffer_load_dwordx4 v233, s[40:43], s71 offen offset:1024 nt lds\n\t"
    "v_mfma_f32_16x16x32_bf16 v[16:19], a[236:239], a[0:3], 0\n\t"
    "v_fma_f32 v0, v0, s72, v240\n\t"
    "v_fma_f32 v1, v1, s72, v240\n\t"
    "v_mfma_f32_16x16x32_bf16 v[16:19], a[232:235], a[4:7], v[16:19]\n\t"
    "s_waitcnt lgkmcnt(2)\n\t"
    "v_exp_f32 v0, v0\n\t"
    "v_mfma_f32_16x16x32_bf16 v[16:19], a[228:231], a[32:35], v[16:19]\n\t"
    "s_waitcnt lgkmcnt(1)\n\t"
    "ds_read_b128 a[248:251], v225 offset:26624\n\t"
    "buffer_store_dwordx4 v[244:247], v237, s[56:59], s83 offen offset:2048 sc1\n\t"
    "v_mfma_f32_16x16x32_bf16 v[16:19], a[224:227], a[36:39], v[16:19]\n\t"
    "v_exp_f32 v1, v1\n\t"
    "v_mfma_f32_16x16x32_bf16 v[20:23], a[224:227], a[44:47], 0\n\t"
    "v_fma_f32 v2, v2, s72, v240\n\t"
    "v_fma_f32 v3, v3, s72, v240\n\t"
    "v_mfma_f32_16x16x32_bf16 v[20:23], a[228:231], a[40:43], v[20:23]\n\t"
    "v_exp_f32 v2, v2\n\t"
    "v_mfma_f32_16x16x32_bf16 v[20:23], a[232:235], a[12:15], v[20:23]\n\t"
    "ds_read_b128 a[244:247], v224 offset:30720\n\t"
    "v_cvt_pk_bf16_f32 v80, v0, v1\n\t"
    "v_mfma_f32_16x16x32_bf16 v[20:23], a[236:239], a[8:11], v[20:23]\n\t"
    "v_exp_f32 v3, v3\n\t"
    "v_mfma_f32_16x16x32_bf16 v[24:27], a[236:239], a[16:19], 0\n\t"
    "v_fma_f32 v4, v4, s72, v241\n\t"
    "v_fma_f32 v5, v5, s72, v241\n\t"
    "v_mfma_f32_16x16x32_bf16 v[24:27], a[232:235], a[20:23], v[24:27]\n\t"
    "v_exp_f32 v4, v4\n\t"
    "v_mfma_f32_16x16x32_bf16 v[24:27], a[228:231], a[48:51], v[24:27]\n\t"
    "ds_read_b128 a[240:243], v225 offset:30720\n\t"
    "v_cvt_pk_bf16_f32 v81, v2, v3\n\t"
    "v_mfma_f32_16x16x32_bf16 v[24:27], a[224:227], a[52:55], v[24:27]\n\t"
    "v_exp_f32 v5, v5\n\t"
    "v_mfma_f32_16x16x32_bf16 v[28:31], a[224:227], a[60:63], 0\n\t"
    "v_fma_f32 v6, v6, s72, v241\n\t"
    "v_fma_f32 v7, v7, s72, v241\n\t"
    "v_mfma_f32_16x16x32_bf16 v[28:31], a[228:231], a[56:59], v[28:31]\n\t"
    "v_exp_f32 v6, v6\n\t"
    "v_mfma_f32_16x16x32_bf16 v[28:31], a[232:235], a[28:31], v[28:31]\n\t"
    "buffer_load_dwordx4 v232, s[40:43], s71 offen offset:2048 nt lds\n\t"
    "v_cvt_pk_bf16_f32 v84, v4, v5\n\t"
    "v_mfma_f32_16x16x32_bf16 v[28:31], a[236:239], a[24:27], v[28:31]\n\t"
    "s_waitcnt lgkmcnt(1)\n\t"
    "v_exp_f32 v7, v7\n\t"
    "v_mfma_f32_16x16x32_bf16 v[16:19], a[252:255], a[64:67], v[16:19]\n\t"
    "v_fma_f32 v8, v8, s72, v242\n\t"
    "v_fma_f32 v9, v9, s72, v242\n\t"
    "v_mfma_f32_16x16x32_bf16 v[16:19], a[248:251], a[68:71], v[16:19]\n\t"
    "s_waitcnt lgkmcnt(0)\n\t"
    "v_exp_f32 v8, v8\n\t"
    "v_mfma_f32_16x16x32_bf16 v[16:19], a[244:247], a[96:99], v[16:19]\n\t"
    "s_waitcnt vmcnt(17)\n\t"
    "v_cvt_pk_bf16_f32 v85, v6, v7\n\t"
    "v_cvt_pk_bf16_f32 v79, v46, v47\n\t"
    "v_mfma_f32_16x16x32_bf16 v[16:19], a[240:243], a[100:103], v[16:19]\n\t"
    "s_barrier\n\t"
    "v_exp_f32 v9, v9\n\t"
    "v_mfma_f32_16x16x32_bf16 v[20:23], a[240:243], a[108:111], v[20:23]\n\t"
    "ds_read_b128 a[224:227], v224 offset:32768\n\t"
    "ds_read_b128 v[248:251], v236 offset:768\n\t"
    "v_fma_f32 v10, v10, s72, v242\n\t"
    "v_fma_f32 v11, v11, s72, v242\n\t"
    "v_mfma_f32_16x16x32_bf16 v[20:23], a[244:247], a[104:107], v[20:23]\n\t"
    "ds_read_b128 a[228:231], v225 offset:32768\n\t"
    "v_exp_f32 v10, v10\n\t"
    "v_mfma_f32_16x16x32_bf16 v[20:23], a[248:251], a[76:79], v[20:23]\n\t"
    "ds_read_b128 a[232:235], v224 offset:36864\n\t"
    "v_cvt_pk_bf16_f32 v88, v8, v9\n\t"
    "v_mfma_f32_16x16x32_bf16 v[20:23], a[252:255], a[72:75], v[20:23]\n\t"
    "v_exp_f32 v11, v11\n\t"
    "v_mfma_f32_16x16x32_bf16 v[24:27], a[252:255], a[80:83], v[24:27]\n\t"
    "v_fma_f32 v12, v12, s72, v243\n\t"
    "v_fma_f32 v13, v13, s72, v243\n\t"
    "v_mfma_f32_16x16x32_bf16 v[24:27], a[248:251], a[84:87], v[24:27]\n\t"
    "s_waitcnt lgkmcnt(2)\n\t"
    "v_exp_f32 v12, v12\n\t"
    "v_mfma_f32_16x16x32_bf16 v[24:27], a[244:247], a[112:115], v[24:27]\n\t"
    "ds_read_b128 a[236:239], v225 offset:36864\n\t"
    "buffer_store_dwordx4 v[248:251], v237, s[56:59], s83 offen offset:3072 sc1\n\t"
    "v_cvt_pk_bf16_f32 v89, v10, v11\n\t"
    "v_mfma_f32_16x16x32_bf16 v[24:27], a[240:243], a[116:119], v[24:27]\n\t"
    "v_exp_f32 v13, v13\n\t"
    "v_mfma_f32_16x16x32_bf16 v[28:31], a[240:243], a[124:127], v[28:31]\n\t"
    "v_fma_f32 v14, v14, s72, v243\n\t"
    "v_fma_f32 v15, v15, s72, v243\n\t"
    "v_mfma_f32_16x16x32_bf16 v[28:31], a[244:247], a[120:123], v[28:31]\n\t"
    "v_exp_f32 v14, v14\n\t"
    "v_mfma_f32_16x16x32_bf16 v[28:31], a[248:251], a[92:95], v[28:31]\n\t"
    "ds_read_b128 a[240:243], v224 offset:40960\n\t"
    "v_cvt_pk_bf16_f32 v92, v12, v13\n\t"
    "v_mfma_f32_16x16x32_bf16 v[28:31], a[252:255], a[88:91], v[28:31]\n\t"
    "s_waitcnt lgkmcnt(2)\n\t"
    "v_exp_f32 v15, v15\n\t"
    "v_mfma_f32_16x16x32_bf16 v[32:35], a[224:227], a[0:3], 0\n\t"
    "buffer_load_dwordx4 v233, s[40:43], s71 offen offset:3072 nt lds\n\t"
    "v_fma_f32 v16, v16, s72, v240\n\t"
    "v_fma_f32 v17, v17, s72, v240\n\t"
    "v_mfma_f32_16x16x32_bf16 v[32:35], a[228:231], a[4:7], v[32:35]\n\t"
    "s_waitcnt lgkmcnt(1)\n\t"
    "v_exp_f32 v16, v16\n\t"
    "v_mfma_f32_16x16x32_bf16 v[32:35], a[232:235], a[32:35], v[32:35]\n\t"
    "ds_read_b128 a[244:247], v225 offset:40960\n\t"
    "v_cvt_pk_bf16_f32 v93, v14, v15\n\t"
    "v_mfma_f32_16x16x32_bf16 v[32:35], a[236:239], a[36:39], v[32:35]\n\t"
    "v_exp_f32 v17, v17\n\t"
    "v_mfma_f32_16x16x32_bf16 v[36:39], a[236:239], a[44:47], 0\n\t"
    "v_fma_f32 v18, v18, s72, v240\n\t"
    "v_fma_f32 v19, v19, s72, v240\n\t"
    "v_mfma_f32_16x16x32_bf16 v[36:39], a[232:235], a[40:43], v[36:39]\n\t"
    "v_exp_f32 v18, v18\n\t"
    "v_mfma_f32_16x16x32_bf16 v[36:39], a[228:231], a[12:15], v[36:39]\n\t"
    "ds_read_b128 a[248:251], v224 offset:45056\n\t"
    "v_cvt_pk_bf16_f32 v82, v16, v17\n\t"
    "v_mfma_f32_16x16x32_bf16 v[36:39], a[224:227], a[8:11], v[36:39]\n\t"
    "v_exp_f32 v19, v19\n\t"
    "v_mfma_f32_16x16x32_bf16 v[40:43], a[224:227], a[16:19], 0\n\t"
    "v_fma_f32 v20, v20, s72, v241\n\t"
    "v_fma_f32 v21, v21, s72, v241\n\t"
    "v_mfma_f32_16x16x32_bf16 v[40:43], a[228:231], a[20:23], v[40:43]\n\t"
    "v_exp_f32 v20, v20\n\t"
    "v_mfma_f32_16x16x32_bf16 v[40:43], a[232:235], a[48:51], v[40:43]\n\t"
    "ds_read_b128 a[252:255], v225 offset:45056\n\t"
    "v_cvt_pk_bf16_f32 v83, v18, v19\n\t"
    "v_mfma_f32_16x16x32_bf16 v[40:43], a[236:239], a[52:55], v[40:43]\n\t"
    "v_exp_f32 v21, v21\n\t"
    "v_mfma_f32_16x16x32_bf16 v[44:47], a[236:239], a[60:63], 0\n\t"
    "s_xor_b32 s71, s64, 0x18000\n\t"
    "v_fma_f32 v22, v22, s72, v241\n\t"
    "v_fma_f32 v23, v23, s72, v241\n\t"
    "v_mfma_f32_16x16x32_bf16 v[44:47], a[232:235], a[56:59], v[44:47]\n\t"
    "s_add_u32 m0, s70, 0x18000\n\t"
    "v_exp_f32 v22, v22\n\t"
    "v_mfma_f32_16x16x32_bf16 v[44:47], a[228:231], a[28:31], v[44:47]\n\t"
    "ds_read_b128 a[236:239], v224 offset:34816\n\t"
    "v_cvt_pk_bf16_f32 v86, v20, v21\n\t"
    "v_mfma_f32_16x16x32_bf16 v[44:47], a[224:227], a[24:27], v[44:47]\n\t"
    "s_waitcnt lgkmcnt(2)\n\t"
    "v_exp_f32 v23, v23\n\t"
    "v_mfma_f32_16x16x32_bf16 v[252:255], v[48:51], v[80:83], 0\n\t"
    "buffer_load_dwordx4 v232, s[40:43], s71 offen offset:0 nt lds\n\t"
    "v_mfma_f32_16x16x32_bf16 v[32:35], a[240:243], a[64:67], v[32:35]\n\t"
    "ds_read_b128 v[244:247], v236 offset:1024\n\t"
    "v_fma_f32 v24, v24, s72, v242\n\t"
    "v_fma_f32 v25, v25, s72, v242\n\t"
    "v_mfma_f32_16x16x32_bf16 v[32:35], a[244:247], a[68:71], v[32:35]\n\t"
    "s_waitcnt lgkmcnt(2)\n\t"
    "v_exp_f32 v24, v24\n\t"
    "v_mfma_f32_16x16x32_bf16 v[32:35], a[248:251], a[96:99], v[32:35]\n\t"
    "ds_read_b128 a[232:235], v225 offset:34816\n\t"
    "v_cvt_pk_bf16_f32 v87, v22, v23\n\t"
    "v_mfma_f32_16x16x32_bf16 v[32:35], a[252:255], a[100:103], v[32:35]\n\t"
    "v_exp_f32 v25, v25\n\t"
    "v_mfma_f32_16x16x32_bf16 v[36:39], a[252:255], a[108:111], v[36:39]\n\t"
    "s_xor_b32 s83, s68, 0x4180\n\t"
    "v_fma_f32 v26, v26, s72, v242\n\t"
    "v_fma_f32 v27, v27, s72, v242\n\t"
    "v_mfma_f32_16x16x32_bf16 v[36:39], a[248:251], a[104:107], v[36:39]\n\t"
    "s_waitcnt lgkmcnt(1)\n\t"
    "v_exp_f32 v26, v26\n\t"
    "v_mfma_f32_16x16x32_bf16 v[36:39], a[244:247], a[76:79], v[36:39]\n\t"
    "ds_read_b128 a[228:231], v224 offset:38912\n\t"
    "buffer_store_dwordx4 v[244:247], v237, s[56:59], s83 offen offset:0 sc1\n\t"
    "v_cvt_pk_bf16_f32 v90, v24, v25\n\t"
    "v_mfma_f32_16x16x32_bf16 v[36:39], a[240:243], a[72:75], v[36:39]\n\t"
    "v_exp_f32 v27, v27\n\t"
    "v_mfma_f32_16x16x32_bf16 v[40:43], a[240:243], a[80:83], v[40:43]\n\t"
    "v_fma_f32 v28, v28, s72, v243\n\t"
    "v_fma_f32 v29, v29, s72, v243\n\t"
    "v_mfma_f32_16x16x32_bf16 v[40:43], a[244:247], a[84:87], v[40:43]\n\t"
    "v_exp_f32 v28, v28\n\t"
    "v_mfma_f32_16x16x32_bf16 v[40:43], a[248:251], a[112:115], v[40:43]\n\t"
    "ds_read_b128 a[224:227], v225 offset:38912\n\t"
    "v_cvt_pk_bf16_f32 v91, v26, v27\n\t"
    "v_mfma_f32_16x16x32_bf16 v[40:43], a[252:255], a[116:119], v[40:43]\n\t"
    "v_exp_f32 v29, v29\n\t"
    "v_mfma_f32_16x16x32_bf16 v[44:47], a[252:255], a[124:127], v[44:47]\n\t"
    "v_fma_f32 v30, v30, s72, v243\n\t"
    "v_fma_f32 v31, v31, s72, v243\n\t"
    "v_mfma_f32_16x16x32_bf16 v[44:47], a[248:251], a[120:123], v[44:47]\n\t"
    "v_exp_f32 v30, v30\n\t"
    "v_mfma_f32_16x16x32_bf16 v[44:47], a[244:247], a[92:95], v[44:47]\n\t"
    "ds_read_b128 a[252:255], v224 offset:43008\n\t"
    "v_cvt_pk_bf16_f32 v94, v28, v29\n\t"
    "v_mfma_f32_16x16x32_bf16 v[44:47], a[240:243], a[88:91], v[44:47]\n\t"
    "s_waitcnt lgkmcnt(2)\n\t"
    "v_exp_f32 v31, v31\n\t"
    "v_mfma_f32_16x16x32_bf16 v[252:255], v[52:55], v[84:87], v[252:255]\n\t"
    "buffer_load_dwordx4 v233, s[40:43], s71 offen offset:1024 nt lds\n\t"
    "v_mfma_f32_16x16x32_bf16 v[0:3], a[236:239], a[0:3], 0\n\t"
    "v_fma_f32 v32, v32, s72, v240\n\t"
    "v_fma_f32 v33, v33, s72, v240\n\t"
    "v_mfma_f32_16x16x32_bf16 v[0:3], a[232:235], a[4:7], v[0:3]\n\t"
    "s_waitcnt lgkmcnt(1)\n\t"
    "v_exp_f32 v32, v32\n\t"
    "v_mfma_f32_16x16x32_bf16 v[0:3], a[228:231], a[32:35], v[0:3]\n\t"
    "ds_read_b128 a[248:251], v225 offset:43008\n\t"
    "v_cvt_pk_bf16_f32 v95, v30, v31\n\t"
    "v_mfma_f32_16x16x32_bf16 v[0:3], a[224:227], a[36:39], v[0:3]\n\t"
    "v_exp_f32 v33, v33\n\t"
    "v_mfma_f32_16x16x32_bf16 v[4:7], a[224:227], a[44:47], 0\n\t"
    "v_fma_f32 v34, v34, s72, v240\n\t"
    "v_fma_f32 v35, v35, s72, v240\n\t"
    "v_mfma_f32_16x16x32_bf16 v[4:7], a[228:231], a[40:43], v[4:7]\n\t"
    "v_exp_f32 v34, v34\n\t"
    "v_mfma_f32_16x16x32_bf16 v[4:7], a[232:235], a[12:15], v[4:7]\n\t"
    "ds_read_b128 a[244:247], v224 offset:47104\n\t"
    "v_cvt_pk_bf16_f32 v32, v32, v33\n\t"
    "v_accvgpr_write_b32 a128, v32\n\t"
    "v_mfma_f32_16x16x32_bf16 v[4:7], a[236:239], a[8:11], v[4:7]\n\t"
    "v_exp_f32 v35, v35\n\t"
    "v_mfma_f32_16x16x32_bf16 v[252:255], v[48:51], v[64:67], v[252:255]\n\t"
    "ds_read_b128 v[248:251], v236 offset:1280\n\t"
    "v_mfma_f32_16x16x32_bf16 v[8:11], a[236:239], a[16:19], 0\n\t"
    "v_fma_f32 v36, v36, s72, v241\n\t"
    "v_fma_f32 v37, v37, s72, v241\n\t"
    "v_mfma_f32_16x16x32_bf16 v[8:11], a[232:235], a[20:23], v[8:11]\n\t"
    "v_exp_f32 v36, v36\n\t"
    "v_mfma_f32_16x16x32_bf16 v[8:11], a[228:231], a[48:51], v[8:11]\n\t"
    "ds_read_b128 a[240:243], v225 offset:47104\n\t"
    "v_cvt_pk_bf16_f32 v34, v34, v35\n\t"
    "v_accvgpr_write_b32 a129, v34\n\t"
    "v_mfma_f32_16x16x32_bf16 v[8:11], a[224:227], a[52:55], v[8:11]\n\t"
    "v_exp_f32 v37, v37\n\t"
    "v_mfma_f32_16x16x32_bf16 v[12:15], a[224:227], a[60:63], 0\n\t"
    "v_fma_f32 v38, v38, s72, v241\n\t"
    "v_fma_f32 v39, v39, s72, v241\n\t"
    "v_mfma_f32_16x16x32_bf16 v[12:15], a[228:231], a[56:59], v[12:15]\n\t"
    "v_exp_f32 v38, v38\n\t"
    "v_mfma_f32_16x16x32_bf16 v[12:15], a[232:235], a[28:31], v[12:15]\n\t"
    "v_cvt_pk_bf16_f32 v36, v36, v37\n\t"
    "v_accvgpr_write_b32 a132, v36\n\t"
    "v_mfma_f32_16x16x32_bf16 v[12:15], a[236:239], a[24:27], v[12:15]\n\t"
    "s_waitcnt lgkmcnt(1)\n\t"
    "v_exp_f32 v39, v39\n\t"
    "v_mfma_f32_16x16x32_bf16 v[252:255], v[56:59], v[88:91], v[252:255]\n\t"
    "buffer_load_dwordx4 v232, s[40:43], s71 offen offset:2048 nt lds\n\t"
    "v_mfma_f32_16x16x32_bf16 v[0:3], a[252:255], a[64:67], v[0:3]\n\t"
    "buffer_store_dwordx4 v[248:251], v237, s[56:59], s83 offen offset:1024 sc1\n\t"
    "v_fma_f32 v40, v40, s72, v242\n\t"
    "v_fma_f32 v41, v41, s72, v242\n\t"
    "v_mfma_f32_16x16x32_bf16 v[0:3], a[248:251], a[68:71], v[0:3]\n\t"
    "s_waitcnt lgkmcnt(0)\n\t"
    "v_exp_f32 v40, v40\n\t"
    "v_mfma_f32_16x16x32_bf16 v[0:3], a[244:247], a[96:99], v[0:3]\n\t"
    "s_waitcnt vmcnt(18)\n\t"
    "v_cvt_pk_bf16_f32 v38, v38, v39\n\t"
    "v_accvgpr_write_b32 a133, v38\n\t"
    "v_mfma_f32_16x16x32_bf16 v[0:3], a[240:243], a[100:103], v[0:3]\n\t"
    "s_barrier\n\t"
    "v_exp_f32 v41, v41\n\t"
    "v_mfma_f32_16x16x32_bf16 v[4:7], a[240:243], a[108:111], v[4:7]\n\t"
    "ds_read_b128 a[224:227], v224 offset:49152\n\t"
    "v_fma_f32 v42, v42, s72, v242\n\t"
    "v_fma_f32 v43, v43, s72, v242\n\t"
    "v_mfma_f32_16x16x32_bf16 v[4:7], a[244:247], a[104:107], v[4:7]\n\t"
    "ds_read_b128 a[228:231], v225 offset:49152\n\t"
    "v_exp_f32 v42, v42\n\t"
    "v_mfma_f32_16x16x32_bf16 v[4:7], a[248:251], a[76:79], v[4:7]\n\t"
    "ds_read_b128 a[232:235], v224 offset:53248\n\t"
    "v_cvt_pk_bf16_f32 v40, v40, v41\n\t"
    "v_accvgpr_write_b32 a136, v40\n\t"
    "v_mfma_f32_16x16x32_bf16 v[4:7], a[252:255], a[72:75], v[4:7]\n\t"
    "v_exp_f32 v43, v43\n\t"
    "v_mfma_f32_16x16x32_bf16 v[8:11], a[252:255], a[80:83], v[8:11]\n\t"
    "v_fma_f32 v44, v44, s72, v243\n\t"
    "v_fma_f32 v45, v45, s72, v243\n\t"
    "v_mfma_f32_16x16x32_bf16 v[8:11], a[248:251], a[84:87], v[8:11]\n\t"
    "v_exp_f32 v44, v44\n\t"
    "v_mfma_f32_16x16x32_bf16 v[8:11], a[244:247], a[112:115], v[8:11]\n\t"
    "ds_read_b128 a[236:239], v225 offset:53248\n\t"
    "v_cvt_pk_bf16_f32 v42, v42, v43\n\t"
    "v_accvgpr_write_b32 a137, v42\n\t"
    "v_mfma_f32_16x16x32_bf16 v[8:11], a[240:243], a[116:119], v[8:11]\n\t"
    "v_exp_f32 v45, v45\n\t"
    "v_mfma_f32_16x16x32_bf16 v[12:15], a[240:243], a[124:127], v[12:15]\n\t"
    "v_fma_f32 v46, v46, s72, v243\n\t"
    "v_fma_f32 v47, v47, s72, v243\n\t"
    "v_mfma_f32_16x16x32_bf16 v[12:15], a[244:247], a[120:123], v[12:15]\n\t"
    "v_exp_f32 v46, v46\n\t"
    "v_mfma_f32_16x16x32_bf16 v[12:15], a[248:251], a[92:95], v[12:15]\n\t"
    "ds_read_b128 a[240:243], v224 offset:57344\n\t"
    "v_cvt_pk_bf16_f32 v44, v44, v45\n\t"
    "v_accvgpr_write_b32 a140, v44\n\t"
    "v_mfma_f32_16x16x32_bf16 v[12:15], a[252:255], a[88:91], v[12:15]\n\t"
    "s_waitcnt lgkmcnt(2)\n\t"
    "v_exp_f32 v47, v47\n\t"
    "v_mfma_f32_16x16x32_bf16 v[252:255], v[60:63], v[92:95], v[252:255]\n\t"
    "buffer_load_dwordx4 v233, s[40:43], s71 offen offset:3072 nt lds\n\t"
    "v_mfma_f32_16x16x32_bf16 v[16:19], a[224:227], a[0:3], 0\n\t"
    "v_fma_f32 v0, v0, s72, v240\n\t"
    "v_fma_f32 v1, v1, s72, v240\n\t"
    "v_mfma_f32_16x16x32_bf16 v[16:19], a[228:231], a[4:7], v[16:19]\n\t"
    "s_waitcnt lgkmcnt(1)\n\t"
    "v_exp_f32 v0, v0\n\t"
    "v_mfma_f32_16x16x32_bf16 v[16:19], a[232:235], a[32:35], v[16:19]\n\t"
    "ds_read_b128 a[244:247], v225 offset:57344\n\t"
    "v_cvt_pk_bf16_f32 v46, v46, v47\n\t"
    "v_accvgpr_write_b32 a141, v46\n\t"
    "v_mfma_f32_16x16x32_bf16 v[16:19], a[236:239], a[36:39], v[16:19]\n\t"
    "v_exp_f32 v1, v1\n\t"
    "v_mfma_f32_16x16x32_bf16 v[20:23], a[236:239], a[44:47], 0\n\t"
    "v_fma_f32 v2, v2, s72, v240\n\t"
    "v_fma_f32 v3, v3, s72, v240\n\t"
    "v_mfma_f32_16x16x32_bf16 v[20:23], a[232:235], a[40:43], v[20:23]\n\t"
    "v_exp_f32 v2, v2\n\t"
    "v_mfma_f32_16x16x32_bf16 v[20:23], a[228:231], a[12:15], v[20:23]\n\t"
    "ds_read_b128 a[248:251], v224 offset:61440\n\t"
    "ds_read_b128 v[244:247], v236 offset:1536\n\t"
    "v_cvt_pk_bf16_f32 v0, v0, v1\n\t"
    "v_accvgpr_write_b32 a130, v0\n\t"
    "v_mfma_f32_16x16x32_bf16 v[20:23], a[224:227], a[8:11], v[20:23]\n\t"
    "v_exp_f32 v3, v3\n\t"
    "v_mfma_f32_16x16x32_bf16 v[24:27], a[224:227], a[16:19], 0\n\t"
    "v_fma_f32 v4, v4, s72, v241\n\t"
    "v_fma_f32 v5, v5, s72, v241\n\t"
    "v_mfma_f32_16x16x32_bf16 v[24:27], a[228:231], a[20:23], v[24:27]\n\t"
    "s_xor_b32 s71, s64, 0x1c000\n\t"
    "v_exp_f32 v4, v4\n\t"
    "v_mfma_f32_16x16x32_bf16 v[24:27], a[232:235], a[48:51], v[24:27]\n\t"
    "ds_read_b128 a[252:255], v225 offset:61440\n\t"
    "v_cvt_pk_bf16_f32 v2, v2, v3\n\t"
    "v_accvgpr_write_b32 a131, v2\n\t"
    "v_mfma_f32_16x16x32_bf16 v[24:27], a[236:239], a[52:55], v[24:27]\n\t"
    "s_waitcnt lgkmcnt(1)\n\t"
    "v_exp_f32 v5, v5\n\t"
    "v_mfma_f32_16x16x32_bf16 v[28:31], a[236:239], a[60:63], 0\n\t"
    "buffer_store_dwordx4 v[244:247], v237, s[56:59], s83 offen offset:2048 sc1\n\t"
    "v_fma_f32 v6, v6, s72, v241\n\t"
    "v_fma_f32 v7, v7, s72, v241\n\t"
    "v_mfma_f32_16x16x32_bf16 v[28:31], a[232:235], a[56:59], v[28:31]\n\t"
    "s_add_u32 m0, s70, 0x1c000\n\t"
    "v_exp_f32 v6, v6\n\t"
    "v_mfma_f32_16x16x32_bf16 v[28:31], a[228:231], a[28:31], v[28:31]\n\t"
    "ds_read_b128 a[236:239], v224 offset:51200\n\t"
    "v_cvt_pk_bf16_f32 v4, v4, v5\n\t"
    "v_accvgpr_write_b32 a134, v4\n\t"
    "v_mfma_f32_16x16x32_bf16 v[28:31], a[224:227], a[24:27], v[28:31]\n\t"
    "v_exp_f32 v7, v7\n\t"
    "v_mfma_f32_16x16x32_bf16 v[252:255], v[48:51], a[128:131], v[252:255]\n\t"
    "buffer_load_dwordx4 v232, s[40:43], s71 offen offset:0 nt lds\n\t"
    "v_mfma_f32_16x16x32_bf16 v[16:19], a[240:243], a[64:67], v[16:19]\n\t"
    "v_fma_f32 v8, v8, s72, v242\n\t"
    "v_fma_f32 v9, v9, s72, v242\n\t"
    "v_mfma_f32_16x16x32_bf16 v[16:19], a[244:247], a[68:71], v[16:19]\n\t"
    "s_waitcnt lgkmcnt(1)\n\t"
    "v_exp_f32 v8, v8\n\t"
    "v_mfma_f32_16x16x32_bf16 v[16:19], a[248:251], a[96:99], v[16:19]\n\t"
    "ds_read_b128 a[232:235], v225 offset:51200\n\t"
    "v_cvt_pk_bf16_f32 v6, v6, v7\n\t"
    "v_accvgpr_write_b32 a135, v6\n\t"
    "v_mfma_f32_16x16x32_bf16 v[16:19], a[252:255], a[100:103], v[16:19]\n\t"
    "v_exp_f32 v9, v9\n\t"
    "v_mfma_f32_16x16x32_bf16 v[20:23], a[252:255], a[108:111], v[20:23]\n\t"
    "v_fma_f32 v10, v10, s72, v242\n\t"
    "v_fma_f32 v11, v11, s72, v242\n\t"
    "v_mfma_f32_16x16x32_bf16 v[20:23], a[248:251], a[104:107], v[20:23]\n\t"
    "v_exp_f32 v10, v10\n\t"
    "v_mfma_f32_16x16x32_bf16 v[20:23], a[244:247], a[76:79], v[20:23]\n\t"
    "ds_read_b128 a[228:231], v224 offset:55296\n\t"
    "v_cvt_pk_bf16_f32 v8, v8, v9\n\t"
    "v_accvgpr_write_b32 a138, v8\n\t"
    "v_mfma_f32_16x16x32_bf16 v[20:23], a[240:243], a[72:75], v[20:23]\n\t"
    "v_exp_f32 v11, v11\n\t"
    "v_mfma_f32_16x16x32_bf16 v[24:27], a[240:243], a[80:83], v[24:27]\n\t"
    "v_fma_f32 v12, v12, s72, v243\n\t"
    "v_fma_f32 v13, v13, s72, v243\n\t"
    "v_mfma_f32_16x16x32_bf16 v[24:27], a[244:247], a[84:87], v[24:27]\n\t"
    "v_exp_f32 v12, v12\n\t"
    "v_mfma_f32_16x16x32_bf16 v[24:27], a[248:251], a[112:115], v[24:27]\n\t"
    "ds_read_b128 a[224:227], v225 offset:55296\n\t"
    "v_cvt_pk_bf16_f32 v10, v10, v11\n\t"
    "v_accvgpr_write_b32 a139, v10\n\t"
    "v_mfma_f32_16x16x32_bf16 v[24:27], a[252:255], a[116:119], v[24:27]\n\t"
    "v_exp_f32 v13, v13\n\t"
    "v_mfma_f32_16x16x32_bf16 v[28:31], a[252:255], a[124:127], v[28:31]\n\t"
    "v_fma_f32 v14, v14, s72, v243\n\t"
    "v_fma_f32 v15, v15, s72, v243\n\t"
    "v_mfma_f32_16x16x32_bf16 v[28:31], a[248:251], a[120:123], v[28:31]\n\t"
    "v_exp_f32 v14, v14\n\t"
    "v_mfma_f32_16x16x32_bf16 v[28:31], a[244:247], a[92:95], v[28:31]\n\t"
    "ds_read_b128 a[252:255], v224 offset:59392\n\t"
    "v_cvt_pk_bf16_f32 v12, v12, v13\n\t"
    "v_accvgpr_write_b32 a142, v12\n\t"
    "v_mfma_f32_16x16x32_bf16 v[28:31], a[240:243], a[88:91], v[28:31]\n\t"
    "s_waitcnt lgkmcnt(2)\n\t"
    "v_exp_f32 v15, v15\n\t"
    "v_mfma_f32_16x16x32_bf16 v[252:255], v[52:55], a[132:135], v[252:255]\n\t"
    "buffer_load_dwordx4 v233, s[40:43], s71 offen offset:1024 nt lds\n\t"
    "v_mfma_f32_16x16x32_bf16 v[32:35], a[236:239], a[0:3], 0\n\t"
    "v_fma_f32 v16, v16, s72, v240\n\t"
    "v_fma_f32 v17, v17, s72, v240\n\t"
    "v_mfma_f32_16x16x32_bf16 v[32:35], a[232:235], a[4:7], v[32:35]\n\t"
    "ds_read_b128 v[248:251], v236 offset:1792\n\t"
    "v_exp_f32 v16, v16\n\t"
    "v_mfma_f32_16x16x32_bf16 v[32:35], a[228:231], a[32:35], v[32:35]\n\t"
    "s_waitcnt lgkmcnt(2)\n\t"
    "ds_read_b128 a[248:251], v225 offset:59392\n\t"
    "v_cvt_pk_bf16_f32 v14, v14, v15\n\t"
    "v_accvgpr_write_b32 a143, v14\n\t"
    "v_mfma_f32_16x16x32_bf16 v[32:35], a[224:227], a[36:39], v[32:35]\n\t"
    "v_exp_f32 v17, v17\n\t"
    "v_mfma_f32_16x16x32_bf16 v[36:39], a[224:227], a[44:47], 0\n\t"
    "v_fma_f32 v18, v18, s72, v240\n\t"
    "v_fma_f32 v19, v19, s72, v240\n\t"
    "v_mfma_f32_16x16x32_bf16 v[36:39], a[228:231], a[40:43], v[36:39]\n\t"
    "v_exp_f32 v18, v18\n\t"
    "v_mfma_f32_16x16x32_bf16 v[36:39], a[232:235], a[12:15], v[36:39]\n\t"
    "ds_read_b128 a[244:247], v224 offset:63488\n\t"
    "v_cvt_pk_bf16_f32 v16, v16, v17\n\t"
    "v_accvgpr_write_b32 a144, v16\n\t"
    "v_mfma_f32_16x16x32_bf16 v[36:39], a[236:239], a[8:11], v[36:39]\n\t"
    "s_waitcnt lgkmcnt(2)\n\t"
    "v_exp_f32 v19, v19\n\t"
    "v_mfma_f32_16x16x32_bf16 v[252:255], v[52:55], v[68:71], v[252:255]\n\t"
    "buffer_store_dwordx4 v[248:251], v237, s[56:59], s83 offen offset:3072 sc1\n\t"
    "v_mfma_f32_16x16x32_bf16 v[40:43], a[236:239], a[16:19], 0\n\t"
    "v_fma_f32 v20, v20, s72, v241\n\t"
    "v_fma_f32 v21, v21, s72, v241\n\t"
    "v_mfma_f32_16x16x32_bf16 v[40:43], a[232:235], a[20:23], v[40:43]\n\t"
    "v_exp_f32 v20, v20\n\t"
    "v_mfma_f32_16x16x32_bf16 v[40:43], a[228:231], a[48:51], v[40:43]\n\t"
    "ds_read_b128 a[240:243], v225 offset:63488\n\t"
    "v_cvt_pk_bf16_f32 v18, v18, v19\n\t"
    "v_accvgpr_write_b32 a145, v18\n\t"
    "v_mfma_f32_16x16x32_bf16 v[40:43], a[224:227], a[52:55], v[40:43]\n\t"
    "v_exp_f32 v21, v21\n\t"
    "v_mfma_f32_16x16x32_bf16 v[44:47], a[224:227], a[60:63], 0\n\t"
    "v_fma_f32 v22, v22, s72, v241\n\t"
    "v_fma_f32 v23, v23, s72, v241\n\t"
    "v_mfma_f32_16x16x32_bf16 v[44:47], a[228:231], a[56:59], v[44:47]\n\t"
    "v_exp_f32 v22, v22\n\t"
    "v_mfma_f32_16x16x32_bf16 v[44:47], a[232:235], a[28:31], v[44:47]\n\t"
    "v_cvt_pk_bf16_f32 v20, v20, v21\n\t"
    "v_accvgpr_write_b32 a148, v20\n\t"
    "v_mfma_f32_16x16x32_bf16 v[44:47], a[236:239], a[24:27], v[44:47]\n\t"
    "s_waitcnt lgkmcnt(1)\n\t"
    "v_exp_f32 v23, v23\n\t"
    "v_mfma_f32_16x16x32_bf16 v[252:255], v[56:59], a[136:139], v[252:255]\n\t"
    "buffer_load_dwordx4 v232, s[40:43], s71 offen offset:2048 nt lds\n\t"
    "v_mfma_f32_16x16x32_bf16 v[32:35], a[252:255], a[64:67], v[32:35]\n\t"
    "v_fma_f32 v24, v24, s72, v242\n\t"
    "v_fma_f32 v25, v25, s72, v242\n\t"
    "v_mfma_f32_16x16x32_bf16 v[32:35], a[248:251], a[68:71], v[32:35]\n\t"
    "s_waitcnt lgkmcnt(0)\n\t"
    "v_exp_f32 v24, v24\n\t"
    "v_mfma_f32_16x16x32_bf16 v[32:35], a[244:247], a[96:99], v[32:35]\n\t"
    "s_waitcnt vmcnt(18)\n\t"
    "v_cvt_pk_bf16_f32 v22, v22, v23\n\t"
    "v_accvgpr_write_b32 a149, v22\n\t"
    "v_mfma_f32_16x16x32_bf16 v[32:35], a[240:243], a[100:103], v[32:35]\n\t"
    "s_barrier\n\t"
    "v_exp_f32 v25, v25\n\t"
    "v_add_u32 v224, 0x10000, v224\n\t"
    "v_mfma_f32_16x16x32_bf16 v[36:39], a[240:243], a[108:111], v[36:39]\n\t"
    "ds_read_b128 a[224:227], v224 offset:0\n\t"
    "v_fma_f32 v26, v26, s72, v242\n\t"
    "v_fma_f32 v27, v27, s72, v242\n\t"
    "v_add_u32 v225, 0x10000, v225\n\t"
    "v_mfma_f32_16x16x32_bf16 v[36:39], a[244:247], a[104:107], v[36:39]\n\t"
    "ds_read_b128 a[228:231], v225 offset:0\n\t"
    "v_exp_f32 v26, v26\n\t"
    "v_mfma_f32_16x16x32_bf16 v[36:39], a[248:251], a[76:79], v[36:39]\n\t"
    "ds_read_b128 a[232:235], v224 offset:4096\n\t"
    "v_cvt_pk_bf16_f32 v24, v24, v25\n\t"
    "v_accvgpr_write_b32 a152, v24\n\t"
    "v_mfma_f32_16x16x32_bf16 v[36:39], a[252:255], a[72:75], v[36:39]\n\t"
    "v_exp_f32 v27, v27\n\t"
    "v_mfma_f32_16x16x32_bf16 v[40:43], a[252:255], a[80:83], v[40:43]\n\t"
    "v_fma_f32 v28, v28, s72, v243\n\t"
    "v_fma_f32 v29, v29, s72, v243\n\t"
    "v_mfma_f32_16x16x32_bf16 v[40:43], a[248:251], a[84:87], v[40:43]\n\t"
    "v_exp_f32 v28, v28\n\t"
    "v_mfma_f32_16x16x32_bf16 v[40:43], a[244:247], a[112:115], v[40:43]\n\t"
    "ds_read_b128 a[236:239], v225 offset:4096\n\t"
    "v_cvt_pk_bf16_f32 v26, v26, v27\n\t"
    "v_accvgpr_write_b32 a153, v26\n\t"
    "v_mfma_f32_16x16x32_bf16 v[40:43], a[240:243], a[116:119], v[40:43]\n\t"
    "v_exp_f32 v29, v29\n\t"
    "v_mfma_f32_16x16x32_bf16 v[44:47], a[240:243], a[124:127], v[44:47]\n\t"
    "v_fma_f32 v30, v30, s72, v243\n\t"
    "v_fma_f32 v31, v31, s72, v243\n\t"
    "v_mfma_f32_16x16x32_bf16 v[44:47], a[244:247], a[120:123], v[44:47]\n\t"
    "v_exp_f32 v30, v30\n\t"
    "v_mfma_f32_16x16x32_bf16 v[44:47], a[248:251], a[92:95], v[44:47]\n\t"
    "ds_read_b128 a[240:243], v224 offset:8192\n\t"
    "v_cvt_pk_bf16_f32 v28, v28, v29\n\t"
    "v_accvgpr_write_b32 a156, v28\n\t"
    "v_mfma_f32_16x16x32_bf16 v[44:47], a[252:255], a[88:91], v[44:47]\n\t"
    "s_waitcnt lgkmcnt(2)\n\t"
    "v_exp_f32 v31, v31\n\t"
    "v_mfma_f32_16x16x32_bf16 v[252:255], v[60:63], a[140:143], v[252:255]\n\t"
    "buffer_load_dwordx4 v233, s[40:43], s71 offen offset:3072 nt lds\n\t"
    "v_mfma_f32_16x16x32_bf16 v[0:3], a[224:227], a[0:3], 0\n\t"
    "v_fma_f32 v32, v32, s72, v240\n\t"
    "v_fma_f32 v33, v33, s72, v240\n\t"
    "v_mfma_f32_16x16x32_bf16 v[0:3], a[228:231], a[4:7], v[0:3]\n\t"
    "s_waitcnt lgkmcnt(1)\n\t"
    "v_exp_f32 v32, v32\n\t"
    "v_mfma_f32_16x16x32_bf16 v[0:3], a[232:235], a[32:35], v[0:3]\n\t"
    "ds_read_b128 a[244:247], v225 offset:8192\n\t"
    "v_cvt_pk_bf16_f32 v30, v30, v31\n\t"
    "v_accvgpr_write_b32 a157, v30\n\t"
    "v_mfma_f32_16x16x32_bf16 v[0:3], a[236:239], a[36:39], v[0:3]\n\t"
    "v_exp_f32 v33, v33\n\t"
    "v_mfma_f32_16x16x32_bf16 v[4:7], a[236:239], a[44:47], 0\n\t"
    "v_fma_f32 v34, v34, s72, v240\n\t"
    "v_fma_f32 v35, v35, s72, v240\n\t"
    "v_mfma_f32_16x16x32_bf16 v[4:7], a[232:235], a[40:43], v[4:7]\n\t"
    "v_exp_f32 v34, v34\n\t"
    "v_mfma_f32_16x16x32_bf16 v[4:7], a[228:231], a[12:15], v[4:7]\n\t"
    "ds_read_b128 a[248:251], v224 offset:12288\n\t"
    "v_cvt_pk_bf16_f32 v32, v32, v33\n\t"
    "v_accvgpr_write_b32 a146, v32\n\t"
    "v_mfma_f32_16x16x32_bf16 v[4:7], a[224:227], a[8:11], v[4:7]\n\t"
    "v_exp_f32 v35, v35\n\t"
    "v_mfma_f32_16x16x32_bf16 v[8:11], a[224:227], a[16:19], 0\n\t"
    "v_fma_f32 v36, v36, s72, v241\n\t"
    "v_fma_f32 v37, v37, s72, v241\n\t"
    "v_mfma_f32_16x16x32_bf16 v[8:11], a[228:231], a[20:23], v[8:11]\n\t"
    "v_exp_f32 v36, v36\n\t"
    "v_mfma_f32_16x16x32_bf16 v[8:11], a[232:235], a[48:51], v[8:11]\n\t"
    "ds_read_b128 a[252:255], v225 offset:12288\n\t"
    "v_cvt_pk_bf16_f32 v34, v34, v35\n\t"
    "v_accvgpr_write_b32 a147, v34\n\t"
    "v_mfma_f32_16x16x32_bf16 v[8:11], a[236:239], a[52:55], v[8:11]\n\t"
    "s_xor_b32 s71, s64, 0x20000\n\t"
    "v_exp_f32 v37, v37\n\t"
    "v_mfma_f32_16x16x32_bf16 v[12:15], a[236:239], a[60:63], 0\n\t"
    "s_add_u32 m0, s70, 0x20000\n\t"
    "v_fma_f32 v38, v38, s72, v241\n\t"
    "v_fma_f32 v39, v39, s72, v241\n\t"
    "v_mfma_f32_16x16x32_bf16 v[12:15], a[232:235], a[56:59], v[12:15]\n\t"
    "v_exp_f32 v38, v38\n\t"
    "v_mfma_f32_16x16x32_bf16 v[12:15], a[228:231], a[28:31], v[12:15]\n\t"
    "ds_read_b128 a[236:239], v224 offset:2048\n\t"
    "v_cvt_pk_bf16_f32 v36, v36, v37\n\t"
    "v_accvgpr_write_b32 a150, v36\n\t"
    "v_mfma_f32_16x16x32_bf16 v[12:15], a[224:227], a[24:27], v[12:15]\n\t"
    "s_waitcnt lgkmcnt(2)\n\t"
    "v_exp_f32 v39, v39\n\t"
    "v_mfma_f32_16x16x32_bf16 v[252:255], v[48:51], a[144:147], v[252:255]\n\t"
    "buffer_load_dwordx4 v232, s[40:43], s71 offen offset:0 nt lds\n\t"
    "v_mfma_f32_16x16x32_bf16 v[0:3], a[240:243], a[64:67], v[0:3]\n\t"
    "v_fma_f32 v40, v40, s72, v242\n\t"
    "v_fma_f32 v41, v41, s72, v242\n\t"
    "v_mfma_f32_16x16x32_bf16 v[0:3], a[244:247], a[68:71], v[0:3]\n\t"
    "s_waitcnt lgkmcnt(1)\n\t"
    "v_exp_f32 v40, v40\n\t"
    "v_mfma_f32_16x16x32_bf16 v[0:3], a[248:251], a[96:99], v[0:3]\n\t"
    "ds_read_b128 a[232:235], v225 offset:2048\n\t"
    "v_cvt_pk_bf16_f32 v38, v38, v39\n\t"
    "v_accvgpr_write_b32 a151, v38\n\t"
    "v_mfma_f32_16x16x32_bf16 v[0:3], a[252:255], a[100:103], v[0:3]\n\t"
    "v_exp_f32 v41, v41\n\t"
    "v_mfma_f32_16x16x32_bf16 v[4:7], a[252:255], a[108:111], v[4:7]\n\t"
    "v_fma_f32 v42, v42, s72, v242\n\t"
    "v_fma_f32 v43, v43, s72, v242\n\t"
    "v_mfma_f32_16x16x32_bf16 v[4:7], a[248:251], a[104:107], v[4:7]\n\t"
    "v_exp_f32 v42, v42\n\t"
    "v_mfma_f32_16x16x32_bf16 v[4:7], a[244:247], a[76:79], v[4:7]\n\t"
    "ds_read_b128 a[228:231], v224 offset:6144\n\t"
    "v_cvt_pk_bf16_f32 v40, v40, v41\n\t"
    "v_accvgpr_write_b32 a154, v40\n\t"
    "v_mfma_f32_16x16x32_bf16 v[4:7], a[240:243], a[72:75], v[4:7]\n\t"
    "v_exp_f32 v43, v43\n\t"
    "v_mfma_f32_16x16x32_bf16 v[8:11], a[240:243], a[80:83], v[8:11]\n\t"
    "v_fma_f32 v44, v44, s72, v243\n\t"
    "v_fma_f32 v45, v45, s72, v243\n\t"
    "v_mfma_f32_16x16x32_bf16 v[8:11], a[244:247], a[84:87], v[8:11]\n\t"
    "v_exp_f32 v44, v44\n\t"
    "v_mfma_f32_16x16x32_bf16 v[8:11], a[248:251], a[112:115], v[8:11]\n\t"
    "ds_read_b128 a[224:227], v225 offset:6144\n\t"
    "v_cvt_pk_bf16_f32 v42, v42, v43\n\t"
    "v_accvgpr_write_b32 a155, v42\n\t"
    "v_mfma_f32_16x16x32_bf16 v[8:11], a[252:255], a[116:119], v[8:11]\n\t"
    "v_exp_f32 v45, v45\n\t"
    "v_mfma_f32_16x16x32_bf16 v[12:15], a[252:255], a[124:127], v[12:15]\n\t"
    "v_fma_f32 v46, v46, s72, v243\n\t"
    "v_fma_f32 v47, v47, s72, v243\n\t"
    "v_mfma_f32_16x16x32_bf16 v[12:15], a[248:251], a[120:123], v[12:15]\n\t"
    "v_exp_f32 v46, v46\n\t"
    "v_mfma_f32_16x16x32_bf16 v[12:15], a[244:247], a[92:95], v[12:15]\n\t"
    "ds_read_b128 a[252:255], v224 offset:10240\n\t"
    "v_cvt_pk_bf16_f32 v44, v44, v45\n\t"
    "v_accvgpr_write_b32 a158, v44\n\t"
    "v_mfma_f32_16x16x32_bf16 v[12:15], a[240:243], a[88:91], v[12:15]\n\t"
    "s_waitcnt lgkmcnt(2)\n\t"
    "v_exp_f32 v47, v47\n\t"
    "v_mfma_f32_16x16x32_bf16 v[252:255], v[52:55], a[148:151], v[252:255]\n\t"
    "buffer_load_dwordx4 v233, s[40:43], s71 offen offset:1024 nt lds\n\t"
    "v_mfma_f32_16x16x32_bf16 v[16:19], a[236:239], a[0:3], 0\n\t"
    "v_fma_f32 v0, v0, s72, v240\n\t"
    "v_fma_f32 v1, v1, s72, v240\n\t"
    "v_mfma_f32_16x16x32_bf16 v[16:19], a[232:235], a[4:7], v[16:19]\n\t"
    "s_waitcnt lgkmcnt(1)\n\t"
    "v_exp_f32 v0, v0\n\t"
    "v_mfma_f32_16x16x32_bf16 v[16:19], a[228:231], a[32:35], v[16:19]\n\t"
    "ds_read_b128 a[248:251], v225 offset:10240\n\t"
    "v_cvt_pk_bf16_f32 v46, v46, v47\n\t"
    "v_accvgpr_write_b32 a159, v46\n\t"
    "v_mfma_f32_16x16x32_bf16 v[16:19], a[224:227], a[36:39], v[16:19]\n\t"
    "v_exp_f32 v1, v1\n\t"
    "v_mfma_f32_16x16x32_bf16 v[20:23], a[224:227], a[44:47], 0\n\t"
    "v_fma_f32 v2, v2, s72, v240\n\t"
    "v_fma_f32 v3, v3, s72, v240\n\t"
    "v_mfma_f32_16x16x32_bf16 v[20:23], a[228:231], a[40:43], v[20:23]\n\t"
    "v_exp_f32 v2, v2\n\t"
    "v_mfma_f32_16x16x32_bf16 v[20:23], a[232:235], a[12:15], v[20:23]\n\t"
    "ds_read_b128 a[244:247], v224 offset:14336\n\t"
    "v_cvt_pk_bf16_f32 v0, v0, v1\n\t"
    "v_accvgpr_write_b32 a160, v0\n\t"
    "v_mfma_f32_16x16x32_bf16 v[20:23], a[236:239], a[8:11], v[20:23]\n\t"
    "v_exp_f32 v3, v3\n\t"
    "v_mfma_f32_16x16x32_bf16 v[252:255], v[56:59], v[72:75], v[252:255]\n\t"
    "buffer_load_dwordx4 v232, s[40:43], s71 offen offset:2048 nt lds\n\t"
    "v_mfma_f32_16x16x32_bf16 v[24:27], a[236:239], a[16:19], 0\n\t"
    "v_fma_f32 v4, v4, s72, v241\n\t"
    "v_fma_f32 v5, v5, s72, v241\n\t"
    "v_mfma_f32_16x16x32_bf16 v[24:27], a[232:235], a[20:23], v[24:27]\n\t"
    "v_exp_f32 v4, v4\n\t"
    "v_mfma_f32_16x16x32_bf16 v[24:27], a[228:231], a[48:51], v[24:27]\n\t"
    "ds_read_b128 a[240:243], v225 offset:14336\n\t"
    "v_cvt_pk_bf16_f32 v2, v2, v3\n\t"
    "v_accvgpr_write_b32 a161, v2\n\t"
    "v_mfma_f32_16x16x32_bf16 v[24:27], a[224:227], a[52:55], v[24:27]\n\t"
    "v_exp_f32 v5, v5\n\t"
    "v_mfma_f32_16x16x32_bf16 v[28:31], a[224:227], a[60:63], 0\n\t"
    "v_fma_f32 v6, v6, s72, v241\n\t"
    "v_fma_f32 v7, v7, s72, v241\n\t"
    "v_mfma_f32_16x16x32_bf16 v[28:31], a[228:231], a[56:59], v[28:31]\n\t"
    "v_exp_f32 v6, v6\n\t"
    "v_mfma_f32_16x16x32_bf16 v[28:31], a[232:235], a[28:31], v[28:31]\n\t"
    "v_cvt_pk_bf16_f32 v4, v4, v5\n\t"
    "v_accvgpr_write_b32 a164, v4\n\t"
    "v_mfma_f32_16x16x32_bf16 v[28:31], a[236:239], a[24:27], v[28:31]\n\t"
    "v_exp_f32 v7, v7\n\t"
    "v_mfma_f32_16x16x32_bf16 v[252:255], v[56:59], a[152:155], v[252:255]\n\t"
    "s_waitcnt lgkmcnt(1)\n\t"
    "v_mfma_f32_16x16x32_bf16 v[16:19], a[252:255], a[64:67], v[16:19]\n\t"
    "v_fma_f32 v8, v8, s72, v242\n\t"
    "v_fma_f32 v9, v9, s72, v242\n\t"
    "v_mfma_f32_16x16x32_bf16 v[16:19], a[248:251], a[68:71], v[16:19]\n\t"
    "s_waitcnt lgkmcnt(0)\n\t"
    "v_exp_f32 v8, v8\n\t"
    "v_mfma_f32_16x16x32_bf16 v[16:19], a[244:247], a[96:99], v[16:19]\n\t"
    "s_waitcnt vmcnt(15)\n\t"
    "v_cvt_pk_bf16_f32 v6, v6, v7\n\t"
    "v_accvgpr_write_b32 a165, v6\n\t"
    "v_mfma_f32_16x16x32_bf16 v[16:19], a[240:243], a[100:103], v[16:19]\n\t"
    "s_barrier\n\t"
    "v_exp_f32 v9, v9\n\t"
    "v_mfma_f32_16x16x32_bf16 v[20:23], a[240:243], a[108:111], v[20:23]\n\t"
    "ds_read_b128 a[224:227], v224 offset:16384\n\t"
    "v_fma_f32 v10, v10, s72, v242\n\t"
    "v_fma_f32 v11, v11, s72, v242\n\t"
    "v_mfma_f32_16x16x32_bf16 v[20:23], a[244:247], a[104:107], v[20:23]\n\t"
    "ds_read_b128 a[228:231], v225 offset:16384\n\t"
    "v_exp_f32 v10, v10\n\t"
    "v_mfma_f32_16x16x32_bf16 v[20:23], a[248:251], a[76:79], v[20:23]\n\t"
    "ds_read_b128 a[232:235], v224 offset:20480\n\t"
    "v_cvt_pk_bf16_f32 v8, v8, v9\n\t"
    "v_accvgpr_write_b32 a168, v8\n\t"
    "v_mfma_f32_16x16x32_bf16 v[20:23], a[252:255], a[72:75], v[20:23]\n\t"
    "v_exp_f32 v11, v11\n\t"
    "v_mfma_f32_16x16x32_bf16 v[24:27], a[252:255], a[80:83], v[24:27]\n\t"
    "v_fma_f32 v12, v12, s72, v243\n\t"
    "v_fma_f32 v13, v13, s72, v243\n\t"
    "v_mfma_f32_16x16x32_bf16 v[24:27], a[248:251], a[84:87], v[24:27]\n\t"
    "v_exp_f32 v12, v12\n\t"
    "v_mfma_f32_16x16x32_bf16 v[24:27], a[244:247], a[112:115], v[24:27]\n\t"
    "ds_read_b128 a[236:239], v225 offset:20480\n\t"
    "v_cvt_pk_bf16_f32 v10, v10, v11\n\t"
    "v_accvgpr_write_b32 a169, v10\n\t"
    "v_mfma_f32_16x16x32_bf16 v[24:27], a[240:243], a[116:119], v[24:27]\n\t"
    "v_exp_f32 v13, v13\n\t"
    "v_mfma_f32_16x16x32_bf16 v[28:31], a[240:243], a[124:127], v[28:31]\n\t"
    "v_fma_f32 v14, v14, s72, v243\n\t"
    "v_fma_f32 v15, v15, s72, v243\n\t"
    "v_mfma_f32_16x16x32_bf16 v[28:31], a[244:247], a[120:123], v[28:31]\n\t"
    "v_exp_f32 v14, v14\n\t"
    "v_mfma_f32_16x16x32_bf16 v[28:31], a[248:251], a[92:95], v[28:31]\n\t"
    "ds_read_b128 a[240:243], v224 offset:24576\n\t"
    "v_cvt_pk_bf16_f32 v12, v12, v13\n\t"
    "v_accvgpr_write_b32 a172, v12\n\t"
    "v_mfma_f32_16x16x32_bf16 v[28:31], a[252:255], a[88:91], v[28:31]\n\t"
    "s_waitcnt lgkmcnt(2)\n\t"
    "v_exp_f32 v15, v15\n\t"
    "v_mfma_f32_16x16x32_bf16 v[252:255], v[60:63], a[156:159], v[252:255]\n\t"
    "buffer_load_dwordx4 v233, s[40:43], s71 offen offset:3072 nt lds\n\t"
    "v_mfma_f32_16x16x32_bf16 v[32:35], a[224:227], a[0:3], 0\n\t"
    "v_fma_f32 v16, v16, s72, v240\n\t"
    "v_fma_f32 v17, v17, s72, v240\n\t"
    "v_mfma_f32_16x16x32_bf16 v[32:35], a[228:231], a[4:7], v[32:35]\n\t"
    "s_waitcnt lgkmcnt(1)\n\t"
    "v_exp_f32 v16, v16\n\t"
    "v_mfma_f32_16x16x32_bf16 v[32:35], a[232:235], a[32:35], v[32:35]\n\t"
    "ds_read_b128 a[244:247], v225 offset:24576\n\t"
    "v_cvt_pk_bf16_f32 v14, v14, v15\n\t"
    "v_accvgpr_write_b32 a173, v14\n\t"
    "v_mfma_f32_16x16x32_bf16 v[32:35], a[236:239], a[36:39], v[32:35]\n\t"
    "v_exp_f32 v17, v17\n\t"
    "v_mfma_f32_16x16x32_bf16 v[36:39], a[236:239], a[44:47], 0\n\t"
    "v_fma_f32 v18, v18, s72, v240\n\t"
    "v_fma_f32 v19, v19, s72, v240\n\t"
    "v_mfma_f32_16x16x32_bf16 v[36:39], a[232:235], a[40:43], v[36:39]\n\t"
    "v_exp_f32 v18, v18\n\t"
    "v_mfma_f32_16x16x32_bf16 v[36:39], a[228:231], a[12:15], v[36:39]\n\t"
    "ds_read_b128 a[248:251], v224 offset:28672\n\t"
    "v_cvt_pk_bf16_f32 v16, v16, v17\n\t"
    "v_accvgpr_write_b32 a162, v16\n\t"
    "v_mfma_f32_16x16x32_bf16 v[36:39], a[224:227], a[8:11], v[36:39]\n\t"
    "v_exp_f32 v19, v19\n\t"
    "v_mfma_f32_16x16x32_bf16 v[40:43], a[224:227], a[16:19], 0\n\t"
    "v_fma_f32 v20, v20, s72, v241\n\t"
    "v_fma_f32 v21, v21, s72, v241\n\t"
    "v_mfma_f32_16x16x32_bf16 v[40:43], a[228:231], a[20:23], v[40:43]\n\t"
    "v_exp_f32 v20, v20\n\t"
    "v_mfma_f32_16x16x32_bf16 v[40:43], a[232:235], a[48:51], v[40:43]\n\t"
    "ds_read_b128 a[252:255], v225 offset:28672\n\t"
    "v_cvt_pk_bf16_f32 v18, v18, v19\n\t"
    "v_accvgpr_write_b32 a163, v18\n\t"
    "v_mfma_f32_16x16x32_bf16 v[40:43], a[236:239], a[52:55], v[40:43]\n\t"
    "s_xor_b32 s71, s64, 0x24000\n\t"
    "v_exp_f32 v21, v21\n\t"
    "v_mfma_f32_16x16x32_bf16 v[44:47], a[236:239], a[60:63], 0\n\t"
    "s_add_u32 m0, s70, 0x24000\n\t"
    "v_fma_f32 v22, v22, s72, v241\n\t"
    "v_fma_f32 v23, v23, s72, v241\n\t"
    "v_mfma_f32_16x16x32_bf16 v[44:47], a[232:235], a[56:59], v[44:47]\n\t"
    "v_exp_f32 v22, v22\n\t"
    "v_mfma_f32_16x16x32_bf16 v[44:47], a[228:231], a[28:31], v[44:47]\n\t"
    "ds_read_b128 a[236:239], v224 offset:18432\n\t"
    "v_cvt_pk_bf16_f32 v20, v20, v21\n\t"
    "v_accvgpr_write_b32 a166, v20\n\t"
    "v_mfma_f32_16x16x32_bf16 v[44:47], a[224:227], a[24:27], v[44:47]\n\t"
    "s_waitcnt lgkmcnt(2)\n\t"
    "v_exp_f32 v23, v23\n\t"
    "v_mfma_f32_16x16x32_bf16 v[252:255], v[48:51], a[160:163], v[252:255]\n\t"
    "buffer_load_dwordx4 v232, s[40:43], s71 offen offset:0 nt lds\n\t"
    "v_mfma_f32_16x16x32_bf16 v[32:35], a[240:243], a[64:67], v[32:35]\n\t"
    "v_fma_f32 v24, v24, s72, v242\n\t"
    "v_fma_f32 v25, v25, s72, v242\n\t"
    "v_mfma_f32_16x16x32_bf16 v[32:35], a[244:247], a[68:71], v[32:35]\n\t"
    "s_waitcnt lgkmcnt(1)\n\t"
    "v_exp_f32 v24, v24\n\t"
    "v_mfma_f32_16x16x32_bf16 v[32:35], a[248:251], a[96:99], v[32:35]\n\t"
    "ds_read_b128 a[232:235], v225 offset:18432\n\t"
    "v_cvt_pk_bf16_f32 v22, v22, v23\n\t"
    "v_accvgpr_write_b32 a167, v22\n\t"
    "v_mfma_f32_16x16x32_bf16 v[32:35], a[252:255], a[100:103], v[32:35]\n\t"
    "v_exp_f32 v25, v25\n\t"
    "v_mfma_f32_16x16x32_bf16 v[36:39], a[252:255], a[108:111], v[36:39]\n\t"
    "v_fma_f32 v26, v26, s72, v242\n\t"
    "v_fma_f32 v27, v27, s72, v242\n\t"
    "v_mfma_f32_16x16x32_bf16 v[36:39], a[248:251], a[104:107], v[36:39]\n\t"
    "v_exp_f32 v26, v26\n\t"
    "v_mfma_f32_16x16x32_bf16 v[36:39], a[244:247], a[76:79], v[36:39]\n\t"
    "ds_read_b128 a[228:231], v224 offset:22528\n\t"
    "v_cvt_pk_bf16_f32 v24, v24, v25\n\t"
    "v_accvgpr_write_b32 a170, v24\n\t"
    "v_mfma_f32_16x16x32_bf16 v[36:39], a[240:243], a[72:75], v[36:39]\n\t"
    "v_exp_f32 v27, v27\n\t"
    "v_mfma_f32_16x16x32_bf16 v[40:43], a[240:243], a[80:83], v[40:43]\n\t"
    "v_fma_f32 v28, v28, s72, v243\n\t"
    "v_fma_f32 v29, v29, s72, v243\n\t"
    "v_mfma_f32_16x16x32_bf16 v[40:43], a[244:247], a[84:87], v[40:43]\n\t"
    "v_exp_f32 v28, v28\n\t"
    "v_mfma_f32_16x16x32_bf16 v[40:43], a[248:251], a[112:115], v[40:43]\n\t"
    "ds_read_b128 a[224:227], v225 offset:22528\n\t"
    "v_cvt_pk_bf16_f32 v26, v26, v27\n\t"
    "v_accvgpr_write_b32 a171, v26\n\t"
    "v_mfma_f32_16x16x32_bf16 v[40:43], a[252:255], a[116:119], v[40:43]\n\t"
    "v_exp_f32 v29, v29\n\t"
    "v_mfma_f32_16x16x32_bf16 v[44:47], a[252:255], a[124:127], v[44:47]\n\t"
    "v_fma_f32 v30, v30, s72, v243\n\t"
    "v_fma_f32 v31, v31, s72, v243\n\t"
    "v_mfma_f32_16x16x32_bf16 v[44:47], a[248:251], a[120:123], v[44:47]\n\t"
    "v_exp_f32 v30, v30\n\t"
    "v_mfma_f32_16x16x32_bf16 v[44:47], a[244:247], a[92:95], v[44:47]\n\t"
    "ds_read_b128 a[252:255], v224 offset:26624\n\t"
    "v_cvt_pk_bf16_f32 v28, v28, v29\n\t"
    "v_accvgpr_write_b32 a174, v28\n\t"
    "v_mfma_f32_16x16x32_bf16 v[44:47], a[240:243], a[88:91], v[44:47]\n\t"
    "s_waitcnt lgkmcnt(2)\n\t"
    "v_exp_f32 v31, v31\n\t"
    "v_mfma_f32_16x16x32_bf16 v[252:255], v[52:55], a[164:167], v[252:255]\n\t"
    "buffer_load_dwordx4 v233, s[40:43], s71 offen offset:1024 nt lds\n\t"
    "v_mfma_f32_16x16x32_bf16 v[0:3], a[236:239], a[0:3], 0\n\t"
    "v_fma_f32 v32, v32, s72, v240\n\t"
    "v_fma_f32 v33, v33, s72, v240\n\t"
    "v_mfma_f32_16x16x32_bf16 v[0:3], a[232:235], a[4:7], v[0:3]\n\t"
    "s_waitcnt lgkmcnt(1)\n\t"
    "v_exp_f32 v32, v32\n\t"
    "v_mfma_f32_16x16x32_bf16 v[0:3], a[228:231], a[32:35], v[0:3]\n\t"
    "ds_read_b128 a[248:251], v225 offset:26624\n\t"
    "v_cvt_pk_bf16_f32 v30, v30, v31\n\t"
    "v_accvgpr_write_b32 a175, v30\n\t"
    "v_mfma_f32_16x16x32_bf16 v[0:3], a[224:227], a[36:39], v[0:3]\n\t"
    "v_exp_f32 v33, v33\n\t"
    "v_mfma_f32_16x16x32_bf16 v[4:7], a[224:227], a[44:47], 0\n\t"
    "v_fma_f32 v34, v34, s72, v240\n\t"
    "v_fma_f32 v35, v35, s72, v240\n\t"
    "v_mfma_f32_16x16x32_bf16 v[4:7], a[228:231], a[40:43], v[4:7]\n\t"
    "v_exp_f32 v34, v34\n\t"
    "v_mfma_f32_16x16x32_bf16 v[4:7], a[232:235], a[12:15], v[4:7]\n\t"
    "ds_read_b128 a[244:247], v224 offset:30720\n\t"
    "v_cvt_pk_bf16_f32 v32, v32, v33\n\t"
    "v_accvgpr_write_b32 a176, v32\n\t"
    "v_mfma_f32_16x16x32_bf16 v[4:7], a[236:239], a[8:11], v[4:7]\n\t"
    "v_exp_f32 v35, v35\n\t"
    "v_mfma_f32_16x16x32_bf16 v[252:255], v[60:63], v[76:79], v[252:255]\n\t"
    "buffer_load_dwordx4 v232, s[40:43], s71 offen offset:2048 nt lds\n\t"
    "v_mfma_f32_16x16x32_bf16 v[8:11], a[236:239], a[16:19], 0\n\t"
    "v_fma_f32 v36, v36, s72, v241\n\t"
    "v_fma_f32 v37, v37, s72, v241\n\t"
    "v_mfma_f32_16x16x32_bf16 v[8:11], a[232:235], a[20:23], v[8:11]\n\t"
    "v_exp_f32 v36, v36\n\t"
    "v_mfma_f32_16x16x32_bf16 v[8:11], a[228:231], a[48:51], v[8:11]\n\t"
    "ds_read_b128 a[240:243], v225 offset:30720\n\t"
    "v_cvt_pk_bf16_f32 v34, v34, v35\n\t"
    "v_accvgpr_write_b32 a177, v34\n\t"
    "v_mfma_f32_16x16x32_bf16 v[8:11], a[224:227], a[52:55], v[8:11]\n\t"
    "v_exp_f32 v37, v37\n\t"
    "v_mfma_f32_16x16x32_bf16 v[12:15], a[224:227], a[60:63], 0\n\t"
    "v_fma_f32 v38, v38, s72, v241\n\t"
    "v_fma_f32 v39, v39, s72, v241\n\t"
    "v_mfma_f32_16x16x32_bf16 v[12:15], a[228:231], a[56:59], v[12:15]\n\t"
    "v_exp_f32 v38, v38\n\t"
    "v_mfma_f32_16x16x32_bf16 v[12:15], a[232:235], a[28:31], v[12:15]\n\t"
    "v_cvt_pk_bf16_f32 v36, v36, v37\n\t"
    "v_accvgpr_write_b32 a180, v36\n\t"
    "v_mfma_f32_16x16x32_bf16 v[12:15], a[236:239], a[24:27], v[12:15]\n\t"
    "v_exp_f32 v39, v39\n\t"
    "v_mfma_f32_16x16x32_bf16 v[252:255], v[56:59], a[168:171], v[252:255]\n\t"
    "s_waitcnt lgkmcnt(1)\n\t"
    "v_mfma_f32_16x16x32_bf16 v[0:3], a[252:255], a[64:67], v[0:3]\n\t"
    "v_fma_f32 v40, v40, s72, v242\n\t"
    "v_fma_f32 v41, v41, s72, v242\n\t"
    "v_mfma_f32_16x16x32_bf16 v[0:3], a[248:251], a[68:71], v[0:3]\n\t"
    "s_waitcnt lgkmcnt(0)\n\t"
    "v_exp_f32 v40, v40\n\t"
    "v_mfma_f32_16x16x32_bf16 v[0:3], a[244:247], a[96:99], v[0:3]\n\t"
    "s_waitcnt vmcnt(13)\n\t"
    "v_cvt_pk_bf16_f32 v38, v38, v39\n\t"
    "v_accvgpr_write_b32 a181, v38\n\t"
    "v_mfma_f32_16x16x32_bf16 v[0:3], a[240:243], a[100:103], v[0:3]\n\t"
    "s_barrier\n\t"
    "v_exp_f32 v41, v41\n\t"
    "v_mfma_f32_16x16x32_bf16 v[4:7], a[240:243], a[108:111], v[4:7]\n\t"
    "ds_read_b128 a[224:227], v224 offset:32768\n\t"
    "v_fma_f32 v42, v42, s72, v242\n\t"
    "v_fma_f32 v43, v43, s72, v242\n\t"
    "v_mfma_f32_16x16x32_bf16 v[4:7], a[244:247], a[104:107], v[4:7]\n\t"
    "ds_read_b128 a[228:231], v225 offset:32768\n\t"
    "v_exp_f32 v42, v42\n\t"
    "v_mfma_f32_16x16x32_bf16 v[4:7], a[248:251], a[76:79], v[4:7]\n\t"
    "ds_read_b128 a[232:235], v224 offset:36864\n\t"
    "v_cvt_pk_bf16_f32 v40, v40, v41\n\t"
    "v_accvgpr_write_b32 a184, v40\n\t"
    "v_mfma_f32_16x16x32_bf16 v[4:7], a[252:255], a[72:75], v[4:7]\n\t"
    "v_exp_f32 v43, v43\n\t"
    "v_mfma_f32_16x16x32_bf16 v[8:11], a[252:255], a[80:83], v[8:11]\n\t"
    "v_fma_f32 v44, v44, s72, v243\n\t"
    "v_fma_f32 v45, v45, s72, v243\n\t"
    "v_mfma_f32_16x16x32_bf16 v[8:11], a[248:251], a[84:87], v[8:11]\n\t"
    "buffer_load_dwordx4 v233, s[40:43], s71 offen offset:3072 nt lds\n\t"
    "v_exp_f32 v44, v44\n\t"
    "v_mfma_f32_16x16x32_bf16 v[8:11], a[244:247], a[112:115], v[8:11]\n\t"
    "ds_read_b128 a[236:239], v225 offset:36864\n\t"
    "v_cvt_pk_bf16_f32 v42, v42, v43\n\t"
    "v_accvgpr_write_b32 a185, v42\n\t"
    "v_mfma_f32_16x16x32_bf16 v[8:11], a[240:243], a[116:119], v[8:11]\n\t"
    "v_exp_f32 v45, v45\n\t"
    "v_mfma_f32_16x16x32_bf16 v[12:15], a[240:243], a[124:127], v[12:15]\n\t"
    "v_fma_f32 v46, v46, s72, v243\n\t"
    "v_fma_f32 v47, v47, s72, v243\n\t"
    "v_mfma_f32_16x16x32_bf16 v[12:15], a[244:247], a[120:123], v[12:15]\n\t"
    "v_exp_f32 v46, v46\n\t"
    "v_mfma_f32_16x16x32_bf16 v[12:15], a[248:251], a[92:95], v[12:15]\n\t"
    "ds_read_b128 a[240:243], v224 offset:40960\n\t"
    "v_cvt_pk_bf16_f32 v44, v44, v45\n\t"
    "v_accvgpr_write_b32 a188, v44\n\t"
    "v_mfma_f32_16x16x32_bf16 v[12:15], a[252:255], a[88:91], v[12:15]\n\t"
    "v_exp_f32 v47, v47\n\t"
    "v_mfma_f32_16x16x32_bf16 v[252:255], v[60:63], a[172:175], v[252:255]\n\t"
    "s_waitcnt lgkmcnt(2)\n\t"
    "2:\n\t"
    "v_mfma_f32_16x16x32_bf16 v[16:19], a[224:227], a[0:3], 0\n\t"
    "v_fma_f32 v0, v0, s72, v240\n\t"
    "v_fma_f32 v1, v1, s72, v240\n\t"
    "v_mfma_f32_16x16x32_bf16 v[16:19], a[228:231], a[4:7], v[16:19]\n\t"
    "s_waitcnt lgkmcnt(1)\n\t"
    "v_exp_f32 v0, v0\n\t"
    "v_mfma_f32_16x16x32_bf16 v[16:19], a[232:235], a[32:35], v[16:19]\n\t"
    "ds_read_b128 a[244:247], v225 offset:40960\n\t"
    "v_cvt_pk_bf16_f32 v46, v46, v47\n\t"
    "v_accvgpr_write_b32 a189, v46\n\t"
    "v_mfma_f32_16x16x32_bf16 v[16:19], a[236:239], a[36:39], v[16:19]\n\t"
    "v_exp_f32 v1, v1\n\t"
    "v_mfma_f32_16x16x32_bf16 v[20:23], a[236:239], a[44:47], 0\n\t"
    "s_xor_b32 s71, s64, 0x28000\n\t"
    "v_fma_f32 v2, v2, s72, v240\n\t"
    "v_fma_f32 v3, v3, s72, v240\n\t"
    "v_mfma_f32_16x16x32_bf16 v[20:23], a[232:235], a[40:43], v[20:23]\n\t"
    "s_add_u32 m0, s70, 0x0\n\t"
    "v_exp_f32 v2, v2\n\t"
    "v_mfma_f32_16x16x32_bf16 v[20:23], a[228:231], a[12:15], v[20:23]\n\t"
    "ds_read_b128 a[248:251], v224 offset:45056\n\t"
    "buffer_load_dwordx4 v232, s[40:43], s71 offen offset:0 nt lds\n\t"
    "v_cvt_pk_bf16_f32 v0, v0, v1\n\t"
    "v_accvgpr_write_b32 a178, v0\n\t"
    "v_mfma_f32_16x16x32_bf16 v[20:23], a[224:227], a[8:11], v[20:23]\n\t"
    "v_exp_f32 v3, v3\n\t"
    "v_mfma_f32_16x16x32_bf16 v[24:27], a[224:227], a[16:19], 0\n\t"
    "v_fma_f32 v4, v4, s72, v241\n\t"
    "v_fma_f32 v5, v5, s72, v241\n\t"
    "v_mfma_f32_16x16x32_bf16 v[24:27], a[228:231], a[20:23], v[24:27]\n\t"
    "v_exp_f32 v4, v4\n\t"
    "v_mfma_f32_16x16x32_bf16 v[24:27], a[232:235], a[48:51], v[24:27]\n\t"
    "ds_read_b128 a[252:255], v225 offset:45056\n\t"
    "v_cvt_pk_bf16_f32 v2, v2, v3\n\t"
    "v_accvgpr_write_b32 a179, v2\n\t"
    "v_mfma_f32_16x16x32_bf16 v[24:27], a[236:239], a[52:55], v[24:27]\n\t"
    "v_exp_f32 v5, v5\n\t"
    "v_mfma_f32_16x16x32_bf16 v[28:31], a[236:239], a[60:63], 0\n\t"
    "v_fma_f32 v6, v6, s72, v241\n\t"
    "v_fma_f32 v7, v7, s72, v241\n\t"
    "v_mfma_f32_16x16x32_bf16 v[28:31], a[232:235], a[56:59], v[28:31]\n\t"
    "v_exp_f32 v6, v6\n\t"
    "v_mfma_f32_16x16x32_bf16 v[28:31], a[228:231], a[28:31], v[28:31]\n\t"
    "ds_read_b128 a[236:239], v224 offset:34816\n\t"
    "v_cvt_pk_bf16_f32 v4, v4, v5\n\t"
    "v_accvgpr_write_b32 a182, v4\n\t"
    "v_mfma_f32_16x16x32_bf16 v[28:31], a[224:227], a[24:27], v[28:31]\n\t"
    "s_waitcnt lgkmcnt(2)\n\t"
    "v_exp_f32 v7, v7\n\t"
    "v_mfma_f32_16x16x32_bf16 v[252:255], v[48:51], a[176:179], v[252:255]\n\t"
    "buffer_load_dwordx4 v233, s[40:43], s71 offen offset:1024 nt lds\n\t"
    "v_mfma_f32_16x16x32_bf16 v[16:19], a[240:243], a[64:67], v[16:19]\n\t"
    "v_fma_f32 v8, v8, s72, v242\n\t"
    "v_fma_f32 v9, v9, s72, v242\n\t"
    "v_mfma_f32_16x16x32_bf16 v[16:19], a[244:247], a[68:71], v[16:19]\n\t"
    "s_waitcnt lgkmcnt(1)\n\t"
    "v_exp_f32 v8, v8\n\t"
    "v_mfma_f32_16x16x32_bf16 v[16:19], a[248:251], a[96:99], v[16:19]\n\t"
    "ds_read_b128 a[232:235], v225 offset:34816\n\t"
    "v_cvt_pk_bf16_f32 v6, v6, v7\n\t"
    "v_accvgpr_write_b32 a183, v6\n\t"
    "v_mfma_f32_16x16x32_bf16 v[16:19], a[252:255], a[100:103], v[16:19]\n\t"
    "v_exp_f32 v9, v9\n\t"
    "v_mfma_f32_16x16x32_bf16 v[20:23], a[252:255], a[108:111], v[20:23]\n\t"
    "v_fma_f32 v10, v10, s72, v242\n\t"
    "v_fma_f32 v11, v11, s72, v242\n\t"
    "v_mfma_f32_16x16x32_bf16 v[20:23], a[248:251], a[104:107], v[20:23]\n\t"
    "v_exp_f32 v10, v10\n\t"
    "v_mfma_f32_16x16x32_bf16 v[20:23], a[244:247], a[76:79], v[20:23]\n\t"
    "ds_read_b128 a[228:231], v224 offset:38912\n\t"
    "v_cvt_pk_bf16_f32 v8, v8, v9\n\t"
    "v_accvgpr_write_b32 a186, v8\n\t"
    "v_mfma_f32_16x16x32_bf16 v[20:23], a[240:243], a[72:75], v[20:23]\n\t"
    "v_exp_f32 v11, v11\n\t"
    "v_mfma_f32_16x16x32_bf16 v[24:27], a[240:243], a[80:83], v[24:27]\n\t"
    "v_fma_f32 v12, v12, s72, v243\n\t"
    "v_fma_f32 v13, v13, s72, v243\n\t"
    "v_mfma_f32_16x16x32_bf16 v[24:27], a[244:247], a[84:87], v[24:27]\n\t"
    "v_exp_f32 v12, v12\n\t"
    "v_mfma_f32_16x16x32_bf16 v[24:27], a[248:251], a[112:115], v[24:27]\n\t"
    "ds_read_b128 a[224:227], v225 offset:38912\n\t"
    "v_cvt_pk_bf16_f32 v10, v10, v11\n\t"
    "v_accvgpr_write_b32 a187, v10\n\t"
    "v_mfma_f32_16x16x32_bf16 v[24:27], a[252:255], a[116:119], v[24:27]\n\t"
    "v_exp_f32 v13, v13\n\t"
    "v_mfma_f32_16x16x32_bf16 v[28:31], a[252:255], a[124:127], v[28:31]\n\t"
    "v_fma_f32 v14, v14, s72, v243\n\t"
    "v_fma_f32 v15, v15, s72, v243\n\t"
    "v_mfma_f32_16x16x32_bf16 v[28:31], a[248:251], a[120:123], v[28:31]\n\t"
    "v_exp_f32 v14, v14\n\t"
    "v_mfma_f32_16x16x32_bf16 v[28:31], a[244:247], a[92:95], v[28:31]\n\t"
    "ds_read_b128 a[252:255], v224 offset:43008\n\t"
    "v_cvt_pk_bf16_f32 v12, v12, v13\n\t"
    "v_accvgpr_write_b32 a190, v12\n\t"
    "v_mfma_f32_16x16x32_bf16 v[28:31], a[240:243], a[88:91], v[28:31]\n\t"
    "s_waitcnt lgkmcnt(2)\n\t"
    "v_exp_f32 v15, v15\n\t"
    "v_mfma_f32_16x16x32_bf16 v[252:255], v[52:55], a[180:183], v[252:255]\n\t"
    "buffer_load_dwordx4 v232, s[40:43], s71 offen offset:2048 nt lds\n\t"
    "v_mfma_f32_16x16x32_bf16 v[32:35], a[236:239], a[0:3], 0\n\t"
    "v_fma_f32 v16, v16, s72, v240\n\t"
    "v_fma_f32 v17, v17, s72, v240\n\t"
    "v_mfma_f32_16x16x32_bf16 v[32:35], a[232:235], a[4:7], v[32:35]\n\t"
    "s_waitcnt lgkmcnt(1)\n\t"
    "v_exp_f32 v16, v16\n\t"
    "v_mfma_f32_16x16x32_bf16 v[32:35], a[228:231], a[32:35], v[32:35]\n\t"
    "ds_read_b128 a[248:251], v225 offset:43008\n\t"
    "v_cvt_pk_bf16_f32 v14, v14, v15\n\t"
    "v_accvgpr_write_b32 a191, v14\n\t"
    "v_mfma_f32_16x16x32_bf16 v[32:35], a[224:227], a[36:39], v[32:35]\n\t"
    "v_exp_f32 v17, v17\n\t"
    "v_mfma_f32_16x16x32_bf16 v[36:39], a[224:227], a[44:47], 0\n\t"
    "v_fma_f32 v18, v18, s72, v240\n\t"
    "v_fma_f32 v19, v19, s72, v240\n\t"
    "v_mfma_f32_16x16x32_bf16 v[36:39], a[228:231], a[40:43], v[36:39]\n\t"
    "v_exp_f32 v18, v18\n\t"
    "v_mfma_f32_16x16x32_bf16 v[36:39], a[232:235], a[12:15], v[36:39]\n\t"
    "ds_read_b128 a[244:247], v224 offset:47104\n\t"
    "buffer_load_dwordx4 v233, s[40:43], s71 offen offset:3072 nt lds\n\t"
    "v_cvt_pk_bf16_f32 v16, v16, v17\n\t"
    "v_accvgpr_write_b32 a192, v16\n\t"
    "v_mfma_f32_16x16x32_bf16 v[36:39], a[236:239], a[8:11], v[36:39]\n\t"
    "v_exp_f32 v19, v19\n\t"
    "v_mfma_f32_16x16x32_bf16 v[40:43], a[236:239], a[16:19], 0\n\t"
    "v_fma_f32 v20, v20, s72, v241\n\t"
    "v_fma_f32 v21, v21, s72, v241\n\t"
    "v_mfma_f32_16x16x32_bf16 v[40:43], a[232:235], a[20:23], v[40:43]\n\t"
    "v_exp_f32 v20, v20\n\t"
    "v_mfma_f32_16x16x32_bf16 v[40:43], a[228:231], a[48:51], v[40:43]\n\t"
    "ds_read_b128 a[240:243], v225 offset:47104\n\t"
    "v_cvt_pk_bf16_f32 v18, v18, v19\n\t"
    "v_accvgpr_write_b32 a193, v18\n\t"
    "v_mfma_f32_16x16x32_bf16 v[40:43], a[224:227], a[52:55], v[40:43]\n\t"
    "v_exp_f32 v21, v21\n\t"
    "v_mfma_f32_16x16x32_bf16 v[44:47], a[224:227], a[60:63], 0\n\t"
    "s_xor_b32 s71, s67, 0x0\n\t"
    "v_fma_f32 v22, v22, s72, v241\n\t"
    "v_fma_f32 v23, v23, s72, v241\n\t"
    "v_mfma_f32_16x16x32_bf16 v[44:47], a[228:231], a[56:59], v[44:47]\n\t"
    "s_add_u32 m0, s70, 0x4000\n\t"
    "v_exp_f32 v22, v22\n\t"
    "v_mfma_f32_16x16x32_bf16 v[44:47], a[232:235], a[28:31], v[44:47]\n\t"
    "v_cvt_pk_bf16_f32 v20, v20, v21\n\t"
    "v_accvgpr_write_b32 a196, v20\n\t"
    "v_mfma_f32_16x16x32_bf16 v[44:47], a[236:239], a[24:27], v[44:47]\n\t"
    "s_waitcnt lgkmcnt(1)\n\t"
    "v_exp_f32 v23, v23\n\t"
    "v_mfma_f32_16x16x32_bf16 v[252:255], v[56:59], a[184:187], v[252:255]\n\t"
    "buffer_load_dwordx4 v232, s[52:55], s71 offen offset:0 lds\n\t"
    "v_mfma_f32_16x16x32_bf16 v[32:35], a[252:255], a[64:67], v[32:35]\n\t"
    "v_fma_f32 v24, v24, s72, v242\n\t"
    "v_fma_f32 v25, v25, s72, v242\n\t"
    "v_mfma_f32_16x16x32_bf16 v[32:35], a[248:251], a[68:71], v[32:35]\n\t"
    "s_waitcnt lgkmcnt(0)\n\t"
    "v_exp_f32 v24, v24\n\t"
    "v_mfma_f32_16x16x32_bf16 v[32:35], a[244:247], a[96:99], v[32:35]\n\t"
    "s_waitcnt vmcnt(13)\n\t"
    "v_cvt_pk_bf16_f32 v22, v22, v23\n\t"
    "v_accvgpr_write_b32 a197, v22\n\t"
    "v_mfma_f32_16x16x32_bf16 v[32:35], a[240:243], a[100:103], v[32:35]\n\t"
    "s_barrier\n\t"
    "v_exp_f32 v25, v25\n\t"
    "v_mfma_f32_16x16x32_bf16 v[36:39], a[240:243], a[108:111], v[36:39]\n\t"
    "ds_read_b128 a[224:227], v224 offset:49152\n\t"
    "v_fma_f32 v26, v26, s72, v242\n\t"
    "v_fma_f32 v27, v27, s72, v242\n\t"
    "v_mfma_f32_16x16x32_bf16 v[36:39], a[244:247], a[104:107], v[36:39]\n\t"
    "ds_read_b128 a[228:231], v225 offset:49152\n\t"
    "v_exp_f32 v26, v26\n\t"
    "v_mfma_f32_16x16x32_bf16 v[36:39], a[248:251], a[76:79], v[36:39]\n\t"
    "ds_read_b128 a[232:235], v224 offset:53248\n\t"
    "v_cvt_pk_bf16_f32 v24, v24, v25\n\t"
    "v_accvgpr_write_b32 a200, v24\n\t"
    "v_mfma_f32_16x16x32_bf16 v[36:39], a[252:255], a[72:75], v[36:39]\n\t"
    "v_exp_f32 v27, v27\n\t"
    "v_mfma_f32_16x16x32_bf16 v[40:43], a[252:255], a[80:83], v[40:43]\n\t"
    "v_fma_f32 v28, v28, s72, v243\n\t"
    "v_fma_f32 v29, v29, s72, v243\n\t"
    "v_mfma_f32_16x16x32_bf16 v[40:43], a[248:251], a[84:87], v[40:43]\n\t"
    "v_exp_f32 v28, v28\n\t"
    "v_mfma_f32_16x16x32_bf16 v[40:43], a[244:247], a[112:115], v[40:43]\n\t"
    "ds_read_b128 a[236:239], v225 offset:53248\n\t"
    "v_cvt_pk_bf16_f32 v26, v26, v27\n\t"
    "v_accvgpr_write_b32 a201, v26\n\t"
    "v_mfma_f32_16x16x32_bf16 v[40:43], a[240:243], a[116:119], v[40:43]\n\t"
    "v_exp_f32 v29, v29\n\t"
    "v_mfma_f32_16x16x32_bf16 v[44:47], a[240:243], a[124:127], v[44:47]\n\t"
    "v_fma_f32 v30, v30, s72, v243\n\t"
    "v_fma_f32 v31, v31, s72, v243\n\t"
    "v_mfma_f32_16x16x32_bf16 v[44:47], a[244:247], a[120:123], v[44:47]\n\t"
    "v_exp_f32 v30, v30\n\t"
    "v_mfma_f32_16x16x32_bf16 v[44:47], a[248:251], a[92:95], v[44:47]\n\t"
    "ds_read_b128 a[240:243], v224 offset:57344\n\t"
    "v_cvt_pk_bf16_f32 v28, v28, v29\n\t"
    "v_accvgpr_write_b32 a204, v28\n\t"
    "v_mfma_f32_16x16x32_bf16 v[44:47], a[252:255], a[88:91], v[44:47]\n\t"
    "s_waitcnt lgkmcnt(2)\n\t"
    "v_exp_f32 v31, v31\n\t"
    "v_mfma_f32_16x16x32_bf16 v[252:255], v[60:63], a[188:191], v[252:255]\n\t"
    "buffer_load_dwordx4 v233, s[52:55], s71 offen offset:1024 lds\n\t"
    "v_mfma_f32_16x16x32_bf16 v[0:3], a[224:227], a[0:3], 0\n\t"
    "v_fma_f32 v32, v32, s72, v240\n\t"
    "v_fma_f32 v33, v33, s72, v240\n\t"
    "v_mfma_f32_16x16x32_bf16 v[0:3], a[228:231], a[4:7], v[0:3]\n\t"
    "s_waitcnt lgkmcnt(1)\n\t"
    "v_exp_f32 v32, v32\n\t"
    "v_mfma_f32_16x16x32_bf16 v[0:3], a[232:235], a[32:35], v[0:3]\n\t"
    "ds_read_b128 a[244:247], v225 offset:57344\n\t"
    "v_cvt_pk_bf16_f32 v30, v30, v31\n\t"
    "v_accvgpr_write_b32 a205, v30\n\t"
    "v_mfma_f32_16x16x32_bf16 v[0:3], a[236:239], a[36:39], v[0:3]\n\t"
    "v_exp_f32 v33, v33\n\t"
    "v_mfma_f32_16x16x32_bf16 v[4:7], a[236:239], a[44:47], 0\n\t"
    "v_fma_f32 v34, v34, s72, v240\n\t"
    "v_fma_f32 v35, v35, s72, v240\n\t"
    "v_mfma_f32_16x16x32_bf16 v[4:7], a[232:235], a[40:43], v[4:7]\n\t"
    "v_exp_f32 v34, v34\n\t"
    "v_mfma_f32_16x16x32_bf16 v[4:7], a[228:231], a[12:15], v[4:7]\n\t"
    "ds_read_b128 a[248:251], v224 offset:61440\n\t"
    "buffer_load_dwordx4 v232, s[52:55], s71 offen offset:2048 lds\n\t"
    "v_cvt_pk_bf16_f32 v32, v32, v33\n\t"
    "v_accvgpr_write_b32 a194, v32\n\t"
    "v_mfma_f32_16x16x32_bf16 v[4:7], a[224:227], a[8:11], v[4:7]\n\t"
    "v_exp_f32 v35, v35\n\t"
    "v_mfma_f32_16x16x32_bf16 v[8:11], a[224:227], a[16:19], 0\n\t"
    "v_fma_f32 v36, v36, s72, v241\n\t"
    "v_fma_f32 v37, v37, s72, v241\n\t"
    "v_mfma_f32_16x16x32_bf16 v[8:11], a[228:231], a[20:23], v[8:11]\n\t"
    "v_exp_f32 v36, v36\n\t"
    "v_mfma_f32_16x16x32_bf16 v[8:11], a[232:235], a[48:51], v[8:11]\n\t"
    "ds_read_b128 a[252:255], v225 offset:61440\n\t"
    "v_cvt_pk_bf16_f32 v34, v34, v35\n\t"
    "v_accvgpr_write_b32 a195, v34\n\t"
    "v_mfma_f32_16x16x32_bf16 v[8:11], a[236:239], a[52:55], v[8:11]\n\t"
    "v_exp_f32 v37, v37\n\t"
    "v_mfma_f32_16x16x32_bf16 v[12:15], a[236:239], a[60:63], 0\n\t"
    "v_fma_f32 v38, v38, s72, v241\n\t"
    "v_fma_f32 v39, v39, s72, v241\n\t"
    "v_mfma_f32_16x16x32_bf16 v[12:15], a[232:235], a[56:59], v[12:15]\n\t"
    "v_exp_f32 v38, v38\n\t"
    "v_mfma_f32_16x16x32_bf16 v[12:15], a[228:231], a[28:31], v[12:15]\n\t"
    "ds_read_b128 a[236:239], v224 offset:51200\n\t"
    "v_cvt_pk_bf16_f32 v36, v36, v37\n\t"
    "v_accvgpr_write_b32 a198, v36\n\t"
    "v_mfma_f32_16x16x32_bf16 v[12:15], a[224:227], a[24:27], v[12:15]\n\t"
    "s_waitcnt lgkmcnt(2)\n\t"
    "v_exp_f32 v39, v39\n\t"
    "v_mfma_f32_16x16x32_bf16 v[252:255], v[48:51], a[192:195], v[252:255]\n\t"
    "buffer_load_dwordx4 v233, s[52:55], s71 offen offset:3072 lds\n\t"
    "v_mfma_f32_16x16x32_bf16 v[0:3], a[240:243], a[64:67], v[0:3]\n\t"
    "v_fma_f32 v40, v40, s72, v242\n\t"
    "v_fma_f32 v41, v41, s72, v242\n\t"
    "v_mfma_f32_16x16x32_bf16 v[0:3], a[244:247], a[68:71], v[0:3]\n\t"
    "s_waitcnt lgkmcnt(1)\n\t"
    "v_exp_f32 v40, v40\n\t"
    "v_mfma_f32_16x16x32_bf16 v[0:3], a[248:251], a[96:99], v[0:3]\n\t"
    "ds_read_b128 a[232:235], v225 offset:51200\n\t"
    "v_cvt_pk_bf16_f32 v38, v38, v39\n\t"
    "v_accvgpr_write_b32 a199, v38\n\t"
    "v_mfma_f32_16x16x32_bf16 v[0:3], a[252:255], a[100:103], v[0:3]\n\t"
    "v_exp_f32 v41, v41\n\t"
    "v_mfma_f32_16x16x32_bf16 v[4:7], a[252:255], a[108:111], v[4:7]\n\t"
    "v_fma_f32 v42, v42, s72, v242\n\t"
    "v_fma_f32 v43, v43, s72, v242\n\t"
    "v_mfma_f32_16x16x32_bf16 v[4:7], a[248:251], a[104:107], v[4:7]\n\t"
    "v_exp_f32 v42, v42\n\t"
    "v_mfma_f32_16x16x32_bf16 v[4:7], a[244:247], a[76:79], v[4:7]\n\t"
    "ds_read_b128 a[228:231], v224 offset:55296\n\t"
    "v_cvt_pk_bf16_f32 v40, v40, v41\n\t"
    "v_accvgpr_write_b32 a202, v40\n\t"
    "v_mfma_f32_16x16x32_bf16 v[4:7], a[240:243], a[72:75], v[4:7]\n\t"
    "v_exp_f32 v43, v43\n\t"
    "v_mfma_f32_16x16x32_bf16 v[8:11], a[240:243], a[80:83], v[8:11]\n\t"
    "v_fma_f32 v44, v44, s72, v243\n\t"
    "v_fma_f32 v45, v45, s72, v243\n\t"
    "v_mfma_f32_16x16x32_bf16 v[8:11], a[244:247], a[84:87], v[8:11]\n\t"
    "v_exp_f32 v44, v44\n\t"
    "v_mfma_f32_16x16x32_bf16 v[8:11], a[248:251], a[112:115], v[8:11]\n\t"
    "ds_read_b128 a[224:227], v225 offset:55296\n\t"
    "v_cvt_pk_bf16_f32 v42, v42, v43\n\t"
    "v_accvgpr_write_b32 a203, v42\n\t"
    "v_mfma_f32_16x16x32_bf16 v[8:11], a[252:255], a[116:119], v[8:11]\n\t"
    "s_xor_b32 s71, s64, 0x2c000\n\t"
    "v_exp_f32 v45, v45\n\t"
    "v_mfma_f32_16x16x32_bf16 v[12:15], a[252:255], a[124:127], v[12:15]\n\t"
    "s_add_u32 m0, s70, 0x8000\n\t"
    "v_fma_f32 v46, v46, s72, v243\n\t"
    "v_fma_f32 v47, v47, s72, v243\n\t"
    "v_mfma_f32_16x16x32_bf16 v[12:15], a[248:251], a[120:123], v[12:15]\n\t"
    "v_exp_f32 v46, v46\n\t"
    "v_mfma_f32_16x16x32_bf16 v[12:15], a[244:247], a[92:95], v[12:15]\n\t"
    "ds_read_b128 a[252:255], v224 offset:59392\n\t"
    "v_cvt_pk_bf16_f32 v44, v44, v45\n\t"
    "v_accvgpr_write_b32 a206, v44\n\t"
    "v_mfma_f32_16x16x32_bf16 v[12:15], a[240:243], a[88:91], v[12:15]\n\t"
    "s_waitcnt lgkmcnt(2)\n\t"
    "v_exp_f32 v47, v47\n\t"
    "v_mfma_f32_16x16x32_bf16 v[252:255], v[52:55], a[196:199], v[252:255]\n\t"
    "buffer_load_dwordx4 v232, s[40:43], s71 offen offset:0 nt lds\n\t"
    "v_mfma_f32_16x16x32_bf16 v[16:19], a[236:239], a[0:3], 0\n\t"
    "v_fma_f32 v0, v0, s72, v240\n\t"
    "v_fma_f32 v1, v1, s72, v240\n\t"
    "v_mfma_f32_16x16x32_bf16 v[16:19], a[232:235], a[4:7], v[16:19]\n\t"
    "s_waitcnt lgkmcnt(1)\n\t"
    "v_exp_f32 v0, v0\n\t"
    "v_mfma_f32_16x16x32_bf16 v[16:19], a[228:231], a[32:35], v[16:19]\n\t"
    "ds_read_b128 a[248:251], v225 offset:59392\n\t"
    "v_cvt_pk_bf16_f32 v46, v46, v47\n\t"
    "v_accvgpr_write_b32 a207, v46\n\t"
    "v_mfma_f32_16x16x32_bf16 v[16:19], a[224:227], a[36:39], v[16:19]\n\t"
    "v_exp_f32 v1, v1\n\t"
    "v_mfma_f32_16x16x32_bf16 v[20:23], a[224:227], a[44:47], 0\n\t"
    "v_fma_f32 v2, v2, s72, v240\n\t"
    "v_fma_f32 v3, v3, s72, v240\n\t"
    "v_mfma_f32_16x16x32_bf16 v[20:23], a[228:231], a[40:43], v[20:23]\n\t"
    "v_exp_f32 v2, v2\n\t"
    "v_mfma_f32_16x16x32_bf16 v[20:23], a[232:235], a[12:15], v[20:23]\n\t"
    "ds_read_b128 a[244:247], v224 offset:63488\n\t"
    "buffer_load_dwordx4 v233, s[40:43], s71 offen offset:1024 nt lds\n\t"
    "v_cvt_pk_bf16_f32 v0, v0, v1\n\t"
    "v_accvgpr_write_b32 a208, v0\n\t"
    "v_mfma_f32_16x16x32_bf16 v[20:23], a[236:239], a[8:11], v[20:23]\n\t"
    "v_exp_f32 v3, v3\n\t"
    "v_mfma_f32_16x16x32_bf16 v[24:27], a[236:239], a[16:19], 0\n\t"
    "v_fma_f32 v4, v4, s72, v241\n\t"
    "v_fma_f32 v5, v5, s72, v241\n\t"
    "v_mfma_f32_16x16x32_bf16 v[24:27], a[232:235], a[20:23], v[24:27]\n\t"
    "v_exp_f32 v4, v4\n\t"
    "v_mfma_f32_16x16x32_bf16 v[24:27], a[228:231], a[48:51], v[24:27]\n\t"
    "ds_read_b128 a[240:243], v225 offset:63488\n\t"
    "v_cvt_pk_bf16_f32 v2, v2, v3\n\t"
    "v_accvgpr_write_b32 a209, v2\n\t"
    "v_mfma_f32_16x16x32_bf16 v[24:27], a[224:227], a[52:55], v[24:27]\n\t"
    "v_exp_f32 v5, v5\n\t"
    "v_mfma_f32_16x16x32_bf16 v[28:31], a[224:227], a[60:63], 0\n\t"
    "v_fma_f32 v6, v6, s72, v241\n\t"
    "v_fma_f32 v7, v7, s72, v241\n\t"
    "v_mfma_f32_16x16x32_bf16 v[28:31], a[228:231], a[56:59], v[28:31]\n\t"
    "v_exp_f32 v6, v6\n\t"
    "v_mfma_f32_16x16x32_bf16 v[28:31], a[232:235], a[28:31], v[28:31]\n\t"
    "v_cvt_pk_bf16_f32 v4, v4, v5\n\t"
    "v_accvgpr_write_b32 a212, v4\n\t"
    "v_mfma_f32_16x16x32_bf16 v[28:31], a[236:239], a[24:27], v[28:31]\n\t"
    "s_waitcnt lgkmcnt(1)\n\t"
    "v_exp_f32 v7, v7\n\t"
    "v_mfma_f32_16x16x32_bf16 v[252:255], v[56:59], a[200:203], v[252:255]\n\t"
    "buffer_load_dwordx4 v232, s[40:43], s71 offen offset:2048 nt lds\n\t"
    "v_mfma_f32_16x16x32_bf16 v[16:19], a[252:255], a[64:67], v[16:19]\n\t"
    "v_fma_f32 v8, v8, s72, v242\n\t"
    "v_fma_f32 v9, v9, s72, v242\n\t"
    "v_mfma_f32_16x16x32_bf16 v[16:19], a[248:251], a[68:71], v[16:19]\n\t"
    "s_waitcnt lgkmcnt(0)\n\t"
    "v_exp_f32 v8, v8\n\t"
    "v_mfma_f32_16x16x32_bf16 v[16:19], a[244:247], a[96:99], v[16:19]\n\t"
    "s_waitcnt vmcnt(15)\n\t"
    "v_cvt_pk_bf16_f32 v6, v6, v7\n\t"
    "v_accvgpr_write_b32 a213, v6\n\t"
    "v_mfma_f32_16x16x32_bf16 v[16:19], a[240:243], a[100:103], v[16:19]\n\t"
    "s_barrier\n\t"
    "v_exp_f32 v9, v9\n\t"
    "v_add_u32 v224, 0x10000, v224\n\t"
    "v_mfma_f32_16x16x32_bf16 v[20:23], a[240:243], a[108:111], v[20:23]\n\t"
    "ds_read_b128 a[224:227], v224 offset:0\n\t"
    "v_fma_f32 v10, v10, s72, v242\n\t"
    "v_fma_f32 v11, v11, s72, v242\n\t"
    "v_add_u32 v225, 0x10000, v225\n\t"
    "v_mfma_f32_16x16x32_bf16 v[20:23], a[244:247], a[104:107], v[20:23]\n\t"
    "ds_read_b128 a[228:231], v225 offset:0\n\t"
    "v_exp_f32 v10, v10\n\t"
    "v_mfma_f32_16x16x32_bf16 v[20:23], a[248:251], a[76:79], v[20:23]\n\t"
    "ds_read_b128 a[232:235], v224 offset:4096\n\t"
    "v_cvt_pk_bf16_f32 v8, v8, v9\n\t"
    "v_accvgpr_write_b32 a216, v8\n\t"
    "v_mfma_f32_16x16x32_bf16 v[20:23], a[252:255], a[72:75], v[20:23]\n\t"
    "v_exp_f32 v11, v11\n\t"
    "v_mfma_f32_16x16x32_bf16 v[24:27], a[252:255], a[80:83], v[24:27]\n\t"
    "v_fma_f32 v12, v12, s72, v243\n\t"
    "v_fma_f32 v13, v13, s72, v243\n\t"
    "v_mfma_f32_16x16x32_bf16 v[24:27], a[248:251], a[84:87], v[24:27]\n\t"
    "v_exp_f32 v12, v12\n\t"
    "v_mfma_f32_16x16x32_bf16 v[24:27], a[244:247], a[112:115], v[24:27]\n\t"
    "ds_read_b128 a[236:239], v225 offset:4096\n\t"
    "v_cvt_pk_bf16_f32 v10, v10, v11\n\t"
    "v_accvgpr_write_b32 a217, v10\n\t"
    "v_mfma_f32_16x16x32_bf16 v[24:27], a[240:243], a[116:119], v[24:27]\n\t"
    "v_exp_f32 v13, v13\n\t"
    "v_mfma_f32_16x16x32_bf16 v[28:31], a[240:243], a[124:127], v[28:31]\n\t"
    "v_fma_f32 v14, v14, s72, v243\n\t"
    "v_fma_f32 v15, v15, s72, v243\n\t"
    "v_mfma_f32_16x16x32_bf16 v[28:31], a[244:247], a[120:123], v[28:31]\n\t"
    "v_exp_f32 v14, v14\n\t"
    "v_mfma_f32_16x16x32_bf16 v[28:31], a[248:251], a[92:95], v[28:31]\n\t"
    "ds_read_b128 a[240:243], v224 offset:8192\n\t"
    "v_cvt_pk_bf16_f32 v12, v12, v13\n\t"
    "v_accvgpr_write_b32 a220, v12\n\t"
    "v_mfma_f32_16x16x32_bf16 v[28:31], a[252:255], a[88:91], v[28:31]\n\t"
    "s_waitcnt lgkmcnt(2)\n\t"
    "v_exp_f32 v15, v15\n\t"
    "v_mfma_f32_16x16x32_bf16 v[252:255], v[60:63], a[204:207], v[252:255]\n\t"
    "buffer_load_dwordx4 v233, s[40:43], s71 offen offset:3072 nt lds\n\t"
    "v_mfma_f32_16x16x32_bf16 v[32:35], a[224:227], a[0:3], 0\n\t"
    "v_fma_f32 v16, v16, s72, v240\n\t"
    "v_fma_f32 v17, v17, s72, v240\n\t"
    "v_mfma_f32_16x16x32_bf16 v[32:35], a[228:231], a[4:7], v[32:35]\n\t"
    "s_waitcnt lgkmcnt(1)\n\t"
    "v_exp_f32 v16, v16\n\t"
    "v_mfma_f32_16x16x32_bf16 v[32:35], a[232:235], a[32:35], v[32:35]\n\t"
    "ds_read_b128 a[244:247], v225 offset:8192\n\t"
    "v_cvt_pk_bf16_f32 v14, v14, v15\n\t"
    "v_accvgpr_write_b32 a221, v14\n\t"
    "v_mfma_f32_16x16x32_bf16 v[32:35], a[236:239], a[36:39], v[32:35]\n\t"
    "s_xor_b32 s71, s67, 0x4000\n\t"
    "v_exp_f32 v17, v17\n\t"
    "v_mfma_f32_16x16x32_bf16 v[36:39], a[236:239], a[44:47], 0\n\t"
    "s_add_u32 m0, s70, 0xc000\n\t"
    "v_fma_f32 v18, v18, s72, v240\n\t"
    "v_fma_f32 v19, v19, s72, v240\n\t"
    "v_mfma_f32_16x16x32_bf16 v[36:39], a[232:235], a[40:43], v[36:39]\n\t"
    "v_exp_f32 v18, v18\n\t"
    "v_mfma_f32_16x16x32_bf16 v[36:39], a[228:231], a[12:15], v[36:39]\n\t"
    "ds_read_b128 a[248:251], v224 offset:12288\n\t"
    "buffer_load_dwordx4 v232, s[52:55], s71 offen offset:0 lds\n\t"
    "v_cvt_pk_bf16_f32 v16, v16, v17\n\t"
    "v_accvgpr_write_b32 a210, v16\n\t"
    "v_mfma_f32_16x16x32_bf16 v[36:39], a[224:227], a[8:11], v[36:39]\n\t"
    "v_exp_f32 v19, v19\n\t"
    "v_mfma_f32_16x16x32_bf16 v[40:43], a[224:227], a[16:19], 0\n\t"
    "v_fma_f32 v20, v20, s72, v241\n\t"
    "v_fma_f32 v21, v21, s72, v241\n\t"
    "v_mfma_f32_16x16x32_bf16 v[40:43], a[228:231], a[20:23], v[40:43]\n\t"
    "v_exp_f32 v20, v20\n\t"
    "v_mfma_f32_16x16x32_bf16 v[40:43], a[232:235], a[48:51], v[40:43]\n\t"
    "ds_read_b128 a[252:255], v225 offset:12288\n\t"
    "v_cvt_pk_bf16_f32 v18, v18, v19\n\t"
    "v_accvgpr_write_b32 a211, v18\n\t"
    "v_mfma_f32_16x16x32_bf16 v[40:43], a[236:239], a[52:55], v[40:43]\n\t"
    "v_exp_f32 v21, v21\n\t"
    "v_mfma_f32_16x16x32_bf16 v[44:47], a[236:239], a[60:63], 0\n\t"
    "v_fma_f32 v22, v22, s72, v241\n\t"
    "v_fma_f32 v23, v23, s72, v241\n\t"
    "v_mfma_f32_16x16x32_bf16 v[44:47], a[232:235], a[56:59], v[44:47]\n\t"
    "v_exp_f32 v22, v22\n\t"
    "v_mfma_f32_16x16x32_bf16 v[44:47], a[228:231], a[28:31], v[44:47]\n\t"
    "ds_read_b128 a[236:239], v224 offset:2048\n\t"
    "v_cvt_pk_bf16_f32 v20, v20, v21\n\t"
    "v_accvgpr_write_b32 a214, v20\n\t"
    "v_mfma_f32_16x16x32_bf16 v[44:47], a[224:227], a[24:27], v[44:47]\n\t"
    "s_waitcnt lgkmcnt(2)\n\t"
    "v_exp_f32 v23, v23\n\t"
    "v_mfma_f32_16x16x32_bf16 v[252:255], v[48:51], a[208:211], v[252:255]\n\t"
    "buffer_load_dwordx4 v233, s[52:55], s71 offen offset:1024 lds\n\t"
    "v_mfma_f32_16x16x32_bf16 v[32:35], a[240:243], a[64:67], v[32:35]\n\t"
    "v_fma_f32 v24, v24, s72, v242\n\t"
    "v_fma_f32 v25, v25, s72, v242\n\t"
    "v_mfma_f32_16x16x32_bf16 v[32:35], a[244:247], a[68:71], v[32:35]\n\t"
    "s_waitcnt lgkmcnt(1)\n\t"
    "v_exp_f32 v24, v24\n\t"
    "v_mfma_f32_16x16x32_bf16 v[32:35], a[248:251], a[96:99], v[32:35]\n\t"
    "ds_read_b128 a[232:235], v225 offset:2048\n\t"
    "v_cvt_pk_bf16_f32 v22, v22, v23\n\t"
    "v_accvgpr_write_b32 a215, v22\n\t"
    "v_mfma_f32_16x16x32_bf16 v[32:35], a[252:255], a[100:103], v[32:35]\n\t"
    "v_exp_f32 v25, v25\n\t"
    "v_mfma_f32_16x16x32_bf16 v[36:39], a[252:255], a[108:111], v[36:39]\n\t"
    "v_fma_f32 v26, v26, s72, v242\n\t"
    "v_fma_f32 v27, v27, s72, v242\n\t"
    "v_mfma_f32_16x16x32_bf16 v[36:39], a[248:251], a[104:107], v[36:39]\n\t"
    "v_exp_f32 v26, v26\n\t"
    "v_mfma_f32_16x16x32_bf16 v[36:39], a[244:247], a[76:79], v[36:39]\n\t"
    "ds_read_b128 a[228:231], v224 offset:6144\n\t"
    "v_cvt_pk_bf16_f32 v24, v24, v25\n\t"
    "v_accvgpr_write_b32 a218, v24\n\t"
    "v_mfma_f32_16x16x32_bf16 v[36:39], a[240:243], a[72:75], v[36:39]\n\t"
    "v_exp_f32 v27, v27\n\t"
    "v_mfma_f32_16x16x32_bf16 v[40:43], a[240:243], a[80:83], v[40:43]\n\t"
    "v_fma_f32 v28, v28, s72, v243\n\t"
    "v_fma_f32 v29, v29, s72, v243\n\t"
    "v_mfma_f32_16x16x32_bf16 v[40:43], a[244:247], a[84:87], v[40:43]\n\t"
    "v_exp_f32 v28, v28\n\t"
    "v_mfma_f32_16x16x32_bf16 v[40:43], a[248:251], a[112:115], v[40:43]\n\t"
    "ds_read_b128 a[224:227], v225 offset:6144\n\t"
    "v_cvt_pk_bf16_f32 v26, v26, v27\n\t"
    "v_accvgpr_write_b32 a219, v26\n\t"
    "v_mfma_f32_16x16x32_bf16 v[40:43], a[252:255], a[116:119], v[40:43]\n\t"
    "v_exp_f32 v29, v29\n\t"
    "v_mfma_f32_16x16x32_bf16 v[44:47], a[252:255], a[124:127], v[44:47]\n\t"
    "v_fma_f32 v30, v30, s72, v243\n\t"
    "v_fma_f32 v31, v31, s72, v243\n\t"
    "v_mfma_f32_16x16x32_bf16 v[44:47], a[248:251], a[120:123], v[44:47]\n\t"
    "v_exp_f32 v30, v30\n\t"
    "v_mfma_f32_16x16x32_bf16 v[44:47], a[244:247], a[92:95], v[44:47]\n\t"
    "ds_read_b128 a[252:255], v224 offset:10240\n\t"
    "v_cvt_pk_bf16_f32 v28, v28, v29\n\t"
    "v_accvgpr_write_b32 a222, v28\n\t"
    "v_mfma_f32_16x16x32_bf16 v[44:47], a[240:243], a[88:91], v[44:47]\n\t"
    "s_waitcnt lgkmcnt(2)\n\t"
    "v_exp_f32 v31, v31\n\t"
    "v_mfma_f32_16x16x32_bf16 v[252:255], v[52:55], a[212:215], v[252:255]\n\t"
    "buffer_load_dwordx4 v232, s[52:55], s71 offen offset:2048 lds\n\t"
    "v_mfma_f32_16x16x32_bf16 v[0:3], a[236:239], a[0:3], 0\n\t"
    "v_fma_f32 v32, v32, s72, v240\n\t"
    "v_fma_f32 v33, v33, s72, v240\n\t"
    "v_mfma_f32_16x16x32_bf16 v[0:3], a[232:235], a[4:7], v[0:3]\n\t"
    "s_waitcnt lgkmcnt(1)\n\t"
    "v_exp_f32 v32, v32\n\t"
    "v_mfma_f32_16x16x32_bf16 v[0:3], a[228:231], a[32:35], v[0:3]\n\t"
    "ds_read_b128 a[248:251], v225 offset:10240\n\t"
    "v_cvt_pk_bf16_f32 v30, v30, v31\n\t"
    "v_accvgpr_write_b32 a223, v30\n\t"
    "v_mfma_f32_16x16x32_bf16 v[0:3], a[224:227], a[36:39], v[0:3]\n\t"
    "v_exp_f32 v33, v33\n\t"
    "v_mfma_f32_16x16x32_bf16 v[4:7], a[224:227], a[44:47], 0\n\t"
    "v_fma_f32 v34, v34, s72, v240\n\t"
    "v_fma_f32 v35, v35, s72, v240\n\t"
    "v_mfma_f32_16x16x32_bf16 v[4:7], a[228:231], a[40:43], v[4:7]\n\t"
    "v_exp_f32 v34, v34\n\t"
    "v_mfma_f32_16x16x32_bf16 v[4:7], a[232:235], a[12:15], v[4:7]\n\t"
    "ds_read_b128 a[244:247], v224 offset:14336\n\t"
    "buffer_load_dwordx4 v233, s[52:55], s71 offen offset:3072 lds\n\t"
    "v_cvt_pk_bf16_f32 v96, v32, v33\n\t"
    "v_mfma_f32_16x16x32_bf16 v[4:7], a[236:239], a[8:11], v[4:7]\n\t"
    "v_exp_f32 v35, v35\n\t"
    "v_mfma_f32_16x16x32_bf16 v[8:11], a[236:239], a[16:19], 0\n\t"
    "v_fma_f32 v36, v36, s72, v241\n\t"
    "v_fma_f32 v37, v37, s72, v241\n\t"
    "v_mfma_f32_16x16x32_bf16 v[8:11], a[232:235], a[20:23], v[8:11]\n\t"
    "v_exp_f32 v36, v36\n\t"
    "v_mfma_f32_16x16x32_bf16 v[8:11], a[228:231], a[48:51], v[8:11]\n\t"
    "ds_read_b128 a[240:243], v225 offset:14336\n\t"
    "v_cvt_pk_bf16_f32 v97, v34, v35\n\t"
    "v_mfma_f32_16x16x32_bf16 v[8:11], a[224:227], a[52:55], v[8:11]\n\t"
    "v_exp_f32 v37, v37\n\t"
    "v_mfma_f32_16x16x32_bf16 v[12:15], a[224:227], a[60:63], 0\n\t"
    "v_fma_f32 v38, v38, s72, v241\n\t"
    "v_fma_f32 v39, v39, s72, v241\n\t"
    "v_mfma_f32_16x16x32_bf16 v[12:15], a[228:231], a[56:59], v[12:15]\n\t"
    "v_exp_f32 v38, v38\n\t"
    "v_mfma_f32_16x16x32_bf16 v[12:15], a[232:235], a[28:31], v[12:15]\n\t"
    "s_xor_b32 s71, s64, 0x30000\n\t"
    "s_add_u32 m0, s70, 0x10000\n\t"
    "v_cvt_pk_bf16_f32 v100, v36, v37\n\t"
    "v_mfma_f32_16x16x32_bf16 v[12:15], a[236:239], a[24:27], v[12:15]\n\t"
    "s_waitcnt lgkmcnt(1)\n\t"
    "v_exp_f32 v39, v39\n\t"
    "v_mfma_f32_16x16x32_bf16 v[252:255], v[56:59], a[216:219], v[252:255]\n\t"
    "buffer_load_dwordx4 v232, s[40:43], s71 offen offset:0 nt lds\n\t"
    "v_mfma_f32_16x16x32_bf16 v[0:3], a[252:255], a[64:67], v[0:3]\n\t"
    "v_fma_f32 v40, v40, s72, v242\n\t"
    "v_fma_f32 v41, v41, s72, v242\n\t"
    "v_mfma_f32_16x16x32_bf16 v[0:3], a[248:251], a[68:71], v[0:3]\n\t"
    "s_waitcnt lgkmcnt(0)\n\t"
    "v_exp_f32 v40, v40\n\t"
    "v_mfma_f32_16x16x32_bf16 v[0:3], a[244:247], a[96:99], v[0:3]\n\t"
    "s_waitcnt vmcnt(17)\n\t"
    "v_cvt_pk_bf16_f32 v101, v38, v39\n\t"
    "v_mfma_f32_16x16x32_bf16 v[0:3], a[240:243], a[100:103], v[0:3]\n\t"
    "s_barrier\n\t"
    "v_exp_f32 v41, v41\n\t"
    "v_mfma_f32_16x16x32_bf16 v[4:7], a[240:243], a[108:111], v[4:7]\n\t"
    "ds_read_b128 a[224:227], v224 offset:16384\n\t"
    "v_fma_f32 v42, v42, s72, v242\n\t"
    "v_fma_f32 v43, v43, s72, v242\n\t"
    "v_mfma_f32_16x16x32_bf16 v[4:7], a[244:247], a[104:107], v[4:7]\n\t"
    "ds_read_b128 a[228:231], v225 offset:16384\n\t"
    "v_exp_f32 v42, v42\n\t"
    "v_mfma_f32_16x16x32_bf16 v[4:7], a[248:251], a[76:79], v[4:7]\n\t"
    "ds_read_b128 a[232:235], v224 offset:20480\n\t"
    "v_cvt_pk_bf16_f32 v104, v40, v41\n\t"
    "v_mfma_f32_16x16x32_bf16 v[4:7], a[252:255], a[72:75], v[4:7]\n\t"
    "v_exp_f32 v43, v43\n\t"
    "v_mfma_f32_16x16x32_bf16 v[8:11], a[252:255], a[80:83], v[8:11]\n\t"
    "v_fma_f32 v44, v44, s72, v243\n\t"
    "v_fma_f32 v45, v45, s72, v243\n\t"
    "v_mfma_f32_16x16x32_bf16 v[8:11], a[248:251], a[84:87], v[8:11]\n\t"
    "v_exp_f32 v44, v44\n\t"
    "v_mfma_f32_16x16x32_bf16 v[8:11], a[244:247], a[112:115], v[8:11]\n\t"
    "ds_read_b128 a[236:239], v225 offset:20480\n\t"
    "v_cvt_pk_bf16_f32 v105, v42, v43\n\t"
    "v_mfma_f32_16x16x32_bf16 v[8:11], a[240:243], a[116:119], v[8:11]\n\t"
    "v_exp_f32 v45, v45\n\t"
    "v_mfma_f32_16x16x32_bf16 v[12:15], a[240:243], a[124:127], v[12:15]\n\t"
    "v_fma_f32 v46, v46, s72, v243\n\t"
    "v_fma_f32 v47, v47, s72, v243\n\t"
    "v_mfma_f32_16x16x32_bf16 v[12:15], a[244:247], a[120:123], v[12:15]\n\t"
    "v_exp_f32 v46, v46\n\t"
    "v_mfma_f32_16x16x32_bf16 v[12:15], a[248:251], a[92:95], v[12:15]\n\t"
    "ds_read_b128 a[240:243], v224 offset:24576\n\t"
    "v_cvt_pk_bf16_f32 v108, v44, v45\n\t"
    "v_mfma_f32_16x16x32_bf16 v[12:15], a[252:255], a[88:91], v[12:15]\n\t"
    "s_waitcnt lgkmcnt(2)\n\t"
    "v_exp_f32 v47, v47\n\t"
    "v_mfma_f32_16x16x32_bf16 v[252:255], v[60:63], a[220:223], v[252:255]\n\t"
    "buffer_load_dwordx4 v233, s[40:43], s71 offen offset:1024 nt lds\n\t"
    "v_mfma_f32_16x16x32_bf16 v[16:19], a[224:227], a[0:3], 0\n\t"
    "v_fma_f32 v0, v0, s72, v240\n\t"
    "v_fma_f32 v1, v1, s72, v240\n\t"
    "v_mfma_f32_16x16x32_bf16 v[16:19], a[228:231], a[4:7], v[16:19]\n\t"
    "s_waitcnt lgkmcnt(1)\n\t"
    "v_exp_f32 v0, v0\n\t"
    "v_mfma_f32_16x16x32_bf16 v[16:19], a[232:235], a[32:35], v[16:19]\n\t"
    "ds_read_b128 a[244:247], v225 offset:24576\n\t"
    "v_cvt_pk_bf16_f32 v109, v46, v47\n\t"
    "v_mfma_f32_16x16x32_bf16 v[16:19], a[236:239], a[36:39], v[16:19]\n\t"
    "v_exp_f32 v1, v1\n\t"
    "v_mfma_f32_16x16x32_bf16 v[20:23], a[236:239], a[44:47], 0\n\t"
    "v_fma_f32 v2, v2, s72, v240\n\t"
    "v_fma_f32 v3, v3, s72, v240\n\t"
    "v_mfma_f32_16x16x32_bf16 v[20:23], a[232:235], a[40:43], v[20:23]\n\t"
    "v_exp_f32 v2, v2\n\t"
    "v_mfma_f32_16x16x32_bf16 v[20:23], a[228:231], a[12:15], v[20:23]\n\t"
    "ds_read_b128 a[248:251], v224 offset:28672\n\t"
    "buffer_load_dwordx4 v232, s[40:43], s71 offen offset:2048 nt lds\n\t"
    "v_cvt_pk_bf16_f32 v98, v0, v1\n\t"
    "v_mfma_f32_16x16x32_bf16 v[20:23], a[224:227], a[8:11], v[20:23]\n\t"
    "v_exp_f32 v3, v3\n\t"
    "v_mfma_f32_16x16x32_bf16 v[24:27], a[224:227], a[16:19], 0\n\t"
    "v_fma_f32 v4, v4, s72, v241\n\t"
    "v_fma_f32 v5, v5, s72, v241\n\t"
    "v_mfma_f32_16x16x32_bf16 v[24:27], a[228:231], a[20:23], v[24:27]\n\t"
    "v_exp_f32 v4, v4\n\t"
    "v_mfma_f32_16x16x32_bf16 v[24:27], a[232:235], a[48:51], v[24:27]\n\t"
    "ds_read_b128 a[252:255], v225 offset:28672\n\t"
    "v_cvt_pk_bf16_f32 v99, v2, v3\n\t"
    "v_mfma_f32_16x16x32_bf16 v[24:27], a[236:239], a[52:55], v[24:27]\n\t"
    "v_exp_f32 v5, v5\n\t"
    "v_mfma_f32_16x16x32_bf16 v[28:31], a[236:239], a[60:63], 0\n\t"
    "v_fma_f32 v6, v6, s72, v241\n\t"
    "v_fma_f32 v7, v7, s72, v241\n\t"
    "v_mfma_f32_16x16x32_bf16 v[28:31], a[232:235], a[56:59], v[28:31]\n\t"
    "v_exp_f32 v6, v6\n\t"
    "v_mfma_f32_16x16x32_bf16 v[28:31], a[228:231], a[28:31], v[28:31]\n\t"
    "ds_read_b128 a[236:239], v224 offset:18432\n\t"
    "v_cvt_pk_bf16_f32 v102, v4, v5\n\t"
    "v_mfma_f32_16x16x32_bf16 v[28:31], a[224:227], a[24:27], v[28:31]\n\t"
    "s_waitcnt lgkmcnt(2)\n\t"
    "v_exp_f32 v7, v7\n\t"
    "v_mfma_f32_16x16x32_bf16 v[252:255], v[48:51], v[96:99], v[252:255]\n\t"
    "buffer_load_dwordx4 v233, s[40:43], s71 offen offset:3072 nt lds\n\t"
    "v_mfma_f32_16x16x32_bf16 v[16:19], a[240:243], a[64:67], v[16:19]\n\t"
    "v_fma_f32 v8, v8, s72, v242\n\t"
    "v_fma_f32 v9, v9, s72, v242\n\t"
    "v_mfma_f32_16x16x32_bf16 v[16:19], a[244:247], a[68:71], v[16:19]\n\t"
    "s_waitcnt lgkmcnt(1)\n\t"
    "v_exp_f32 v8, v8\n\t"
    "v_mfma_f32_16x16x32_bf16 v[16:19], a[248:251], a[96:99], v[16:19]\n\t"
    "ds_read_b128 a[232:235], v225 offset:18432\n\t"
    "v_cvt_pk_bf16_f32 v103, v6, v7\n\t"
    "v_mfma_f32_16x16x32_bf16 v[16:19], a[252:255], a[100:103], v[16:19]\n\t"
    "v_exp_f32 v9, v9\n\t"
    "v_mfma_f32_16x16x32_bf16 v[20:23], a[252:255], a[108:111], v[20:23]\n\t"
    "v_fma_f32 v10, v10, s72, v242\n\t"
    "v_fma_f32 v11, v11, s72, v242\n\t"
    "v_mfma_f32_16x16x32_bf16 v[20:23], a[248:251], a[104:107], v[20:23]\n\t"
    "v_exp_f32 v10, v10\n\t"
    "v_mfma_f32_16x16x32_bf16 v[20:23], a[244:247], a[76:79], v[20:23]\n\t"
    "ds_read_b128 a[228:231], v224 offset:22528\n\t"
    "v_cvt_pk_bf16_f32 v106, v8, v9\n\t"
    "v_mfma_f32_16x16x32_bf16 v[20:23], a[240:243], a[72:75], v[20:23]\n\t"
    "v_exp_f32 v11, v11\n\t"
    "v_mfma_f32_16x16x32_bf16 v[24:27], a[240:243], a[80:83], v[24:27]\n\t"
    "v_fma_f32 v12, v12, s72, v243\n\t"
    "v_fma_f32 v13, v13, s72, v243\n\t"
    "v_mfma_f32_16x16x32_bf16 v[24:27], a[244:247], a[84:87], v[24:27]\n\t"
    "v_exp_f32 v12, v12\n\t"
    "v_mfma_f32_16x16x32_bf16 v[24:27], a[248:251], a[112:115], v[24:27]\n\t"
    "ds_read_b128 a[224:227], v225 offset:22528\n\t"
    "v_cvt_pk_bf16_f32 v107, v10, v11\n\t"
    "v_mfma_f32_16x16x32_bf16 v[24:27], a[252:255], a[116:119], v[24:27]\n\t"
    "s_xor_b32 s71, s67, 0x80\n\t"
    "v_exp_f32 v13, v13\n\t"
    "v_mfma_f32_16x16x32_bf16 v[28:31], a[252:255], a[124:127], v[28:31]\n\t"
    "s_add_u32 m0, s70, 0x20000\n\t"
    "v_fma_f32 v14, v14, s72, v243\n\t"
    "v_fma_f32 v15, v15, s72, v243\n\t"
    "v_mfma_f32_16x16x32_bf16 v[28:31], a[248:251], a[120:123], v[28:31]\n\t"
    "v_exp_f32 v14, v14\n\t"
    "v_mfma_f32_16x16x32_bf16 v[28:31], a[244:247], a[92:95], v[28:31]\n\t"
    "ds_read_b128 a[252:255], v224 offset:26624\n\t"
    "v_cvt_pk_bf16_f32 v110, v12, v13\n\t"
    "v_mfma_f32_16x16x32_bf16 v[28:31], a[240:243], a[88:91], v[28:31]\n\t"
    "s_waitcnt lgkmcnt(2)\n\t"
    "v_exp_f32 v15, v15\n\t"
    "v_mfma_f32_16x16x32_bf16 v[252:255], v[52:55], v[100:103], v[252:255]\n\t"
    "buffer_load_dwordx4 v232, s[52:55], s71 offen offset:0 lds\n\t"
    "v_mfma_f32_16x16x32_bf16 v[32:35], a[236:239], a[0:3], 0\n\t"
    "v_fma_f32 v16, v16, s72, v240\n\t"
    "v_fma_f32 v17, v17, s72, v240\n\t"
    "v_mfma_f32_16x16x32_bf16 v[32:35], a[232:235], a[4:7], v[32:35]\n\t"
    "s_waitcnt lgkmcnt(1)\n\t"
    "v_exp_f32 v16, v16\n\t"
    "v_mfma_f32_16x16x32_bf16 v[32:35], a[228:231], a[32:35], v[32:35]\n\t"
    "ds_read_b128 a[248:251], v225 offset:26624\n\t"
    "v_cvt_pk_bf16_f32 v111, v14, v15\n\t"
    "v_mfma_f32_16x16x32_bf16 v[32:35], a[224:227], a[36:39], v[32:35]\n\t"
    "v_exp_f32 v17, v17\n\t"
    "v_mfma_f32_16x16x32_bf16 v[36:39], a[224:227], a[44:47], 0\n\t"
    "v_fma_f32 v18, v18, s72, v240\n\t"
    "v_fma_f32 v19, v19, s72, v240\n\t"
    "v_mfma_f32_16x16x32_bf16 v[36:39], a[228:231], a[40:43], v[36:39]\n\t"
    "v_exp_f32 v18, v18\n\t"
    "v_mfma_f32_16x16x32_bf16 v[36:39], a[232:235], a[12:15], v[36:39]\n\t"
    "ds_read_b128 a[244:247], v224 offset:30720\n\t"
    "buffer_load_dwordx4 v233, s[52:55], s71 offen offset:1024 lds\n\t"
    "v_cvt_pk_bf16_f32 v112, v16, v17\n\t"
    "v_mfma_f32_16x16x32_bf16 v[36:39], a[236:239], a[8:11], v[36:39]\n\t"
    "v_exp_f32 v19, v19\n\t"
    "v_mfma_f32_16x16x32_bf16 v[40:43], a[236:239], a[16:19], 0\n\t"
    "v_fma_f32 v20, v20, s72, v241\n\t"
    "v_fma_f32 v21, v21, s72, v241\n\t"
    "v_mfma_f32_16x16x32_bf16 v[40:43], a[232:235], a[20:23], v[40:43]\n\t"
    "v_exp_f32 v20, v20\n\t"
    "v_mfma_f32_16x16x32_bf16 v[40:43], a[228:231], a[48:51], v[40:43]\n\t"
    "ds_read_b128 a[240:243], v225 offset:30720\n\t"
    "v_cvt_pk_bf16_f32 v113, v18, v19\n\t"
    "v_mfma_f32_16x16x32_bf16 v[40:43], a[224:227], a[52:55], v[40:43]\n\t"
    "v_exp_f32 v21, v21\n\t"
    "v_mfma_f32_16x16x32_bf16 v[44:47], a[224:227], a[60:63], 0\n\t"
    "v_fma_f32 v22, v22, s72, v241\n\t"
    "v_fma_f32 v23, v23, s72, v241\n\t"
    "v_mfma_f32_16x16x32_bf16 v[44:47], a[228:231], a[56:59], v[44:47]\n\t"
    "v_exp_f32 v22, v22\n\t"
    "v_mfma_f32_16x16x32_bf16 v[44:47], a[232:235], a[28:31], v[44:47]\n\t"
    "v_cvt_pk_bf16_f32 v116, v20, v21\n\t"
    "v_add_u32 v224, 0xfffe0000, v224\n\t"
    "v_mfma_f32_16x16x32_bf16 v[44:47], a[236:239], a[24:27], v[44:47]\n\t"
    "s_waitcnt lgkmcnt(1)\n\t"
    "v_exp_f32 v23, v23\n\t"
    "v_mfma_f32_16x16x32_bf16 v[252:255], v[56:59], v[104:107], v[252:255]\n\t"
    "buffer_load_dwordx4 v232, s[52:55], s71 offen offset:2048 lds\n\t"
    "v_mfma_f32_16x16x32_bf16 v[32:35], a[252:255], a[64:67], v[32:35]\n\t"
    "v_fma_f32 v24, v24, s72, v242\n\t"
    "v_fma_f32 v25, v25, s72, v242\n\t"
    "v_mfma_f32_16x16x32_bf16 v[32:35], a[248:251], a[68:71], v[32:35]\n\t"
    "s_waitcnt lgkmcnt(0)\n\t"
    "v_exp_f32 v24, v24\n\t"
    "v_mfma_f32_16x16x32_bf16 v[32:35], a[244:247], a[96:99], v[32:35]\n\t"
    "s_waitcnt vmcnt(19)\n\t"
    "v_cvt_pk_bf16_f32 v117, v22, v23\n\t"
    "v_add_u32 v225, 0xfffe0000, v225\n\t"
    "v_mfma_f32_16x16x32_bf16 v[32:35], a[240:243], a[100:103], v[32:35]\n\t"
    "s_barrier\n\t"
    "v_exp_f32 v25, v25\n\t"
    "v_mfma_f32_16x16x32_bf16 v[36:39], a[240:243], a[108:111], v[36:39]\n\t"
    "ds_read_b128 a[224:227], v224 offset:0\n\t"
    "v_fma_f32 v26, v26, s72, v242\n\t"
    "v_fma_f32 v27, v27, s72, v242\n\t"
    "v_mfma_f32_16x16x32_bf16 v[36:39], a[244:247], a[104:107], v[36:39]\n\t"
    "ds_read_b128 a[228:231], v225 offset:0\n\t"
    "v_exp_f32 v26, v26\n\t"
    "v_mfma_f32_16x16x32_bf16 v[36:39], a[248:251], a[76:79], v[36:39]\n\t"
    "ds_read_b128 a[232:235], v224 offset:4096\n\t"
    "v_cvt_pk_bf16_f32 v120, v24, v25\n\t"
    "v_mfma_f32_16x16x32_bf16 v[36:39], a[252:255], a[72:75], v[36:39]\n\t"
    "v_exp_f32 v27, v27\n\t"
    "v_mfma_f32_16x16x32_bf16 v[40:43], a[252:255], a[80:83], v[40:43]\n\t"
    "v_fma_f32 v28, v28, s72, v243\n\t"
    "v_fma_f32 v29, v29, s72, v243\n\t"
    "v_mfma_f32_16x16x32_bf16 v[40:43], a[248:251], a[84:87], v[40:43]\n\t"
    "v_exp_f32 v28, v28\n\t"
    "v_mfma_f32_16x16x32_bf16 v[40:43], a[244:247], a[112:115], v[40:43]\n\t"
    "ds_read_b128 a[236:239], v225 offset:4096\n\t"
    "v_cvt_pk_bf16_f32 v121, v26, v27\n\t"
    "v_mfma_f32_16x16x32_bf16 v[40:43], a[240:243], a[116:119], v[40:43]\n\t"
    "v_exp_f32 v29, v29\n\t"
    "v_mfma_f32_16x16x32_bf16 v[44:47], a[240:243], a[124:127], v[44:47]\n\t"
    "v_fma_f32 v30, v30, s72, v243\n\t"
    "v_fma_f32 v31, v31, s72, v243\n\t"
    "v_mfma_f32_16x16x32_bf16 v[44:47], a[244:247], a[120:123], v[44:47]\n\t"
    "v_exp_f32 v30, v30\n\t"
    "v_mfma_f32_16x16x32_bf16 v[44:47], a[248:251], a[92:95], v[44:47]\n\t"
    "ds_read_b128 a[240:243], v224 offset:8192\n\t"
    "v_cvt_pk_bf16_f32 v124, v28, v29\n\t"
    "v_mfma_f32_16x16x32_bf16 v[44:47], a[252:255], a[88:91], v[44:47]\n\t"
    "s_waitcnt lgkmcnt(2)\n\t"
    "v_exp_f32 v31, v31\n\t"
    "v_mfma_f32_16x16x32_bf16 v[252:255], v[60:63], v[108:111], v[252:255]\n\t"
    "buffer_load_dwordx4 v233, s[52:55], s71 offen offset:3072 lds\n\t"
    "v_mfma_f32_16x16x32_bf16 v[0:3], a[224:227], a[0:3], 0\n\t"
    "v_fma_f32 v32, v32, s72, v240\n\t"
    "v_fma_f32 v33, v33, s72, v240\n\t"
    "v_mfma_f32_16x16x32_bf16 v[0:3], a[228:231], a[4:7], v[0:3]\n\t"
    "s_waitcnt lgkmcnt(1)\n\t"
    "v_exp_f32 v32, v32\n\t"
    "v_mfma_f32_16x16x32_bf16 v[0:3], a[232:235], a[32:35], v[0:3]\n\t"
    "ds_read_b128 a[244:247], v225 offset:8192\n\t"
    "v_cvt_pk_bf16_f32 v125, v30, v31\n\t"
    "v_mfma_f32_16x16x32_bf16 v[0:3], a[236:239], a[36:39], v[0:3]\n\t"
    "s_xor_b32 s71, s64, 0x34000\n\t"
    "v_exp_f32 v33, v33\n\t"
    "v_mfma_f32_16x16x32_bf16 v[4:7], a[236:239], a[44:47], 0\n\t"
    "s_add_u32 m0, s70, 0x14000\n\t"
    "v_fma_f32 v34, v34, s72, v240\n\t"
    "v_fma_f32 v35, v35, s72, v240\n\t"
    "v_mfma_f32_16x16x32_bf16 v[4:7], a[232:235], a[40:43], v[4:7]\n\t"
    "v_exp_f32 v34, v34\n\t"
    "v_mfma_f32_16x16x32_bf16 v[4:7], a[228:231], a[12:15], v[4:7]\n\t"
    "ds_read_b128 a[248:251], v224 offset:12288\n\t"
    "buffer_load_dwordx4 v232, s[40:43], s71 offen offset:0 nt lds\n\t"
    "v_cvt_pk_bf16_f32 v114, v32, v33\n\t"
    "v_mfma_f32_16x16x32_bf16 v[4:7], a[224:227], a[8:11], v[4:7]\n\t"
    "v_exp_f32 v35, v35\n\t"
    "v_mfma_f32_16x16x32_bf16 v[8:11], a[224:227], a[16:19], 0\n\t"
    "v_fma_f32 v36, v36, s72, v241\n\t"
    "v_fma_f32 v37, v37, s72, v241\n\t"
    "v_mfma_f32_16x16x32_bf16 v[8:11], a[228:231], a[20:23], v[8:11]\n\t"
    "v_exp_f32 v36, v36\n\t"
    "v_mfma_f32_16x16x32_bf16 v[8:11], a[232:235], a[48:51], v[8:11]\n\t"
    "ds_read_b128 a[252:255], v225 offset:12288\n\t"
    "v_cvt_pk_bf16_f32 v115, v34, v35\n\t"
    "v_mfma_f32_16x16x32_bf16 v[8:11], a[236:239], a[52:55], v[8:11]\n\t"
    "v_exp_f32 v37, v37\n\t"
    "v_mfma_f32_16x16x32_bf16 v[12:15], a[236:239], a[60:63], 0\n\t"
    "v_fma_f32 v38, v38, s72, v241\n\t"
    "v_fma_f32 v39, v39, s72, v241\n\t"
    "v_mfma_f32_16x16x32_bf16 v[12:15], a[232:235], a[56:59], v[12:15]\n\t"
    "v_exp_f32 v38, v38\n\t"
    "v_mfma_f32_16x16x32_bf16 v[12:15], a[228:231], a[28:31], v[12:15]\n\t"
    "ds_read_b128 a[236:239], v224 offset:2048\n\t"
    "v_cvt_pk_bf16_f32 v118, v36, v37\n\t"
    "v_mfma_f32_16x16x32_bf16 v[12:15], a[224:227], a[24:27], v[12:15]\n\t"
    "s_waitcnt lgkmcnt(2)\n\t"
    "v_exp_f32 v39, v39\n\t"
    "v_mfma_f32_16x16x32_bf16 v[252:255], v[48:51], v[112:115], v[252:255]\n\t"
    "buffer_load_dwordx4 v233, s[40:43], s71 offen offset:1024 nt lds\n\t"
    "v_mfma_f32_16x16x32_bf16 v[0:3], a[240:243], a[64:67], v[0:3]\n\t"
    "v_fma_f32 v40, v40, s72, v242\n\t"
    "v_fma_f32 v41, v41, s72, v242\n\t"
    "v_mfma_f32_16x16x32_bf16 v[0:3], a[244:247], a[68:71], v[0:3]\n\t"
    "s_waitcnt lgkmcnt(1)\n\t"
    "v_exp_f32 v40, v40\n\t"
    "v_mfma_f32_16x16x32_bf16 v[0:3], a[248:251], a[96:99], v[0:3]\n\t"
    "ds_read_b128 a[232:235], v225 offset:2048\n\t"
    "v_cvt_pk_bf16_f32 v119, v38, v39\n\t"
    "v_mfma_f32_16x16x32_bf16 v[0:3], a[252:255], a[100:103], v[0:3]\n\t"
    "v_exp_f32 v41, v41\n\t"
    "v_mfma_f32_16x16x32_bf16 v[4:7], a[252:255], a[108:111], v[4:7]\n\t"
    "v_fma_f32 v42, v42, s72, v242\n\t"
    "v_fma_f32 v43, v43, s72, v242\n\t"
    "v_mfma_f32_16x16x32_bf16 v[4:7], a[248:251], a[104:107], v[4:7]\n\t"
    "v_exp_f32 v42, v42\n\t"
    "v_mfma_f32_16x16x32_bf16 v[4:7], a[244:247], a[76:79], v[4:7]\n\t"
    "ds_read_b128 a[228:231], v224 offset:6144\n\t"
    "v_cvt_pk_bf16_f32 v122, v40, v41\n\t"
    "v_mfma_f32_16x16x32_bf16 v[4:7], a[240:243], a[72:75], v[4:7]\n\t"
    "v_exp_f32 v43, v43\n\t"
    "v_mfma_f32_16x16x32_bf16 v[8:11], a[240:243], a[80:83], v[8:11]\n\t"
    "v_fma_f32 v44, v44, s72, v243\n\t"
    "v_fma_f32 v45, v45, s72, v243\n\t"
    "v_mfma_f32_16x16x32_bf16 v[8:11], a[244:247], a[84:87], v[8:11]\n\t"
    "v_exp_f32 v44, v44\n\t"
    "v_mfma_f32_16x16x32_bf16 v[8:11], a[248:251], a[112:115], v[8:11]\n\t"
    "ds_read_b128 a[224:227], v225 offset:6144\n\t"
    "v_cvt_pk_bf16_f32 v123, v42, v43\n\t"
    "v_mfma_f32_16x16x32_bf16 v[8:11], a[252:255], a[116:119], v[8:11]\n\t"
    "v_exp_f32 v45, v45\n\t"
    "v_mfma_f32_16x16x32_bf16 v[12:15], a[252:255], a[124:127], v[12:15]\n\t"
    "v_fma_f32 v46, v46, s72, v243\n\t"
    "v_fma_f32 v47, v47, s72, v243\n\t"
    "v_mfma_f32_16x16x32_bf16 v[12:15], a[248:251], a[120:123], v[12:15]\n\t"
    "v_exp_f32 v46, v46\n\t"
    "v_mfma_f32_16x16x32_bf16 v[12:15], a[244:247], a[92:95], v[12:15]\n\t"
    "ds_read_b128 a[252:255], v224 offset:10240\n\t"
    "v_cvt_pk_bf16_f32 v126, v44, v45\n\t"
    "v_mfma_f32_16x16x32_bf16 v[12:15], a[240:243], a[88:91], v[12:15]\n\t"
    "s_waitcnt lgkmcnt(2)\n\t"
    "v_exp_f32 v47, v47\n\t"
    "v_mfma_f32_16x16x32_bf16 v[252:255], v[52:55], v[116:119], v[252:255]\n\t"
    "buffer_load_dwordx4 v232, s[40:43], s71 offen offset:2048 nt lds\n\t"
    "v_mfma_f32_16x16x32_bf16 v[16:19], a[236:239], a[0:3], 0\n\t"
    "v_fma_f32 v0, v0, s72, v240\n\t"
    "v_fma_f32 v1, v1, s72, v240\n\t"
    "v_mfma_f32_16x16x32_bf16 v[16:19], a[232:235], a[4:7], v[16:19]\n\t"
    "s_waitcnt lgkmcnt(1)\n\t"
    "v_exp_f32 v0, v0\n\t"
    "v_mfma_f32_16x16x32_bf16 v[16:19], a[228:231], a[32:35], v[16:19]\n\t"
    "ds_read_b128 a[248:251], v225 offset:10240\n\t"
    "v_cvt_pk_bf16_f32 v127, v46, v47\n\t"
    "v_mfma_f32_16x16x32_bf16 v[16:19], a[224:227], a[36:39], v[16:19]\n\t"
    "v_exp_f32 v1, v1\n\t"
    "v_mfma_f32_16x16x32_bf16 v[20:23], a[224:227], a[44:47], 0\n\t"
    "v_fma_f32 v2, v2, s72, v240\n\t"
    "v_fma_f32 v3, v3, s72, v240\n\t"
    "v_mfma_f32_16x16x32_bf16 v[20:23], a[228:231], a[40:43], v[20:23]\n\t"
    "v_exp_f32 v2, v2\n\t"
    "v_mfma_f32_16x16x32_bf16 v[20:23], a[232:235], a[12:15], v[20:23]\n\t"
    "ds_read_b128 a[244:247], v224 offset:14336\n\t"
    "buffer_load_dwordx4 v233, s[40:43], s71 offen offset:3072 nt lds\n\t"
    "v_cvt_pk_bf16_f32 v128, v0, v1\n\t"
    "v_mfma_f32_16x16x32_bf16 v[20:23], a[236:239], a[8:11], v[20:23]\n\t"
    "v_exp_f32 v3, v3\n\t"
    "v_mfma_f32_16x16x32_bf16 v[24:27], a[236:239], a[16:19], 0\n\t"
    "v_fma_f32 v4, v4, s72, v241\n\t"
    "v_fma_f32 v5, v5, s72, v241\n\t"
    "v_mfma_f32_16x16x32_bf16 v[24:27], a[232:235], a[20:23], v[24:27]\n\t"
    "v_exp_f32 v4, v4\n\t"
    "v_mfma_f32_16x16x32_bf16 v[24:27], a[228:231], a[48:51], v[24:27]\n\t"
    "ds_read_b128 a[240:243], v225 offset:14336\n\t"
    "v_cvt_pk_bf16_f32 v129, v2, v3\n\t"
    "v_mfma_f32_16x16x32_bf16 v[24:27], a[224:227], a[52:55], v[24:27]\n\t"
    "v_exp_f32 v5, v5\n\t"
    "v_mfma_f32_16x16x32_bf16 v[28:31], a[224:227], a[60:63], 0\n\t"
    "v_fma_f32 v6, v6, s72, v241\n\t"
    "v_fma_f32 v7, v7, s72, v241\n\t"
    "v_mfma_f32_16x16x32_bf16 v[28:31], a[228:231], a[56:59], v[28:31]\n\t"
    "v_exp_f32 v6, v6\n\t"
    "v_mfma_f32_16x16x32_bf16 v[28:31], a[232:235], a[28:31], v[28:31]\n\t"
    "s_xor_b32 s71, s64, 0x38000\n\t"
    "s_add_u32 m0, s70, 0x18000\n\t"
    "v_cvt_pk_bf16_f32 v132, v4, v5\n\t"
    "v_mfma_f32_16x16x32_bf16 v[28:31], a[236:239], a[24:27], v[28:31]\n\t"
    "s_waitcnt lgkmcnt(1)\n\t"
    "v_exp_f32 v7, v7\n\t"
    "v_mfma_f32_16x16x32_bf16 v[252:255], v[56:59], v[120:123], v[252:255]\n\t"
    "buffer_load_dwordx4 v232, s[40:43], s71 offen offset:0 nt lds\n\t"
    "v_mfma_f32_16x16x32_bf16 v[16:19], a[252:255], a[64:67], v[16:19]\n\t"
    "v_fma_f32 v8, v8, s72, v242\n\t"
    "v_fma_f32 v9, v9, s72, v242\n\t"
    "v_mfma_f32_16x16x32_bf16 v[16:19], a[248:251], a[68:71], v[16:19]\n\t"
    "s_waitcnt lgkmcnt(0)\n\t"
    "v_exp_f32 v8, v8\n\t"
    "v_mfma_f32_16x16x32_bf16 v[16:19], a[244:247], a[96:99], v[16:19]\n\t"
    "s_waitcnt vmcnt(17)\n\t"
    "v_cvt_pk_bf16_f32 v133, v6, v7\n\t"
    "v_mfma_f32_16x16x32_bf16 v[16:19], a[240:243], a[100:103], v[16:19]\n\t"
    "s_barrier\n\t"
    "v_exp_f32 v9, v9\n\t"
    "v_mfma_f32_16x16x32_bf16 v[20:23], a[240:243], a[108:111], v[20:23]\n\t"
    "ds_read_b128 a[224:227], v224 offset:32768\n\t"
    "v_fma_f32 v10, v10, s72, v242\n\t"
    "v_fma_f32 v11, v11, s72, v242\n\t"
    "v_mfma_f32_16x16x32_bf16 v[20:23], a[244:247], a[104:107], v[20:23]\n\t"
    "ds_read_b128 a[228:231], v225 offset:32768\n\t"
    "v_exp_f32 v10, v10\n\t"
    "v_mfma_f32_16x16x32_bf16 v[20:23], a[248:251], a[76:79], v[20:23]\n\t"
    "ds_read_b128 a[232:235], v224 offset:36864\n\t"
    "v_cvt_pk_bf16_f32 v136, v8, v9\n\t"
    "v_mfma_f32_16x16x32_bf16 v[20:23], a[252:255], a[72:75], v[20:23]\n\t"
    "v_exp_f32 v11, v11\n\t"
    "v_mfma_f32_16x16x32_bf16 v[24:27], a[252:255], a[80:83], v[24:27]\n\t"
    "v_fma_f32 v12, v12, s72, v243\n\t"
    "v_fma_f32 v13, v13, s72, v243\n\t"
    "v_mfma_f32_16x16x32_bf16 v[24:27], a[248:251], a[84:87], v[24:27]\n\t"
    "v_exp_f32 v12, v12\n\t"
    "v_mfma_f32_16x16x32_bf16 v[24:27], a[244:247], a[112:115], v[24:27]\n\t"
    "ds_read_b128 a[236:239], v225 offset:36864\n\t"
    "v_cvt_pk_bf16_f32 v137, v10, v11\n\t"
    "v_mfma_f32_16x16x32_bf16 v[24:27], a[240:243], a[116:119], v[24:27]\n\t"
    "v_exp_f32 v13, v13\n\t"
    "v_mfma_f32_16x16x32_bf16 v[28:31], a[240:243], a[124:127], v[28:31]\n\t"
    "v_fma_f32 v14, v14, s72, v243\n\t"
    "v_fma_f32 v15, v15, s72, v243\n\t"
    "v_mfma_f32_16x16x32_bf16 v[28:31], a[244:247], a[120:123], v[28:31]\n\t"
    "v_exp_f32 v14, v14\n\t"
    "v_mfma_f32_16x16x32_bf16 v[28:31], a[248:251], a[92:95], v[28:31]\n\t"
    "ds_read_b128 a[240:243], v224 offset:40960\n\t"
    "v_cvt_pk_bf16_f32 v140, v12, v13\n\t"
    "v_mfma_f32_16x16x32_bf16 v[28:31], a[252:255], a[88:91], v[28:31]\n\t"
    "s_waitcnt lgkmcnt(2)\n\t"
    "v_exp_f32 v15, v15\n\t"
    "v_mfma_f32_16x16x32_bf16 v[252:255], v[60:63], v[124:127], v[252:255]\n\t"
    "buffer_load_dwordx4 v233, s[40:43], s71 offen offset:1024 nt lds\n\t"
    "v_mfma_f32_16x16x32_bf16 v[32:35], a[224:227], a[0:3], 0\n\t"
    "v_fma_f32 v16, v16, s72, v240\n\t"
    "v_fma_f32 v17, v17, s72, v240\n\t"
    "v_mfma_f32_16x16x32_bf16 v[32:35], a[228:231], a[4:7], v[32:35]\n\t"
    "s_waitcnt lgkmcnt(1)\n\t"
    "v_exp_f32 v16, v16\n\t"
    "v_mfma_f32_16x16x32_bf16 v[32:35], a[232:235], a[32:35], v[32:35]\n\t"
    "ds_read_b128 a[244:247], v225 offset:40960\n\t"
    "v_cvt_pk_bf16_f32 v141, v14, v15\n\t"
    "v_mfma_f32_16x16x32_bf16 v[32:35], a[236:239], a[36:39], v[32:35]\n\t"
    "v_exp_f32 v17, v17\n\t"
    "v_mfma_f32_16x16x32_bf16 v[36:39], a[236:239], a[44:47], 0\n\t"
    "v_fma_f32 v18, v18, s72, v240\n\t"
    "v_fma_f32 v19, v19, s72, v240\n\t"
    "v_mfma_f32_16x16x32_bf16 v[36:39], a[232:235], a[40:43], v[36:39]\n\t"
    "v_exp_f32 v18, v18\n\t"
    "v_mfma_f32_16x16x32_bf16 v[36:39], a[228:231], a[12:15], v[36:39]\n\t"
    "ds_read_b128 a[248:251], v224 offset:45056\n\t"
    "buffer_load_dwordx4 v232, s[40:43], s71 offen offset:2048 nt lds\n\t"
    "v_cvt_pk_bf16_f32 v130, v16, v17\n\t"
    "v_mfma_f32_16x16x32_bf16 v[36:39], a[224:227], a[8:11], v[36:39]\n\t"
    "v_exp_f32 v19, v19\n\t"
    "v_mfma_f32_16x16x32_bf16 v[40:43], a[224:227], a[16:19], 0\n\t"
    "v_fma_f32 v20, v20, s72, v241\n\t"
    "v_fma_f32 v21, v21, s72, v241\n\t"
    "v_mfma_f32_16x16x32_bf16 v[40:43], a[228:231], a[20:23], v[40:43]\n\t"
    "v_exp_f32 v20, v20\n\t"
    "v_mfma_f32_16x16x32_bf16 v[40:43], a[232:235], a[48:51], v[40:43]\n\t"
    "ds_read_b128 a[252:255], v225 offset:45056\n\t"
    "v_cvt_pk_bf16_f32 v131, v18, v19\n\t"
    "v_mfma_f32_16x16x32_bf16 v[40:43], a[236:239], a[52:55], v[40:43]\n\t"
    "v_exp_f32 v21, v21\n\t"
    "v_mfma_f32_16x16x32_bf16 v[44:47], a[236:239], a[60:63], 0\n\t"
    "v_fma_f32 v22, v22, s72, v241\n\t"
    "v_fma_f32 v23, v23, s72, v241\n\t"
    "v_mfma_f32_16x16x32_bf16 v[44:47], a[232:235], a[56:59], v[44:47]\n\t"
    "v_exp_f32 v22, v22\n\t"
    "v_mfma_f32_16x16x32_bf16 v[44:47], a[228:231], a[28:31], v[44:47]\n\t"
    "ds_read_b128 a[236:239], v224 offset:34816\n\t"
    "v_cvt_pk_bf16_f32 v134, v20, v21\n\t"
    "v_mfma_f32_16x16x32_bf16 v[44:47], a[224:227], a[24:27], v[44:47]\n\t"
    "s_waitcnt lgkmcnt(2)\n\t"
    "v_exp_f32 v23, v23\n\t"
    "v_mfma_f32_16x16x32_bf16 v[252:255], v[48:51], v[128:131], v[252:255]\n\t"
    "buffer_load_dwordx4 v233, s[40:43], s71 offen offset:3072 nt lds\n\t"
    "v_mfma_f32_16x16x32_bf16 v[32:35], a[240:243], a[64:67], v[32:35]\n\t"
    "v_fma_f32 v24, v24, s72, v242\n\t"
    "v_fma_f32 v25, v25, s72, v242\n\t"
    "v_mfma_f32_16x16x32_bf16 v[32:35], a[244:247], a[68:71], v[32:35]\n\t"
    "s_waitcnt lgkmcnt(1)\n\t"
    "v_exp_f32 v24, v24\n\t"
    "v_mfma_f32_16x16x32_bf16 v[32:35], a[248:251], a[96:99], v[32:35]\n\t"
    "ds_read_b128 a[232:235], v225 offset:34816\n\t"
    "v_cvt_pk_bf16_f32 v135, v22, v23\n\t"
    "v_mfma_f32_16x16x32_bf16 v[32:35], a[252:255], a[100:103], v[32:35]\n\t"
    "v_exp_f32 v25, v25\n\t"
    "v_mfma_f32_16x16x32_bf16 v[36:39], a[252:255], a[108:111], v[36:39]\n\t"
    "v_fma_f32 v26, v26, s72, v242\n\t"
    "v_fma_f32 v27, v27, s72, v242\n\t"
    "v_mfma_f32_16x16x32_bf16 v[36:39], a[248:251], a[104:107], v[36:39]\n\t"
    "v_exp_f32 v26, v26\n\t"
    "v_mfma_f32_16x16x32_bf16 v[36:39], a[244:247], a[76:79], v[36:39]\n\t"
    "ds_read_b128 a[228:231], v224 offset:38912\n\t"
    "v_cvt_pk_bf16_f32 v138, v24, v25\n\t"
    "v_mfma_f32_16x16x32_bf16 v[36:39], a[240:243], a[72:75], v[36:39]\n\t"
    "v_exp_f32 v27, v27\n\t"
    "v_mfma_f32_16x16x32_bf16 v[40:43], a[240:243], a[80:83], v[40:43]\n\t"
    "v_fma_f32 v28, v28, s72, v243\n\t"
    "v_fma_f32 v29, v29, s72, v243\n\t"
    "v_mfma_f32_16x16x32_bf16 v[40:43], a[244:247], a[84:87], v[40:43]\n\t"
    "v_exp_f32 v28, v28\n\t"
    "v_mfma_f32_16x16x32_bf16 v[40:43], a[248:251], a[112:115], v[40:43]\n\t"
    "ds_read_b128 a[224:227], v225 offset:38912\n\t"
    "v_cvt_pk_bf16_f32 v139, v26, v27\n\t"
    "v_mfma_f32_16x16x32_bf16 v[40:43], a[252:255], a[116:119], v[40:43]\n\t"
    "s_xor_b32 s71, s67, 0x4080\n\t"
    "v_exp_f32 v29, v29\n\t"
    "v_mfma_f32_16x16x32_bf16 v[44:47], a[252:255], a[124:127], v[44:47]\n\t"
    "s_add_u32 m0, s70, 0x24000\n\t"
    "v_fma_f32 v30, v30, s72, v243\n\t"
    "v_fma_f32 v31, v31, s72, v243\n\t"
    "v_mfma_f32_16x16x32_bf16 v[44:47], a[248:251], a[120:123], v[44:47]\n\t"
    "v_exp_f32 v30, v30\n\t"
    "v_mfma_f32_16x16x32_bf16 v[44:47], a[244:247], a[92:95], v[44:47]\n\t"
    "ds_read_b128 a[252:255], v224 offset:43008\n\t"
    "v_cvt_pk_bf16_f32 v142, v28, v29\n\t"
    "v_mfma_f32_16x16x32_bf16 v[44:47], a[240:243], a[88:91], v[44:47]\n\t"
    "s_waitcnt lgkmcnt(2)\n\t"
    "v_exp_f32 v31, v31\n\t"
    "v_mfma_f32_16x16x32_bf16 v[252:255], v[52:55], v[132:135], v[252:255]\n\t"
    "buffer_load_dwordx4 v232, s[52:55], s71 offen offset:0 lds\n\t"
    "v_mfma_f32_16x16x32_bf16 v[0:3], a[236:239], a[0:3], 0\n\t"
    "v_fma_f32 v32, v32, s72, v240\n\t"
    "v_fma_f32 v33, v33, s72, v240\n\t"
    "v_mfma_f32_16x16x32_bf16 v[0:3], a[232:235], a[4:7], v[0:3]\n\t"
    "s_waitcnt lgkmcnt(1)\n\t"
    "v_exp_f32 v32, v32\n\t"
    "v_mfma_f32_16x16x32_bf16 v[0:3], a[228:231], a[32:35], v[0:3]\n\t"
    "ds_read_b128 a[248:251], v225 offset:43008\n\t"
    "v_cvt_pk_bf16_f32 v143, v30, v31\n\t"
    "v_mfma_f32_16x16x32_bf16 v[0:3], a[224:227], a[36:39], v[0:3]\n\t"
    "v_exp_f32 v33, v33\n\t"
    "v_mfma_f32_16x16x32_bf16 v[4:7], a[224:227], a[44:47], 0\n\t"
    "v_fma_f32 v34, v34, s72, v240\n\t"
    "v_fma_f32 v35, v35, s72, v240\n\t"
    "v_mfma_f32_16x16x32_bf16 v[4:7], a[228:231], a[40:43], v[4:7]\n\t"
    "v_exp_f32 v34, v34\n\t"
    "v_mfma_f32_16x16x32_bf16 v[4:7], a[232:235], a[12:15], v[4:7]\n\t"
    "ds_read_b128 a[244:247], v224 offset:47104\n\t"
    "buffer_load_dwordx4 v233, s[52:55], s71 offen offset:1024 lds\n\t"
    "v_cvt_pk_bf16_f32 v144, v32, v33\n\t"
    "v_mfma_f32_16x16x32_bf16 v[4:7], a[236:239], a[8:11], v[4:7]\n\t"
    "v_exp_f32 v35, v35\n\t"
    "v_mfma_f32_16x16x32_bf16 v[8:11], a[236:239], a[16:19], 0\n\t"
    "v_fma_f32 v36, v36, s72, v241\n\t"
    "v_fma_f32 v37, v37, s72, v241\n\t"
    "v_mfma_f32_16x16x32_bf16 v[8:11], a[232:235], a[20:23], v[8:11]\n\t"
    "v_exp_f32 v36, v36\n\t"
    "v_mfma_f32_16x16x32_bf16 v[8:11], a[228:231], a[48:51], v[8:11]\n\t"
    "ds_read_b128 a[240:243], v225 offset:47104\n\t"
    "v_cvt_pk_bf16_f32 v145, v34, v35\n\t"
    "v_mfma_f32_16x16x32_bf16 v[8:11], a[224:227], a[52:55], v[8:11]\n\t"
    "v_exp_f32 v37, v37\n\t"
    "v_mfma_f32_16x16x32_bf16 v[12:15], a[224:227], a[60:63], 0\n\t"
    "v_fma_f32 v38, v38, s72, v241\n\t"
    "v_fma_f32 v39, v39, s72, v241\n\t"
    "v_mfma_f32_16x16x32_bf16 v[12:15], a[228:231], a[56:59], v[12:15]\n\t"
    "v_exp_f32 v38, v38\n\t"
    "v_mfma_f32_16x16x32_bf16 v[12:15], a[232:235], a[28:31], v[12:15]\n\t"
    "v_cvt_pk_bf16_f32 v148, v36, v37\n\t"
    "v_add_u32 v224, 0x10000, v224\n\t"
    "v_mfma_f32_16x16x32_bf16 v[12:15], a[236:239], a[24:27], v[12:15]\n\t"
    "s_waitcnt lgkmcnt(1)\n\t"
    "v_exp_f32 v39, v39\n\t"
    "v_mfma_f32_16x16x32_bf16 v[252:255], v[56:59], v[136:139], v[252:255]\n\t"
    "buffer_load_dwordx4 v232, s[52:55], s71 offen offset:2048 lds\n\t"
    "v_mfma_f32_16x16x32_bf16 v[0:3], a[252:255], a[64:67], v[0:3]\n\t"
    "v_fma_f32 v40, v40, s72, v242\n\t"
    "v_fma_f32 v41, v41, s72, v242\n\t"
    "v_mfma_f32_16x16x32_bf16 v[0:3], a[248:251], a[68:71], v[0:3]\n\t"
    "s_waitcnt lgkmcnt(0)\n\t"
    "v_exp_f32 v40, v40\n\t"
    "v_mfma_f32_16x16x32_bf16 v[0:3], a[244:247], a[96:99], v[0:3]\n\t"
    "s_waitcnt vmcnt(15)\n\t"
    "v_cvt_pk_bf16_f32 v149, v38, v39\n\t"
    "v_add_u32 v225, 0x10000, v225\n\t"
    "v_mfma_f32_16x16x32_bf16 v[0:3], a[240:243], a[100:103], v[0:3]\n\t"
    "s_barrier\n\t"
    "v_exp_f32 v41, v41\n\t"
    "v_mfma_f32_16x16x32_bf16 v[4:7], a[240:243], a[108:111], v[4:7]\n\t"
    "ds_read_b128 a[224:227], v224 offset:0\n\t"
    "v_fma_f32 v42, v42, s72, v242\n\t"
    "v_fma_f32 v43, v43, s72, v242\n\t"
    "v_mfma_f32_16x16x32_bf16 v[4:7], a[244:247], a[104:107], v[4:7]\n\t"
    "ds_read_b128 a[228:231], v225 offset:0\n\t"
    "v_exp_f32 v42, v42\n\t"
    "v_mfma_f32_16x16x32_bf16 v[4:7], a[248:251], a[76:79], v[4:7]\n\t"
    "ds_read_b128 a[232:235], v224 offset:4096\n\t"
    "v_cvt_pk_bf16_f32 v152, v40, v41\n\t"
    "v_mfma_f32_16x16x32_bf16 v[4:7], a[252:255], a[72:75], v[4:7]\n\t"
    "v_exp_f32 v43, v43\n\t"
    "v_mfma_f32_16x16x32_bf16 v[8:11], a[252:255], a[80:83], v[8:11]\n\t"
    "v_fma_f32 v44, v44, s72, v243\n\t"
    "v_fma_f32 v45, v45, s72, v243\n\t"
    "v_mfma_f32_16x16x32_bf16 v[8:11], a[248:251], a[84:87], v[8:11]\n\t"
    "v_exp_f32 v44, v44\n\t"
    "v_mfma_f32_16x16x32_bf16 v[8:11], a[244:247], a[112:115], v[8:11]\n\t"
    "ds_read_b128 a[236:239], v225 offset:4096\n\t"
    "v_cvt_pk_bf16_f32 v153, v42, v43\n\t"
    "v_mfma_f32_16x16x32_bf16 v[8:11], a[240:243], a[116:119], v[8:11]\n\t"
    "v_exp_f32 v45, v45\n\t"
    "v_mfma_f32_16x16x32_bf16 v[12:15], a[240:243], a[124:127], v[12:15]\n\t"
    "v_fma_f32 v46, v46, s72, v243\n\t"
    "v_fma_f32 v47, v47, s72, v243\n\t"
    "v_mfma_f32_16x16x32_bf16 v[12:15], a[244:247], a[120:123], v[12:15]\n\t"
    "v_exp_f32 v46, v46\n\t"
    "v_mfma_f32_16x16x32_bf16 v[12:15], a[248:251], a[92:95], v[12:15]\n\t"
    "ds_read_b128 a[240:243], v224 offset:8192\n\t"
    "v_cvt_pk_bf16_f32 v156, v44, v45\n\t"
    "v_mfma_f32_16x16x32_bf16 v[12:15], a[252:255], a[88:91], v[12:15]\n\t"
    "s_waitcnt lgkmcnt(2)\n\t"
    "v_exp_f32 v47, v47\n\t"
    "v_mfma_f32_16x16x32_bf16 v[252:255], v[60:63], v[140:143], v[252:255]\n\t"
    "buffer_load_dwordx4 v233, s[52:55], s71 offen offset:3072 lds\n\t"
    "v_mfma_f32_16x16x32_bf16 v[16:19], a[224:227], a[0:3], 0\n\t"
    "s_mov_b32 s54, s46\n\t"
    "v_fma_f32 v0, v0, s72, v240\n\t"
    "v_fma_f32 v1, v1, s72, v240\n\t"
    "v_mfma_f32_16x16x32_bf16 v[16:19], a[228:231], a[4:7], v[16:19]\n\t"
    "s_waitcnt lgkmcnt(1)\n\t"
    "v_exp_f32 v0, v0\n\t"
    "v_mfma_f32_16x16x32_bf16 v[16:19], a[232:235], a[32:35], v[16:19]\n\t"
    "ds_read_b128 a[244:247], v225 offset:8192\n\t"
    "v_cvt_pk_bf16_f32 v157, v46, v47\n\t"
    "v_mfma_f32_16x16x32_bf16 v[16:19], a[236:239], a[36:39], v[16:19]\n\t"
    "v_exp_f32 v1, v1\n\t"
    "v_mfma_f32_16x16x32_bf16 v[20:23], a[236:239], a[44:47], 0\n\t"
    "s_xor_b32 s71, s64, 0x3c000\n\t"
    "v_fma_f32 v2, v2, s72, v240\n\t"
    "v_fma_f32 v3, v3, s72, v240\n\t"
    "v_mfma_f32_16x16x32_bf16 v[20:23], a[232:235], a[40:43], v[20:23]\n\t"
    "s_add_u32 m0, s70, 0x1c000\n\t"
    "v_exp_f32 v2, v2\n\t"
    "v_mfma_f32_16x16x32_bf16 v[20:23], a[228:231], a[12:15], v[20:23]\n\t"
    "ds_read_b128 a[248:251], v224 offset:12288\n\t"
    "buffer_load_dwordx4 v232, s[40:43], s71 offen offset:0 nt lds\n\t"
    "v_cvt_pk_bf16_f32 v146, v0, v1\n\t"
    "v_mfma_f32_16x16x32_bf16 v[20:23], a[224:227], a[8:11], v[20:23]\n\t"
    "v_exp_f32 v3, v3\n\t"
    "v_mfma_f32_16x16x32_bf16 v[24:27], a[224:227], a[16:19], 0\n\t"
    "v_fma_f32 v4, v4, s72, v241\n\t"
    "v_fma_f32 v5, v5, s72, v241\n\t"
    "v_mfma_f32_16x16x32_bf16 v[24:27], a[228:231], a[20:23], v[24:27]\n\t"
    "v_exp_f32 v4, v4\n\t"
    "v_mfma_f32_16x16x32_bf16 v[24:27], a[232:235], a[48:51], v[24:27]\n\t"
    "ds_read_b128 a[252:255], v225 offset:12288\n\t"
    "v_cvt_pk_bf16_f32 v147, v2, v3\n\t"
    "v_mfma_f32_16x16x32_bf16 v[24:27], a[236:239], a[52:55], v[24:27]\n\t"
    "v_exp_f32 v5, v5\n\t"
    "v_mfma_f32_16x16x32_bf16 v[28:31], a[236:239], a[60:63], 0\n\t"
    "v_fma_f32 v6, v6, s72, v241\n\t"
    "v_fma_f32 v7, v7, s72, v241\n\t"
    "v_mfma_f32_16x16x32_bf16 v[28:31], a[232:235], a[56:59], v[28:31]\n\t"
    "v_exp_f32 v6, v6\n\t"
    "v_mfma_f32_16x16x32_bf16 v[28:31], a[228:231], a[28:31], v[28:31]\n\t"
    "ds_read_b128 a[236:239], v224 offset:2048\n\t"
    "v_cvt_pk_bf16_f32 v150, v4, v5\n\t"
    "v_mfma_f32_16x16x32_bf16 v[28:31], a[224:227], a[24:27], v[28:31]\n\t"
    "s_waitcnt lgkmcnt(2)\n\t"
    "v_exp_f32 v7, v7\n\t"
    "v_mfma_f32_16x16x32_bf16 v[252:255], v[48:51], v[144:147], v[252:255]\n\t"
    "buffer_load_dwordx4 v233, s[40:43], s71 offen offset:1024 nt lds\n\t"
    "v_mfma_f32_16x16x32_bf16 v[16:19], a[240:243], a[64:67], v[16:19]\n\t"
    "v_fma_f32 v8, v8, s72, v242\n\t"
    "v_fma_f32 v9, v9, s72, v242\n\t"
    "v_mfma_f32_16x16x32_bf16 v[16:19], a[244:247], a[68:71], v[16:19]\n\t"
    "s_waitcnt lgkmcnt(1)\n\t"
    "v_exp_f32 v8, v8\n\t"
    "v_mfma_f32_16x16x32_bf16 v[16:19], a[248:251], a[96:99], v[16:19]\n\t"
    "ds_read_b128 a[232:235], v225 offset:2048\n\t"
    "v_cvt_pk_bf16_f32 v151, v6, v7\n\t"
    "v_mfma_f32_16x16x32_bf16 v[16:19], a[252:255], a[100:103], v[16:19]\n\t"
    "v_exp_f32 v9, v9\n\t"
    "v_mfma_f32_16x16x32_bf16 v[20:23], a[252:255], a[108:111], v[20:23]\n\t"
    "v_fma_f32 v10, v10, s72, v242\n\t"
    "v_fma_f32 v11, v11, s72, v242\n\t"
    "v_mfma_f32_16x16x32_bf16 v[20:23], a[248:251], a[104:107], v[20:23]\n\t"
    "v_exp_f32 v10, v10\n\t"
    "v_mfma_f32_16x16x32_bf16 v[20:23], a[244:247], a[76:79], v[20:23]\n\t"
    "ds_read_b128 a[228:231], v224 offset:6144\n\t"
    "v_cvt_pk_bf16_f32 v154, v8, v9\n\t"
    "v_mfma_f32_16x16x32_bf16 v[20:23], a[240:243], a[72:75], v[20:23]\n\t"
    "v_exp_f32 v11, v11\n\t"
    "v_mfma_f32_16x16x32_bf16 v[24:27], a[240:243], a[80:83], v[24:27]\n\t"
    "v_fma_f32 v12, v12, s72, v243\n\t"
    "v_fma_f32 v13, v13, s72, v243\n\t"
    "v_mfma_f32_16x16x32_bf16 v[24:27], a[244:247], a[84:87], v[24:27]\n\t"
    "v_exp_f32 v12, v12\n\t"
    "v_mfma_f32_16x16x32_bf16 v[24:27], a[248:251], a[112:115], v[24:27]\n\t"
    "ds_read_b128 a[224:227], v225 offset:6144\n\t"
    "v_cvt_pk_bf16_f32 v155, v10, v11\n\t"
    "v_mfma_f32_16x16x32_bf16 v[24:27], a[252:255], a[116:119], v[24:27]\n\t"
    "v_exp_f32 v13, v13\n\t"
    "v_mfma_f32_16x16x32_bf16 v[28:31], a[252:255], a[124:127], v[28:31]\n\t"
    "v_fma_f32 v14, v14, s72, v243\n\t"
    "v_fma_f32 v15, v15, s72, v243\n\t"
    "v_mfma_f32_16x16x32_bf16 v[28:31], a[248:251], a[120:123], v[28:31]\n\t"
    "v_exp_f32 v14, v14\n\t"
    "v_mfma_f32_16x16x32_bf16 v[28:31], a[244:247], a[92:95], v[28:31]\n\t"
    "ds_read_b128 a[252:255], v224 offset:10240\n\t"
    "v_cvt_pk_bf16_f32 v158, v12, v13\n\t"
    "v_mfma_f32_16x16x32_bf16 v[28:31], a[240:243], a[88:91], v[28:31]\n\t"
    "s_waitcnt lgkmcnt(2)\n\t"
    "v_exp_f32 v15, v15\n\t"
    "v_mfma_f32_16x16x32_bf16 v[252:255], v[52:55], v[148:151], v[252:255]\n\t"
    "buffer_load_dwordx4 v232, s[40:43], s71 offen offset:2048 nt lds\n\t"
    "v_mfma_f32_16x16x32_bf16 v[32:35], a[236:239], a[0:3], 0\n\t"
    "v_fma_f32 v16, v16, s72, v240\n\t"
    "v_fma_f32 v17, v17, s72, v240\n\t"
    "v_mfma_f32_16x16x32_bf16 v[32:35], a[232:235], a[4:7], v[32:35]\n\t"
    "s_waitcnt lgkmcnt(1)\n\t"
    "v_exp_f32 v16, v16\n\t"
    "v_mfma_f32_16x16x32_bf16 v[32:35], a[228:231], a[32:35], v[32:35]\n\t"
    "ds_read_b128 a[248:251], v225 offset:10240\n\t"
    "v_cvt_pk_bf16_f32 v159, v14, v15\n\t"
    "v_mfma_f32_16x16x32_bf16 v[32:35], a[224:227], a[36:39], v[32:35]\n\t"
    "v_exp_f32 v17, v17\n\t"
    "v_mfma_f32_16x16x32_bf16 v[36:39], a[224:227], a[44:47], 0\n\t"
    "v_fma_f32 v18, v18, s72, v240\n\t"
    "v_fma_f32 v19, v19, s72, v240\n\t"
    "v_mfma_f32_16x16x32_bf16 v[36:39], a[228:231], a[40:43], v[36:39]\n\t"
    "v_exp_f32 v18, v18\n\t"
    "v_mfma_f32_16x16x32_bf16 v[36:39], a[232:235], a[12:15], v[36:39]\n\t"
    "ds_read_b128 a[244:247], v224 offset:14336\n\t"
    "buffer_load_dwordx4 v233, s[40:43], s71 offen offset:3072 nt lds\n\t"
    "v_cvt_pk_bf16_f32 v160, v16, v17\n\t"
    "v_mfma_f32_16x16x32_bf16 v[36:39], a[236:239], a[8:11], v[36:39]\n\t"
    "v_exp_f32 v19, v19\n\t"
    "v_mfma_f32_16x16x32_bf16 v[40:43], a[236:239], a[16:19], 0\n\t"
    "v_fma_f32 v20, v20, s72, v241\n\t"
    "v_fma_f32 v21, v21, s72, v241\n\t"
    "v_mfma_f32_16x16x32_bf16 v[40:43], a[232:235], a[20:23], v[40:43]\n\t"
    "v_exp_f32 v20, v20\n\t"
    "v_mfma_f32_16x16x32_bf16 v[40:43], a[228:231], a[48:51], v[40:43]\n\t"
    "ds_read_b128 a[240:243], v225 offset:14336\n\t"
    "v_cvt_pk_bf16_f32 v161, v18, v19\n\t"
    "v_mfma_f32_16x16x32_bf16 v[40:43], a[224:227], a[52:55], v[40:43]\n\t"
    "v_exp_f32 v21, v21\n\t"
    "v_mfma_f32_16x16x32_bf16 v[44:47], a[224:227], a[60:63], 0\n\t"
    "v_fma_f32 v22, v22, s72, v241\n\t"
    "v_fma_f32 v23, v23, s72, v241\n\t"
    "v_mfma_f32_16x16x32_bf16 v[44:47], a[228:231], a[56:59], v[44:47]\n\t"
    "v_exp_f32 v22, v22\n\t"
    "v_mfma_f32_16x16x32_bf16 v[44:47], a[232:235], a[28:31], v[44:47]\n\t"
    "s_xor_b32 s71, s66, 0x0\n\t"
    "s_add_u32 m0, s70, 0x0\n\t"
    "v_cvt_pk_bf16_f32 v164, v20, v21\n\t"
    "v_mfma_f32_16x16x32_bf16 v[44:47], a[236:239], a[24:27], v[44:47]\n\t"
    "s_waitcnt lgkmcnt(1)\n\t"
    "v_exp_f32 v23, v23\n\t"
    "v_mfma_f32_16x16x32_bf16 v[252:255], v[56:59], v[152:155], v[252:255]\n\t"
    "buffer_load_dwordx4 v234, s[48:51], s71 offen offset:0 lds\n\t"
    "v_mfma_f32_16x16x32_bf16 v[32:35], a[252:255], a[64:67], v[32:35]\n\t"
    "v_fma_f32 v24, v24, s72, v242\n\t"
    "v_fma_f32 v25, v25, s72, v242\n\t"
    "v_mfma_f32_16x16x32_bf16 v[32:35], a[248:251], a[68:71], v[32:35]\n\t"
    "s_waitcnt lgkmcnt(0)\n\t"
    "v_exp_f32 v24, v24\n\t"
    "v_mfma_f32_16x16x32_bf16 v[32:35], a[244:247], a[96:99], v[32:35]\n\t"
    "s_waitcnt vmcnt(13)\n\t"
    "v_cvt_pk_bf16_f32 v165, v22, v23\n\t"
    "v_mfma_f32_16x16x32_bf16 v[32:35], a[240:243], a[100:103], v[32:35]\n\t"
    "s_barrier\n\t"
    "v_exp_f32 v25, v25\n\t"
    "v_mfma_f32_16x16x32_bf16 v[36:39], a[240:243], a[108:111], v[36:39]\n\t"
    "ds_read_b128 a[224:227], v224 offset:16384\n\t"
    "v_fma_f32 v26, v26, s72, v242\n\t"
    "v_fma_f32 v27, v27, s72, v242\n\t"
    "v_mfma_f32_16x16x32_bf16 v[36:39], a[244:247], a[104:107], v[36:39]\n\t"
    "ds_read_b128 a[228:231], v225 offset:16384\n\t"
    "v_exp_f32 v26, v26\n\t"
    "v_mfma_f32_16x16x32_bf16 v[36:39], a[248:251], a[76:79], v[36:39]\n\t"
    "ds_read_b128 a[232:235], v224 offset:20480\n\t"
    "v_cvt_pk_bf16_f32 v168, v24, v25\n\t"
    "v_mfma_f32_16x16x32_bf16 v[36:39], a[252:255], a[72:75], v[36:39]\n\t"
    "v_exp_f32 v27, v27\n\t"
    "v_mfma_f32_16x16x32_bf16 v[40:43], a[252:255], a[80:83], v[40:43]\n\t"
    "v_fma_f32 v28, v28, s72, v243\n\t"
    "v_fma_f32 v29, v29, s72, v243\n\t"
    "v_mfma_f32_16x16x32_bf16 v[40:43], a[248:251], a[84:87], v[40:43]\n\t"
    "v_exp_f32 v28, v28\n\t"
    "v_mfma_f32_16x16x32_bf16 v[40:43], a[244:247], a[112:115], v[40:43]\n\t"
    "ds_read_b128 a[236:239], v225 offset:20480\n\t"
    "v_cvt_pk_bf16_f32 v169, v26, v27\n\t"
    "v_mfma_f32_16x16x32_bf16 v[40:43], a[240:243], a[116:119], v[40:43]\n\t"
    "v_exp_f32 v29, v29\n\t"
    "v_mfma_f32_16x16x32_bf16 v[44:47], a[240:243], a[124:127], v[44:47]\n\t"
    "v_fma_f32 v30, v30, s72, v243\n\t"
    "v_fma_f32 v31, v31, s72, v243\n\t"
    "v_mfma_f32_16x16x32_bf16 v[44:47], a[244:247], a[120:123], v[44:47]\n\t"
    "v_exp_f32 v30, v30\n\t"
    "v_mfma_f32_16x16x32_bf16 v[44:47], a[248:251], a[92:95], v[44:47]\n\t"
    "ds_read_b128 a[240:243], v224 offset:24576\n\t"
    "v_cvt_pk_bf16_f32 v172, v28, v29\n\t"
    "v_mfma_f32_16x16x32_bf16 v[44:47], a[252:255], a[88:91], v[44:47]\n\t"
    "s_waitcnt lgkmcnt(2)\n\t"
    "v_exp_f32 v31, v31\n\t"
    "v_mfma_f32_16x16x32_bf16 v[252:255], v[60:63], v[156:159], v[252:255]\n\t"
    "buffer_load_dwordx4 v234, s[48:51], s71 offen offset:1024 lds\n\t"
    "v_mfma_f32_16x16x32_bf16 v[0:3], a[224:227], a[0:3], 0\n\t"
    "v_fma_f32 v32, v32, s72, v240\n\t"
    "v_fma_f32 v33, v33, s72, v240\n\t"
    "v_mfma_f32_16x16x32_bf16 v[0:3], a[228:231], a[4:7], v[0:3]\n\t"
    "s_waitcnt lgkmcnt(1)\n\t"
    "v_exp_f32 v32, v32\n\t"
    "v_mfma_f32_16x16x32_bf16 v[0:3], a[232:235], a[32:35], v[0:3]\n\t"
    "ds_read_b128 a[244:247], v225 offset:24576\n\t"
    "v_cvt_pk_bf16_f32 v173, v30, v31\n\t"
    "v_mfma_f32_16x16x32_bf16 v[0:3], a[236:239], a[36:39], v[0:3]\n\t"
    "v_exp_f32 v33, v33\n\t"
    "v_mfma_f32_16x16x32_bf16 v[4:7], a[236:239], a[44:47], 0\n\t"
    "v_fma_f32 v34, v34, s72, v240\n\t"
    "v_fma_f32 v35, v35, s72, v240\n\t"
    "v_mfma_f32_16x16x32_bf16 v[4:7], a[232:235], a[40:43], v[4:7]\n\t"
    "v_exp_f32 v34, v34\n\t"
    "v_mfma_f32_16x16x32_bf16 v[4:7], a[228:231], a[12:15], v[4:7]\n\t"
    "ds_read_b128 a[248:251], v224 offset:28672\n\t"
    "buffer_load_dwordx4 v234, s[48:51], s71 offen offset:2048 lds\n\t"
    "v_cvt_pk_bf16_f32 v162, v32, v33\n\t"
    "v_mfma_f32_16x16x32_bf16 v[4:7], a[224:227], a[8:11], v[4:7]\n\t"
    "v_exp_f32 v35, v35\n\t"
    "v_mfma_f32_16x16x32_bf16 v[8:11], a[224:227], a[16:19], 0\n\t"
    "v_fma_f32 v36, v36, s72, v241\n\t"
    "v_fma_f32 v37, v37, s72, v241\n\t"
    "v_mfma_f32_16x16x32_bf16 v[8:11], a[228:231], a[20:23], v[8:11]\n\t"
    "v_exp_f32 v36, v36\n\t"
    "v_mfma_f32_16x16x32_bf16 v[8:11], a[232:235], a[48:51], v[8:11]\n\t"
    "ds_read_b128 a[252:255], v225 offset:28672\n\t"
    "v_cvt_pk_bf16_f32 v163, v34, v35\n\t"
    "v_mfma_f32_16x16x32_bf16 v[8:11], a[236:239], a[52:55], v[8:11]\n\t"
    "v_exp_f32 v37, v37\n\t"
    "v_mfma_f32_16x16x32_bf16 v[12:15], a[236:239], a[60:63], 0\n\t"
    "v_fma_f32 v38, v38, s72, v241\n\t"
    "v_fma_f32 v39, v39, s72, v241\n\t"
    "v_mfma_f32_16x16x32_bf16 v[12:15], a[232:235], a[56:59], v[12:15]\n\t"
    "v_exp_f32 v38, v38\n\t"
    "v_mfma_f32_16x16x32_bf16 v[12:15], a[228:231], a[28:31], v[12:15]\n\t"
    "ds_read_b128 a[236:239], v224 offset:18432\n\t"
    "v_cvt_pk_bf16_f32 v166, v36, v37\n\t"
    "v_mfma_f32_16x16x32_bf16 v[12:15], a[224:227], a[24:27], v[12:15]\n\t"
    "s_waitcnt lgkmcnt(2)\n\t"
    "v_exp_f32 v39, v39\n\t"
    "v_mfma_f32_16x16x32_bf16 v[252:255], v[48:51], v[160:163], v[252:255]\n\t"
    "buffer_load_dwordx4 v234, s[48:51], s71 offen offset:3072 lds\n\t"
    "v_mfma_f32_16x16x32_bf16 v[0:3], a[240:243], a[64:67], v[0:3]\n\t"
    "v_fma_f32 v40, v40, s72, v242\n\t"
    "v_fma_f32 v41, v41, s72, v242\n\t"
    "v_mfma_f32_16x16x32_bf16 v[0:3], a[244:247], a[68:71], v[0:3]\n\t"
    "s_waitcnt lgkmcnt(1)\n\t"
    "v_exp_f32 v40, v40\n\t"
    "v_mfma_f32_16x16x32_bf16 v[0:3], a[248:251], a[96:99], v[0:3]\n\t"
    "ds_read_b128 a[232:235], v225 offset:18432\n\t"
    "v_cvt_pk_bf16_f32 v167, v38, v39\n\t"
    "v_mfma_f32_16x16x32_bf16 v[0:3], a[252:255], a[100:103], v[0:3]\n\t"
    "v_exp_f32 v41, v41\n\t"
    "v_mfma_f32_16x16x32_bf16 v[4:7], a[252:255], a[108:111], v[4:7]\n\t"
    "v_fma_f32 v42, v42, s72, v242\n\t"
    "v_fma_f32 v43, v43, s72, v242\n\t"
    "v_mfma_f32_16x16x32_bf16 v[4:7], a[248:251], a[104:107], v[4:7]\n\t"
    "v_exp_f32 v42, v42\n\t"
    "v_mfma_f32_16x16x32_bf16 v[4:7], a[244:247], a[76:79], v[4:7]\n\t"
    "ds_read_b128 a[228:231], v224 offset:22528\n\t"
    "v_cvt_pk_bf16_f32 v170, v40, v41\n\t"
    "v_mfma_f32_16x16x32_bf16 v[4:7], a[240:243], a[72:75], v[4:7]\n\t"
    "v_exp_f32 v43, v43\n\t"
    "v_mfma_f32_16x16x32_bf16 v[8:11], a[240:243], a[80:83], v[8:11]\n\t"
    "v_fma_f32 v44, v44, s72, v243\n\t"
    "v_fma_f32 v45, v45, s72, v243\n\t"
    "v_mfma_f32_16x16x32_bf16 v[8:11], a[244:247], a[84:87], v[8:11]\n\t"
    "v_exp_f32 v44, v44\n\t"
    "v_mfma_f32_16x16x32_bf16 v[8:11], a[248:251], a[112:115], v[8:11]\n\t"
    "ds_read_b128 a[224:227], v225 offset:22528\n\t"
    "v_cvt_pk_bf16_f32 v171, v42, v43\n\t"
    "v_mfma_f32_16x16x32_bf16 v[8:11], a[252:255], a[116:119], v[8:11]\n\t"
    "v_exp_f32 v45, v45\n\t"
    "v_mfma_f32_16x16x32_bf16 v[12:15], a[252:255], a[124:127], v[12:15]\n\t"
    "s_xor_b32 s71, s67, 0x100\n\t"
    "v_fma_f32 v46, v46, s72, v243\n\t"
    "v_fma_f32 v47, v47, s72, v243\n\t"
    "v_mfma_f32_16x16x32_bf16 v[12:15], a[248:251], a[120:123], v[12:15]\n\t"
    "s_add_u32 m0, s70, 0x8000\n\t"
    "v_exp_f32 v46, v46\n\t"
    "v_mfma_f32_16x16x32_bf16 v[12:15], a[244:247], a[92:95], v[12:15]\n\t"
    "ds_read_b128 a[252:255], v224 offset:26624\n\t"
    "v_cvt_pk_bf16_f32 v174, v44, v45\n\t"
    "v_mfma_f32_16x16x32_bf16 v[12:15], a[240:243], a[88:91], v[12:15]\n\t"
    "s_waitcnt lgkmcnt(2)\n\t"
    "v_exp_f32 v47, v47\n\t"
    "v_mfma_f32_16x16x32_bf16 v[252:255], v[52:55], v[164:167], v[252:255]\n\t"
    "buffer_load_dwordx4 v232, s[52:55], s71 offen offset:0 nt lds\n\t"
    "v_mfma_f32_16x16x32_bf16 v[16:19], a[236:239], a[0:3], 0\n\t"
    "v_fma_f32 v0, v0, s72, v240\n\t"
    "v_fma_f32 v1, v1, s72, v240\n\t"
    "v_mfma_f32_16x16x32_bf16 v[16:19], a[232:235], a[4:7], v[16:19]\n\t"
    "s_waitcnt lgkmcnt(1)\n\t"
    "v_exp_f32 v0, v0\n\t"
    "v_mfma_f32_16x16x32_bf16 v[16:19], a[228:231], a[32:35], v[16:19]\n\t"
    "ds_read_b128 a[248:251], v225 offset:26624\n\t"
    "v_cvt_pk_bf16_f32 v175, v46, v47\n\t"
    "v_mfma_f32_16x16x32_bf16 v[16:19], a[224:227], a[36:39], v[16:19]\n\t"
    "v_exp_f32 v1, v1\n\t"
    "v_mfma_f32_16x16x32_bf16 v[20:23], a[224:227], a[44:47], 0\n\t"
    "v_fma_f32 v2, v2, s72, v240\n\t"
    "v_fma_f32 v3, v3, s72, v240\n\t"
    "v_mfma_f32_16x16x32_bf16 v[20:23], a[228:231], a[40:43], v[20:23]\n\t"
    "v_exp_f32 v2, v2\n\t"
    "v_mfma_f32_16x16x32_bf16 v[20:23], a[232:235], a[12:15], v[20:23]\n\t"
    "ds_read_b128 a[244:247], v224 offset:30720\n\t"
    "buffer_load_dwordx4 v233, s[52:55], s71 offen offset:1024 nt lds\n\t"
    "v_cvt_pk_bf16_f32 v176, v0, v1\n\t"
    "v_mfma_f32_16x16x32_bf16 v[20:23], a[236:239], a[8:11], v[20:23]\n\t"
    "v_exp_f32 v3, v3\n\t"
    "v_mfma_f32_16x16x32_bf16 v[24:27], a[236:239], a[16:19], 0\n\t"
    "v_fma_f32 v4, v4, s72, v241\n\t"
    "v_fma_f32 v5, v5, s72, v241\n\t"
    "v_mfma_f32_16x16x32_bf16 v[24:27], a[232:235], a[20:23], v[24:27]\n\t"
    "v_exp_f32 v4, v4\n\t"
    "v_mfma_f32_16x16x32_bf16 v[24:27], a[228:231], a[48:51], v[24:27]\n\t"
    "ds_read_b128 a[240:243], v225 offset:30720\n\t"
    "v_cvt_pk_bf16_f32 v177, v2, v3\n\t"
    "v_mfma_f32_16x16x32_bf16 v[24:27], a[224:227], a[52:55], v[24:27]\n\t"
    "v_exp_f32 v5, v5\n\t"
    "v_mfma_f32_16x16x32_bf16 v[28:31], a[224:227], a[60:63], 0\n\t"
    "v_fma_f32 v6, v6, s72, v241\n\t"
    "v_fma_f32 v7, v7, s72, v241\n\t"
    "v_mfma_f32_16x16x32_bf16 v[28:31], a[228:231], a[56:59], v[28:31]\n\t"
    "v_exp_f32 v6, v6\n\t"
    "v_mfma_f32_16x16x32_bf16 v[28:31], a[232:235], a[28:31], v[28:31]\n\t"
    "v_cvt_pk_bf16_f32 v180, v4, v5\n\t"
    "v_mfma_f32_16x16x32_bf16 v[28:31], a[236:239], a[24:27], v[28:31]\n\t"
    "s_waitcnt lgkmcnt(1)\n\t"
    "v_exp_f32 v7, v7\n\t"
    "v_mfma_f32_16x16x32_bf16 v[252:255], v[56:59], v[168:171], v[252:255]\n\t"
    "buffer_load_dwordx4 v232, s[52:55], s71 offen offset:2048 nt lds\n\t"
    "v_mfma_f32_16x16x32_bf16 v[16:19], a[252:255], a[64:67], v[16:19]\n\t"
    "v_fma_f32 v8, v8, s72, v242\n\t"
    "v_fma_f32 v9, v9, s72, v242\n\t"
    "v_mfma_f32_16x16x32_bf16 v[16:19], a[248:251], a[68:71], v[16:19]\n\t"
    "s_waitcnt lgkmcnt(0)\n\t"
    "v_exp_f32 v8, v8\n\t"
    "v_mfma_f32_16x16x32_bf16 v[16:19], a[244:247], a[96:99], v[16:19]\n\t"
    "s_waitcnt vmcnt(15)\n\t"
    "v_cvt_pk_bf16_f32 v181, v6, v7\n\t"
    "v_mfma_f32_16x16x32_bf16 v[16:19], a[240:243], a[100:103], v[16:19]\n\t"
    "s_barrier\n\t"
    "v_exp_f32 v9, v9\n\t"
    "v_mfma_f32_16x16x32_bf16 v[20:23], a[240:243], a[108:111], v[20:23]\n\t"
    "ds_read_b128 a[224:227], v224 offset:32768\n\t"
    "v_fma_f32 v10, v10, s72, v242\n\t"
    "v_fma_f32 v11, v11, s72, v242\n\t"
    "v_mfma_f32_16x16x32_bf16 v[20:23], a[244:247], a[104:107], v[20:23]\n\t"
    "ds_read_b128 a[228:231], v225 offset:32768\n\t"
    "v_exp_f32 v10, v10\n\t"
    "v_mfma_f32_16x16x32_bf16 v[20:23], a[248:251], a[76:79], v[20:23]\n\t"
    "ds_read_b128 a[232:235], v224 offset:36864\n\t"
    "v_cvt_pk_bf16_f32 v184, v8, v9\n\t"
    "v_mfma_f32_16x16x32_bf16 v[20:23], a[252:255], a[72:75], v[20:23]\n\t"
    "v_exp_f32 v11, v11\n\t"
    "v_mfma_f32_16x16x32_bf16 v[24:27], a[252:255], a[80:83], v[24:27]\n\t"
    "v_fma_f32 v12, v12, s72, v243\n\t"
    "v_fma_f32 v13, v13, s72, v243\n\t"
    "v_mfma_f32_16x16x32_bf16 v[24:27], a[248:251], a[84:87], v[24:27]\n\t"
    "v_exp_f32 v12, v12\n\t"
    "v_mfma_f32_16x16x32_bf16 v[24:27], a[244:247], a[112:115], v[24:27]\n\t"
    "ds_read_b128 a[236:239], v225 offset:36864\n\t"
    "v_cvt_pk_bf16_f32 v185, v10, v11\n\t"
    "v_mfma_f32_16x16x32_bf16 v[24:27], a[240:243], a[116:119], v[24:27]\n\t"
    "v_exp_f32 v13, v13\n\t"
    "v_mfma_f32_16x16x32_bf16 v[28:31], a[240:243], a[124:127], v[28:31]\n\t"
    "v_fma_f32 v14, v14, s72, v243\n\t"
    "v_fma_f32 v15, v15, s72, v243\n\t"
    "v_mfma_f32_16x16x32_bf16 v[28:31], a[244:247], a[120:123], v[28:31]\n\t"
    "v_exp_f32 v14, v14\n\t"
    "v_mfma_f32_16x16x32_bf16 v[28:31], a[248:251], a[92:95], v[28:31]\n\t"
    "ds_read_b128 a[240:243], v224 offset:40960\n\t"
    "v_cvt_pk_bf16_f32 v188, v12, v13\n\t"
    "v_mfma_f32_16x16x32_bf16 v[28:31], a[252:255], a[88:91], v[28:31]\n\t"
    "s_waitcnt lgkmcnt(2)\n\t"
    "v_exp_f32 v15, v15\n\t"
    "v_mfma_f32_16x16x32_bf16 v[252:255], v[60:63], v[172:175], v[252:255]\n\t"
    "buffer_load_dwordx4 v233, s[52:55], s71 offen offset:3072 nt lds\n\t"
    "v_mfma_f32_16x16x32_bf16 v[32:35], a[224:227], a[0:3], 0\n\t"
    "v_fma_f32 v16, v16, s72, v240\n\t"
    "v_fma_f32 v17, v17, s72, v240\n\t"
    "v_mfma_f32_16x16x32_bf16 v[32:35], a[228:231], a[4:7], v[32:35]\n\t"
    "s_waitcnt lgkmcnt(1)\n\t"
    "v_exp_f32 v16, v16\n\t"
    "v_mfma_f32_16x16x32_bf16 v[32:35], a[232:235], a[32:35], v[32:35]\n\t"
    "ds_read_b128 a[244:247], v225 offset:40960\n\t"
    "v_cvt_pk_bf16_f32 v189, v14, v15\n\t"
    "v_mfma_f32_16x16x32_bf16 v[32:35], a[236:239], a[36:39], v[32:35]\n\t"
    "v_exp_f32 v17, v17\n\t"
    "v_mfma_f32_16x16x32_bf16 v[36:39], a[236:239], a[44:47], 0\n\t"
    "s_xor_b32 s71, s66, 0x10000\n\t"
    "v_fma_f32 v18, v18, s72, v240\n\t"
    "v_fma_f32 v19, v19, s72, v240\n\t"
    "v_mfma_f32_16x16x32_bf16 v[36:39], a[232:235], a[40:43], v[36:39]\n\t"
    "s_add_u32 m0, s70, 0x10000\n\t"
    "v_exp_f32 v18, v18\n\t"
    "v_mfma_f32_16x16x32_bf16 v[36:39], a[228:231], a[12:15], v[36:39]\n\t"
    "ds_read_b128 a[248:251], v224 offset:45056\n\t"
    "buffer_load_dwordx4 v234, s[48:51], s71 offen offset:0 lds\n\t"
    "v_cvt_pk_bf16_f32 v178, v16, v17\n\t"
    "v_mfma_f32_16x16x32_bf16 v[36:39], a[224:227], a[8:11], v[36:39]\n\t"
    "v_exp_f32 v19, v19\n\t"
    "v_mfma_f32_16x16x32_bf16 v[40:43], a[224:227], a[16:19], 0\n\t"
    "v_fma_f32 v20, v20, s72, v241\n\t"
    "v_fma_f32 v21, v21, s72, v241\n\t"
    "v_mfma_f32_16x16x32_bf16 v[40:43], a[228:231], a[20:23], v[40:43]\n\t"
    "v_exp_f32 v20, v20\n\t"
    "v_mfma_f32_16x16x32_bf16 v[40:43], a[232:235], a[48:51], v[40:43]\n\t"
    "ds_read_b128 a[252:255], v225 offset:45056\n\t"
    "v_cvt_pk_bf16_f32 v179, v18, v19\n\t"
    "v_mfma_f32_16x16x32_bf16 v[40:43], a[236:239], a[52:55], v[40:43]\n\t"
    "v_exp_f32 v21, v21\n\t"
    "v_mfma_f32_16x16x32_bf16 v[44:47], a[236:239], a[60:63], 0\n\t"
    "v_fma_f32 v22, v22, s72, v241\n\t"
    "v_fma_f32 v23, v23, s72, v241\n\t"
    "v_mfma_f32_16x16x32_bf16 v[44:47], a[232:235], a[56:59], v[44:47]\n\t"
    "v_exp_f32 v22, v22\n\t"
    "v_mfma_f32_16x16x32_bf16 v[44:47], a[228:231], a[28:31], v[44:47]\n\t"
    "ds_read_b128 a[236:239], v224 offset:34816\n\t"
    "v_cvt_pk_bf16_f32 v182, v20, v21\n\t"
    "v_mfma_f32_16x16x32_bf16 v[44:47], a[224:227], a[24:27], v[44:47]\n\t"
    "s_waitcnt lgkmcnt(2)\n\t"
    "v_exp_f32 v23, v23\n\t"
    "v_mfma_f32_16x16x32_bf16 v[252:255], v[48:51], v[176:179], v[252:255]\n\t"
    "buffer_load_dwordx4 v234, s[48:51], s71 offen offset:1024 lds\n\t"
    "v_mfma_f32_16x16x32_bf16 v[32:35], a[240:243], a[64:67], v[32:35]\n\t"
    "v_fma_f32 v24, v24, s72, v242\n\t"
    "v_fma_f32 v25, v25, s72, v242\n\t"
    "v_mfma_f32_16x16x32_bf16 v[32:35], a[244:247], a[68:71], v[32:35]\n\t"
    "s_waitcnt lgkmcnt(1)\n\t"
    "v_exp_f32 v24, v24\n\t"
    "v_mfma_f32_16x16x32_bf16 v[32:35], a[248:251], a[96:99], v[32:35]\n\t"
    "ds_read_b128 a[232:235], v225 offset:34816\n\t"
    "v_cvt_pk_bf16_f32 v183, v22, v23\n\t"
    "v_mfma_f32_16x16x32_bf16 v[32:35], a[252:255], a[100:103], v[32:35]\n\t"
    "v_exp_f32 v25, v25\n\t"
    "v_mfma_f32_16x16x32_bf16 v[36:39], a[252:255], a[108:111], v[36:39]\n\t"
    "v_fma_f32 v26, v26, s72, v242\n\t"
    "v_fma_f32 v27, v27, s72, v242\n\t"
    "v_mfma_f32_16x16x32_bf16 v[36:39], a[248:251], a[104:107], v[36:39]\n\t"
    "v_exp_f32 v26, v26\n\t"
    "v_mfma_f32_16x16x32_bf16 v[36:39], a[244:247], a[76:79], v[36:39]\n\t"
    "ds_read_b128 a[228:231], v224 offset:38912\n\t"
    "v_cvt_pk_bf16_f32 v186, v24, v25\n\t"
    "v_mfma_f32_16x16x32_bf16 v[36:39], a[240:243], a[72:75], v[36:39]\n\t"
    "v_exp_f32 v27, v27\n\t"
    "v_mfma_f32_16x16x32_bf16 v[40:43], a[240:243], a[80:83], v[40:43]\n\t"
    "v_fma_f32 v28, v28, s72, v243\n\t"
    "v_fma_f32 v29, v29, s72, v243\n\t"
    "v_mfma_f32_16x16x32_bf16 v[40:43], a[244:247], a[84:87], v[40:43]\n\t"
    "v_exp_f32 v28, v28\n\t"
    "v_mfma_f32_16x16x32_bf16 v[40:43], a[248:251], a[112:115], v[40:43]\n\t"
    "ds_read_b128 a[224:227], v225 offset:38912\n\t"
    "v_cvt_pk_bf16_f32 v187, v26, v27\n\t"
    "v_mfma_f32_16x16x32_bf16 v[40:43], a[252:255], a[116:119], v[40:43]\n\t"
    "v_exp_f32 v29, v29\n\t"
    "v_mfma_f32_16x16x32_bf16 v[44:47], a[252:255], a[124:127], v[44:47]\n\t"
    "v_fma_f32 v30, v30, s72, v243\n\t"
    "v_fma_f32 v31, v31, s72, v243\n\t"
    "v_mfma_f32_16x16x32_bf16 v[44:47], a[248:251], a[120:123], v[44:47]\n\t"
    "v_exp_f32 v30, v30\n\t"
    "v_mfma_f32_16x16x32_bf16 v[44:47], a[244:247], a[92:95], v[44:47]\n\t"
    "ds_read_b128 a[252:255], v224 offset:43008\n\t"
    "v_cvt_pk_bf16_f32 v190, v28, v29\n\t"
    "v_mfma_f32_16x16x32_bf16 v[44:47], a[240:243], a[88:91], v[44:47]\n\t"
    "s_waitcnt lgkmcnt(2)\n\t"
    "v_exp_f32 v31, v31\n\t"
    "v_mfma_f32_16x16x32_bf16 v[252:255], v[52:55], v[180:183], v[252:255]\n\t"
    "buffer_load_dwordx4 v234, s[48:51], s71 offen offset:2048 lds\n\t"
    "v_mfma_f32_16x16x32_bf16 v[0:3], a[236:239], a[0:3], 0\n\t"
    "v_fma_f32 v32, v32, s72, v240\n\t"
    "v_fma_f32 v33, v33, s72, v240\n\t"
    "v_mfma_f32_16x16x32_bf16 v[0:3], a[232:235], a[4:7], v[0:3]\n\t"
    "s_waitcnt lgkmcnt(1)\n\t"
    "v_exp_f32 v32, v32\n\t"
    "v_mfma_f32_16x16x32_bf16 v[0:3], a[228:231], a[32:35], v[0:3]\n\t"
    "ds_read_b128 a[248:251], v225 offset:43008\n\t"
    "v_cvt_pk_bf16_f32 v191, v30, v31\n\t"
    "v_mfma_f32_16x16x32_bf16 v[0:3], a[224:227], a[36:39], v[0:3]\n\t"
    "v_exp_f32 v33, v33\n\t"
    "v_mfma_f32_16x16x32_bf16 v[4:7], a[224:227], a[44:47], 0\n\t"
    "v_fma_f32 v34, v34, s72, v240\n\t"
    "v_fma_f32 v35, v35, s72, v240\n\t"
    "v_mfma_f32_16x16x32_bf16 v[4:7], a[228:231], a[40:43], v[4:7]\n\t"
    "v_exp_f32 v34, v34\n\t"
    "v_mfma_f32_16x16x32_bf16 v[4:7], a[232:235], a[12:15], v[4:7]\n\t"
    "ds_read_b128 a[244:247], v224 offset:47104\n\t"
    "buffer_load_dwordx4 v234, s[48:51], s71 offen offset:3072 lds\n\t"
    "v_cvt_pk_bf16_f32 v192, v32, v33\n\t"
    "v_mfma_f32_16x16x32_bf16 v[4:7], a[236:239], a[8:11], v[4:7]\n\t"
    "v_exp_f32 v35, v35\n\t"
    "v_mfma_f32_16x16x32_bf16 v[8:11], a[236:239], a[16:19], 0\n\t"
    "v_fma_f32 v36, v36, s72, v241\n\t"
    "v_fma_f32 v37, v37, s72, v241\n\t"
    "v_mfma_f32_16x16x32_bf16 v[8:11], a[232:235], a[20:23], v[8:11]\n\t"
    "v_exp_f32 v36, v36\n\t"
    "v_mfma_f32_16x16x32_bf16 v[8:11], a[228:231], a[48:51], v[8:11]\n\t"
    "ds_read_b128 a[240:243], v225 offset:47104\n\t"
    "v_cvt_pk_bf16_f32 v193, v34, v35\n\t"
    "v_mfma_f32_16x16x32_bf16 v[8:11], a[224:227], a[52:55], v[8:11]\n\t"
    "v_exp_f32 v37, v37\n\t"
    "v_mfma_f32_16x16x32_bf16 v[12:15], a[224:227], a[60:63], 0\n\t"
    "v_fma_f32 v38, v38, s72, v241\n\t"
    "v_fma_f32 v39, v39, s72, v241\n\t"
    "v_mfma_f32_16x16x32_bf16 v[12:15], a[228:231], a[56:59], v[12:15]\n\t"
    "s_xor_b32 s71, s66, 0x20000\n\t"
    "v_exp_f32 v38, v38\n\t"
    "v_mfma_f32_16x16x32_bf16 v[12:15], a[232:235], a[28:31], v[12:15]\n\t"
    "s_add_u32 m0, s70, 0x14000\n\t"
    "v_cvt_pk_bf16_f32 v196, v36, v37\n\t"
    "v_mfma_f32_16x16x32_bf16 v[12:15], a[236:239], a[24:27], v[12:15]\n\t"
    "s_waitcnt lgkmcnt(1)\n\t"
    "v_exp_f32 v39, v39\n\t"
    "v_mfma_f32_16x16x32_bf16 v[252:255], v[56:59], v[184:187], v[252:255]\n\t"
    "buffer_load_dwordx4 v234, s[48:51], s71 offen offset:0 lds\n\t"
    "v_mfma_f32_16x16x32_bf16 v[0:3], a[252:255], a[64:67], v[0:3]\n\t"
    "v_fma_f32 v40, v40, s72, v242\n\t"
    "v_fma_f32 v41, v41, s72, v242\n\t"
    "v_mfma_f32_16x16x32_bf16 v[0:3], a[248:251], a[68:71], v[0:3]\n\t"
    "s_waitcnt lgkmcnt(0)\n\t"
    "v_exp_f32 v40, v40\n\t"
    "v_mfma_f32_16x16x32_bf16 v[0:3], a[244:247], a[96:99], v[0:3]\n\t"
    "s_waitcnt vmcnt(13)\n\t"
    "v_cvt_pk_bf16_f32 v197, v38, v39\n\t"
    "v_mfma_f32_16x16x32_bf16 v[0:3], a[240:243], a[100:103], v[0:3]\n\t"
    "s_barrier\n\t"
    "v_exp_f32 v41, v41\n\t"
    "v_mfma_f32_16x16x32_bf16 v[4:7], a[240:243], a[108:111], v[4:7]\n\t"
    "ds_read_b128 a[224:227], v224 offset:49152\n\t"
    "v_fma_f32 v42, v42, s72, v242\n\t"
    "v_fma_f32 v43, v43, s72, v242\n\t"
    "v_mfma_f32_16x16x32_bf16 v[4:7], a[244:247], a[104:107], v[4:7]\n\t"
    "ds_read_b128 a[228:231], v225 offset:49152\n\t"
    "v_exp_f32 v42, v42\n\t"
    "v_mfma_f32_16x16x32_bf16 v[4:7], a[248:251], a[76:79], v[4:7]\n\t"
    "ds_read_b128 a[232:235], v224 offset:53248\n\t"
    "v_cvt_pk_bf16_f32 v200, v40, v41\n\t"
    "v_mfma_f32_16x16x32_bf16 v[4:7], a[252:255], a[72:75], v[4:7]\n\t"
    "v_exp_f32 v43, v43\n\t"
    "v_mfma_f32_16x16x32_bf16 v[8:11], a[252:255], a[80:83], v[8:11]\n\t"
    "v_fma_f32 v44, v44, s72, v243\n\t"
    "v_fma_f32 v45, v45, s72, v243\n\t"
    "v_mfma_f32_16x16x32_bf16 v[8:11], a[248:251], a[84:87], v[8:11]\n\t"
    "v_exp_f32 v44, v44\n\t"
    "v_mfma_f32_16x16x32_bf16 v[8:11], a[244:247], a[112:115], v[8:11]\n\t"
    "ds_read_b128 a[236:239], v225 offset:53248\n\t"
    "v_cvt_pk_bf16_f32 v201, v42, v43\n\t"
    "v_mfma_f32_16x16x32_bf16 v[8:11], a[240:243], a[116:119], v[8:11]\n\t"
    "v_exp_f32 v45, v45\n\t"
    "v_mfma_f32_16x16x32_bf16 v[12:15], a[240:243], a[124:127], v[12:15]\n\t"
    "v_fma_f32 v46, v46, s72, v243\n\t"
    "v_fma_f32 v47, v47, s72, v243\n\t"
    "v_mfma_f32_16x16x32_bf16 v[12:15], a[244:247], a[120:123], v[12:15]\n\t"
    "v_exp_f32 v46, v46\n\t"
    "v_mfma_f32_16x16x32_bf16 v[12:15], a[248:251], a[92:95], v[12:15]\n\t"
    "ds_read_b128 a[240:243], v224 offset:57344\n\t"
    "v_cvt_pk_bf16_f32 v204, v44, v45\n\t"
    "v_mfma_f32_16x16x32_bf16 v[12:15], a[252:255], a[88:91], v[12:15]\n\t"
    "s_waitcnt lgkmcnt(2)\n\t"
    "v_exp_f32 v47, v47\n\t"
    "v_mfma_f32_16x16x32_bf16 v[252:255], v[60:63], v[188:191], v[252:255]\n\t"
    "buffer_load_dwordx4 v234, s[48:51], s71 offen offset:1024 lds\n\t"
    "v_mfma_f32_16x16x32_bf16 v[16:19], a[224:227], a[0:3], 0\n\t"
    "v_fma_f32 v0, v0, s72, v240\n\t"
    "v_fma_f32 v1, v1, s72, v240\n\t"
    "v_mfma_f32_16x16x32_bf16 v[16:19], a[228:231], a[4:7], v[16:19]\n\t"
    "s_waitcnt lgkmcnt(1)\n\t"
    "v_exp_f32 v0, v0\n\t"
    "v_mfma_f32_16x16x32_bf16 v[16:19], a[232:235], a[32:35], v[16:19]\n\t"
    "ds_read_b128 a[244:247], v225 offset:57344\n\t"
    "v_cvt_pk_bf16_f32 v205, v46, v47\n\t"
    "v_mfma_f32_16x16x32_bf16 v[16:19], a[236:239], a[36:39], v[16:19]\n\t"
    "v_exp_f32 v1, v1\n\t"
    "v_mfma_f32_16x16x32_bf16 v[20:23], a[236:239], a[44:47], 0\n\t"
    "v_fma_f32 v2, v2, s72, v240\n\t"
    "v_fma_f32 v3, v3, s72, v240\n\t"
    "v_mfma_f32_16x16x32_bf16 v[20:23], a[232:235], a[40:43], v[20:23]\n\t"
    "v_exp_f32 v2, v2\n\t"
    "v_mfma_f32_16x16x32_bf16 v[20:23], a[228:231], a[12:15], v[20:23]\n\t"
    "ds_read_b128 a[248:251], v224 offset:61440\n\t"
    "buffer_load_dwordx4 v234, s[48:51], s71 offen offset:2048 lds\n\t"
    "v_cvt_pk_bf16_f32 v194, v0, v1\n\t"
    "v_mfma_f32_16x16x32_bf16 v[20:23], a[224:227], a[8:11], v[20:23]\n\t"
    "v_exp_f32 v3, v3\n\t"
    "v_mfma_f32_16x16x32_bf16 v[24:27], a[224:227], a[16:19], 0\n\t"
    "v_fma_f32 v4, v4, s72, v241\n\t"
    "v_fma_f32 v5, v5, s72, v241\n\t"
    "v_mfma_f32_16x16x32_bf16 v[24:27], a[228:231], a[20:23], v[24:27]\n\t"
    "v_exp_f32 v4, v4\n\t"
    "v_mfma_f32_16x16x32_bf16 v[24:27], a[232:235], a[48:51], v[24:27]\n\t"
    "ds_read_b128 a[252:255], v225 offset:61440\n\t"
    "v_cvt_pk_bf16_f32 v195, v2, v3\n\t"
    "v_mfma_f32_16x16x32_bf16 v[24:27], a[236:239], a[52:55], v[24:27]\n\t"
    "v_exp_f32 v5, v5\n\t"
    "v_mfma_f32_16x16x32_bf16 v[28:31], a[236:239], a[60:63], 0\n\t"
    "v_fma_f32 v6, v6, s72, v241\n\t"
    "v_fma_f32 v7, v7, s72, v241\n\t"
    "v_mfma_f32_16x16x32_bf16 v[28:31], a[232:235], a[56:59], v[28:31]\n\t"
    "v_exp_f32 v6, v6\n\t"
    "v_mfma_f32_16x16x32_bf16 v[28:31], a[228:231], a[28:31], v[28:31]\n\t"
    "ds_read_b128 a[236:239], v224 offset:51200\n\t"
    "v_cvt_pk_bf16_f32 v198, v4, v5\n\t"
    "v_mfma_f32_16x16x32_bf16 v[28:31], a[224:227], a[24:27], v[28:31]\n\t"
    "s_waitcnt lgkmcnt(2)\n\t"
    "v_exp_f32 v7, v7\n\t"
    "v_mfma_f32_16x16x32_bf16 v[252:255], v[48:51], v[192:195], v[252:255]\n\t"
    "buffer_load_dwordx4 v234, s[48:51], s71 offen offset:3072 lds\n\t"
    "v_mfma_f32_16x16x32_bf16 v[16:19], a[240:243], a[64:67], v[16:19]\n\t"
    "v_fma_f32 v8, v8, s72, v242\n\t"
    "v_fma_f32 v9, v9, s72, v242\n\t"
    "v_mfma_f32_16x16x32_bf16 v[16:19], a[244:247], a[68:71], v[16:19]\n\t"
    "s_waitcnt lgkmcnt(1)\n\t"
    "v_exp_f32 v8, v8\n\t"
    "v_mfma_f32_16x16x32_bf16 v[16:19], a[248:251], a[96:99], v[16:19]\n\t"
    "ds_read_b128 a[232:235], v225 offset:51200\n\t"
    "v_cvt_pk_bf16_f32 v199, v6, v7\n\t"
    "v_mfma_f32_16x16x32_bf16 v[16:19], a[252:255], a[100:103], v[16:19]\n\t"
    "v_exp_f32 v9, v9\n\t"
    "v_mfma_f32_16x16x32_bf16 v[20:23], a[252:255], a[108:111], v[20:23]\n\t"
    "v_fma_f32 v10, v10, s72, v242\n\t"
    "v_fma_f32 v11, v11, s72, v242\n\t"
    "v_mfma_f32_16x16x32_bf16 v[20:23], a[248:251], a[104:107], v[20:23]\n\t"
    "v_exp_f32 v10, v10\n\t"
    "v_mfma_f32_16x16x32_bf16 v[20:23], a[244:247], a[76:79], v[20:23]\n\t"
    "ds_read_b128 a[228:231], v224 offset:55296\n\t"
    "v_cvt_pk_bf16_f32 v202, v8, v9\n\t"
    "v_mfma_f32_16x16x32_bf16 v[20:23], a[240:243], a[72:75], v[20:23]\n\t"
    "v_exp_f32 v11, v11\n\t"
    "v_mfma_f32_16x16x32_bf16 v[24:27], a[240:243], a[80:83], v[24:27]\n\t"
    "v_fma_f32 v12, v12, s72, v243\n\t"
    "v_fma_f32 v13, v13, s72, v243\n\t"
    "v_mfma_f32_16x16x32_bf16 v[24:27], a[244:247], a[84:87], v[24:27]\n\t"
    "v_exp_f32 v12, v12\n\t"
    "v_mfma_f32_16x16x32_bf16 v[24:27], a[248:251], a[112:115], v[24:27]\n\t"
    "ds_read_b128 a[224:227], v225 offset:55296\n\t"
    "v_cvt_pk_bf16_f32 v203, v10, v11\n\t"
    "v_mfma_f32_16x16x32_bf16 v[24:27], a[252:255], a[116:119], v[24:27]\n\t"
    "v_exp_f32 v13, v13\n\t"
    "v_mfma_f32_16x16x32_bf16 v[28:31], a[252:255], a[124:127], v[28:31]\n\t"
    "s_xor_b32 s71, s66, 0x30000\n\t"
    "v_fma_f32 v14, v14, s72, v243\n\t"
    "v_fma_f32 v15, v15, s72, v243\n\t"
    "v_mfma_f32_16x16x32_bf16 v[28:31], a[248:251], a[120:123], v[28:31]\n\t"
    "s_add_u32 m0, s70, 0x18000\n\t"
    "v_exp_f32 v14, v14\n\t"
    "v_mfma_f32_16x16x32_bf16 v[28:31], a[244:247], a[92:95], v[28:31]\n\t"
    "ds_read_b128 a[252:255], v224 offset:59392\n\t"
    "v_cvt_pk_bf16_f32 v206, v12, v13\n\t"
    "v_mfma_f32_16x16x32_bf16 v[28:31], a[240:243], a[88:91], v[28:31]\n\t"
    "s_waitcnt lgkmcnt(2)\n\t"
    "v_exp_f32 v15, v15\n\t"
    "v_mfma_f32_16x16x32_bf16 v[252:255], v[52:55], v[196:199], v[252:255]\n\t"
    "buffer_load_dwordx4 v234, s[48:51], s71 offen offset:0 lds\n\t"
    "v_mfma_f32_16x16x32_bf16 v[32:35], a[236:239], a[0:3], 0\n\t"
    "v_fma_f32 v16, v16, s72, v240\n\t"
    "v_fma_f32 v17, v17, s72, v240\n\t"
    "v_mfma_f32_16x16x32_bf16 v[32:35], a[232:235], a[4:7], v[32:35]\n\t"
    "s_waitcnt lgkmcnt(1)\n\t"
    "v_exp_f32 v16, v16\n\t"
    "v_mfma_f32_16x16x32_bf16 v[32:35], a[228:231], a[32:35], v[32:35]\n\t"
    "ds_read_b128 a[248:251], v225 offset:59392\n\t"
    "v_cvt_pk_bf16_f32 v207, v14, v15\n\t"
    "v_mfma_f32_16x16x32_bf16 v[32:35], a[224:227], a[36:39], v[32:35]\n\t"
    "v_exp_f32 v17, v17\n\t"
    "v_mfma_f32_16x16x32_bf16 v[36:39], a[224:227], a[44:47], 0\n\t"
    "v_fma_f32 v18, v18, s72, v240\n\t"
    "v_fma_f32 v19, v19, s72, v240\n\t"
    "v_mfma_f32_16x16x32_bf16 v[36:39], a[228:231], a[40:43], v[36:39]\n\t"
    "v_exp_f32 v18, v18\n\t"
    "v_mfma_f32_16x16x32_bf16 v[36:39], a[232:235], a[12:15], v[36:39]\n\t"
    "ds_read_b128 a[244:247], v224 offset:63488\n\t"
    "buffer_load_dwordx4 v234, s[48:51], s71 offen offset:1024 lds\n\t"
    "v_cvt_pk_bf16_f32 v208, v16, v17\n\t"
    "v_mfma_f32_16x16x32_bf16 v[36:39], a[236:239], a[8:11], v[36:39]\n\t"
    "v_exp_f32 v19, v19\n\t"
    "v_mfma_f32_16x16x32_bf16 v[40:43], a[236:239], a[16:19], 0\n\t"
    "v_fma_f32 v20, v20, s72, v241\n\t"
    "v_fma_f32 v21, v21, s72, v241\n\t"
    "v_mfma_f32_16x16x32_bf16 v[40:43], a[232:235], a[20:23], v[40:43]\n\t"
    "v_exp_f32 v20, v20\n\t"
    "v_mfma_f32_16x16x32_bf16 v[40:43], a[228:231], a[48:51], v[40:43]\n\t"
    "ds_read_b128 a[240:243], v225 offset:63488\n\t"
    "v_cvt_pk_bf16_f32 v209, v18, v19\n\t"
    "v_mfma_f32_16x16x32_bf16 v[40:43], a[224:227], a[52:55], v[40:43]\n\t"
    "v_exp_f32 v21, v21\n\t"
    "v_mfma_f32_16x16x32_bf16 v[44:47], a[224:227], a[60:63], 0\n\t"
    "v_fma_f32 v22, v22, s72, v241\n\t"
    "v_fma_f32 v23, v23, s72, v241\n\t"
    "v_mfma_f32_16x16x32_bf16 v[44:47], a[228:231], a[56:59], v[44:47]\n\t"
    "v_exp_f32 v22, v22\n\t"
    "v_mfma_f32_16x16x32_bf16 v[44:47], a[232:235], a[28:31], v[44:47]\n\t"
    "v_cvt_pk_bf16_f32 v212, v20, v21\n\t"
    "v_add_u32 v229, 0xffff0000, v229\n\t"
    "v_mfma_f32_16x16x32_bf16 v[44:47], a[236:239], a[24:27], v[44:47]\n\t"
    "s_waitcnt lgkmcnt(1)\n\t"
    "v_exp_f32 v23, v23\n\t"
    "v_mfma_f32_16x16x32_bf16 v[252:255], v[56:59], v[200:203], v[252:255]\n\t"
    "buffer_load_dwordx4 v234, s[48:51], s71 offen offset:2048 lds\n\t"
    "v_mfma_f32_16x16x32_bf16 v[32:35], a[252:255], a[64:67], v[32:35]\n\t"
    "v_fma_f32 v24, v24, s72, v242\n\t"
    "v_fma_f32 v25, v25, s72, v242\n\t"
    "v_mfma_f32_16x16x32_bf16 v[32:35], a[248:251], a[68:71], v[32:35]\n\t"
    "s_waitcnt lgkmcnt(0)\n\t"
    "v_exp_f32 v24, v24\n\t"
    "v_mfma_f32_16x16x32_bf16 v[32:35], a[244:247], a[96:99], v[32:35]\n\t"
    "s_waitcnt vmcnt(15)\n\t"
    "v_cvt_pk_bf16_f32 v213, v22, v23\n\t"
    "v_add_u32 v228, 0xffff0000, v228\n\t"
    "v_mfma_f32_16x16x32_bf16 v[32:35], a[240:243], a[100:103], v[32:35]\n\t"
    "s_barrier\n\t"
    "v_exp_f32 v25, v25\n\t"
    "v_mfma_f32_16x16x32_bf16 v[36:39], a[240:243], a[108:111], v[36:39]\n\t"
    "ds_read_b64_tr_b16 a[224:225], v228 offset:0\n\t"
    "v_fma_f32 v26, v26, s72, v242\n\t"
    "v_fma_f32 v27, v27, s72, v242\n\t"
    "v_mfma_f32_16x16x32_bf16 v[36:39], a[244:247], a[104:107], v[36:39]\n\t"
    "ds_read_b64_tr_b16 a[226:227], v228 offset:2048\n\t"
    "v_exp_f32 v26, v26\n\t"
    "v_mfma_f32_16x16x32_bf16 v[36:39], a[248:251], a[76:79], v[36:39]\n\t"
    "ds_read_b64_tr_b16 a[228:229], v228 offset:4096\n\t"
    "v_cvt_pk_bf16_f32 v216, v24, v25\n\t"
    "v_mfma_f32_16x16x32_bf16 v[36:39], a[252:255], a[72:75], v[36:39]\n\t"
    "ds_read_b64_tr_b16 a[230:231], v228 offset:6144\n\t"
    "v_exp_f32 v27, v27\n\t"
    "v_mfma_f32_16x16x32_bf16 v[40:43], a[252:255], a[80:83], v[40:43]\n\t"
    "ds_read_b64_tr_b16 a[232:233], v228 offset:8192\n\t"
    "v_fma_f32 v28, v28, s72, v243\n\t"
    "v_fma_f32 v29, v29, s72, v243\n\t"
    "v_mfma_f32_16x16x32_bf16 v[40:43], a[248:251], a[84:87], v[40:43]\n\t"
    "ds_read_b64_tr_b16 a[234:235], v228 offset:10240\n\t"
    "v_exp_f32 v28, v28\n\t"
    "v_mfma_f32_16x16x32_bf16 v[40:43], a[244:247], a[112:115], v[40:43]\n\t"
    "ds_read_b64_tr_b16 a[236:237], v228 offset:12288\n\t"
    "v_cvt_pk_bf16_f32 v217, v26, v27\n\t"
    "v_mfma_f32_16x16x32_bf16 v[40:43], a[240:243], a[116:119], v[40:43]\n\t"
    "ds_read_b64_tr_b16 a[238:239], v228 offset:14336\n\t"
    "v_exp_f32 v29, v29\n\t"
    "v_mfma_f32_16x16x32_bf16 v[44:47], a[240:243], a[124:127], v[44:47]\n\t"
    "v_fma_f32 v30, v30, s72, v243\n\t"
    "v_fma_f32 v31, v31, s72, v243\n\t"
    "v_mfma_f32_16x16x32_bf16 v[44:47], a[244:247], a[120:123], v[44:47]\n\t"
    "v_exp_f32 v30, v30\n\t"
    "v_mfma_f32_16x16x32_bf16 v[44:47], a[248:251], a[92:95], v[44:47]\n\t"
    "ds_read_b64_tr_b16 a[240:241], v229 offset:0\n\t"
    "v_cvt_pk_bf16_f32 v220, v28, v29\n\t"
    "v_mfma_f32_16x16x32_bf16 v[44:47], a[252:255], a[88:91], v[44:47]\n\t"
    "ds_read_b64_tr_b16 a[242:243], v229 offset:2048\n\t"
    "v_exp_f32 v31, v31\n\t"
    "v_mfma_f32_16x16x32_bf16 v[252:255], v[60:63], v[204:207], v[252:255]\n\t"
    "s_waitcnt lgkmcnt(4)\n\t"
    "buffer_load_dwordx4 v234, s[48:51], s71 offen offset:3072 lds\n\t"
    "v_mfma_f32_16x16x32_bf16 v[0:3], a[224:227], v[64:67], 0\n\t"
    "ds_read_b64_tr_b16 a[244:245], v229 offset:4096\n\t"
    "v_fma_f32 v32, v32, s72, v240\n\t"
    "v_fma_f32 v33, v33, s72, v240\n\t"
    "v_exp_f32 v32, v32\n\t"
    "v_exp_f32 v33, v33\n\t"
    "v_mfma_f32_16x16x32_bf16 v[0:3], a[228:231], v[80:83], v[0:3]\n\t"
    "s_waitcnt lgkmcnt(3)\n\t"
    "v_fma_f32 v34, v34, s72, v240\n\t"
    "v_fma_f32 v35, v35, s72, v240\n\t"
    "v_exp_f32 v34, v34\n\t"
    "v_cvt_pk_bf16_f32 v210, v32, v33\n\t"
    "v_mfma_f32_16x16x32_bf16 v[0:3], a[232:235], a[128:131], v[0:3]\n\t"
    "ds_read_b64_tr_b16 a[246:247], v229 offset:6144\n\t"
    "v_cvt_pk_bf16_f32 v221, v30, v31\n\t"
    "v_mfma_f32_16x16x32_bf16 v[0:3], a[236:239], a[144:147], v[0:3]\n\t"
    "s_waitcnt vmcnt(52)\n\t"
    "v_exp_f32 v35, v35\n\t"
    "v_add_u32 v226, 0xffff0000, v226\n\t"
    "v_mfma_f32_16x16x32_bf16 v[4:7], a[236:239], a[148:151], 0\n\t"
    "s_xor_b32 s71, s66, 0x80\n\t"
    "s_add_u32 m0, s70, 0x1c000\n\t"
    "ds_read_b64_tr_b16 a[248:249], v229 offset:8192\n\t"
    "ds_read_b128 a[0:3], v226 offset:16384\n\t"
    "v_fma_f32 v36, v36, s72, v241\n\t"
    "v_add_u32 v227, 0xffff0000, v227\n\t"
    "v_mfma_f32_16x16x32_bf16 v[4:7], a[232:235], a[132:135], v[4:7]\n\t"
    "buffer_load_dwordx4 v234, s[48:51], s71 offen offset:0 lds\n\t"
    "ds_read_b128 a[4:7], v227 offset:16384\n\t"
    "v_mfma_f32_16x16x32_bf16 v[4:7], a[228:231], v[84:87], v[4:7]\n\t"
    "ds_read_b64_tr_b16 a[250:251], v229 offset:10240\n\t"
    "ds_read_b128 a[8:11], v226 offset:18432\n\t"
    "v_fma_f32 v37, v37, s72, v241\n\t"
    "v_mfma_f32_16x16x32_bf16 v[4:7], a[224:227], v[68:71], v[4:7]\n\t"
    "ds_read_b128 a[12:15], v227 offset:18432\n\t"
    "v_exp_f32 v36, v36\n\t"
    "v_mfma_f32_16x16x32_bf16 v[8:11], a[224:227], v[72:75], 0\n\t"
    "ds_read_b64_tr_b16 a[252:253], v229 offset:12288\n\t"
    "v_cvt_pk_bf16_f32 v211, v34, v35\n\t"
    "v_mfma_f32_16x16x32_bf16 v[8:11], a[228:231], v[88:91], v[8:11]\n\t"
    "s_waitcnt vmcnt(45)\n\t"
    "v_exp_f32 v37, v37\n\t"
    "v_fma_f32 v38, v38, s72, v241\n\t"
    "v_mfma_f32_16x16x32_bf16 v[8:11], a[232:235], a[136:139], v[8:11]\n\t"
    "ds_read_b64_tr_b16 a[254:255], v229 offset:14336\n\t"
    "ds_read_b128 a[16:19], v226 offset:49152\n\t"
    "v_fma_f32 v39, v39, s72, v241\n\t"
    "v_mfma_f32_16x16x32_bf16 v[8:11], a[236:239], a[152:155], v[8:11]\n\t"
    "ds_read_b128 a[20:23], v227 offset:49152\n\t"
    "v_exp_f32 v38, v38\n\t"
    "v_add_u32 v230, 0xffff0000, v230\n\t"
    "v_mfma_f32_16x16x32_bf16 v[12:15], a[236:239], a[156:159], 0\n\t"
    "ds_read_b64_tr_b16 a[236:237], v230 offset:0\n\t"
    "ds_read_b128 a[24:27], v226 offset:51200\n\t"
    "v_cvt_pk_bf16_f32 v214, v36, v37\n\t"
    "v_mfma_f32_16x16x32_bf16 v[12:15], a[232:235], a[140:143], v[12:15]\n\t"
    "v_exp_f32 v39, v39\n\t"
    "v_mfma_f32_16x16x32_bf16 v[12:15], a[228:231], v[92:95], v[12:15]\n\t"
    "ds_read_b64_tr_b16 a[238:239], v230 offset:2048\n\t"
    "v_fma_f32 v40, v40, s72, v242\n\t"
    "v_mfma_f32_16x16x32_bf16 v[12:15], a[224:227], v[76:79], v[12:15]\n\t"
    "s_waitcnt lgkmcnt(9)\n\t"
    "buffer_load_dwordx4 v234, s[48:51], s71 offen offset:1024 lds\n\t"
    "v_mfma_f32_16x16x32_bf16 v[16:19], a[240:243], v[64:67], 0\n\t"
    "ds_read_b64_tr_b16 a[232:233], v230 offset:4096\n\t"
    "v_fma_f32 v41, v41, s72, v242\n\t"
    "v_mfma_f32_16x16x32_bf16 v[16:19], a[244:247], v[80:83], v[16:19]\n\t"
    "s_waitcnt lgkmcnt(6)\n\t"
    "v_exp_f32 v40, v40\n\t"
    "v_mfma_f32_16x16x32_bf16 v[16:19], a[248:251], a[128:131], v[16:19]\n\t"
    "ds_read_b64_tr_b16 a[234:235], v230 offset:6144\n\t"
    "v_cvt_pk_bf16_f32 v215, v38, v39\n\t"
    "v_mfma_f32_16x16x32_bf16 v[16:19], a[252:255], a[144:147], v[16:19]\n\t"
    "v_exp_f32 v41, v41\n\t"
    "v_fma_f32 v42, v42, s72, v242\n\t"
    "v_mfma_f32_16x16x32_bf16 v[20:23], a[252:255], a[148:151], 0\n\t"
    "ds_read_b64_tr_b16 a[228:229], v230 offset:8192\n\t"
    "v_fma_f32 v43, v43, s72, v242\n\t"
    "v_mfma_f32_16x16x32_bf16 v[20:23], a[248:251], a[132:135], v[20:23]\n\t"
    "v_exp_f32 v42, v42\n\t"
    "v_cvt_pk_bf16_f32 v218, v40, v41\n\t"
    "v_mfma_f32_16x16x32_bf16 v[20:23], a[244:247], v[84:87], v[20:23]\n\t"
    "ds_read_b64_tr_b16 a[230:231], v230 offset:10240\n\t"
    "v_exp_f32 v43, v43\n\t"
    "v_mfma_f32_16x16x32_bf16 v[20:23], a[240:243], v[68:71], v[20:23]\n\t"
    "v_fma_f32 v44, v44, s72, v243\n\t"
    "v_fma_f32 v45, v45, s72, v243\n\t"
    "v_mfma_f32_16x16x32_bf16 v[24:27], a[240:243], v[72:75], 0\n\t"
    "ds_read_b64_tr_b16 a[224:225], v230 offset:12288\n\t"
    "v_exp_f32 v44, v44\n\t"
    "v_mfma_f32_16x16x32_bf16 v[24:27], a[244:247], v[88:91], v[24:27]\n\t"
    "buffer_load_dwordx4 v234, s[48:51], s71 offen offset:2048 lds\n\t"
    "v_mfma_f32_16x16x32_bf16 v[24:27], a[248:251], a[136:139], v[24:27]\n\t"
    "ds_read_b64_tr_b16 a[226:227], v230 offset:14336\n\t"
    "v_cvt_pk_bf16_f32 v219, v42, v43\n\t"
    "v_mfma_f32_16x16x32_bf16 v[24:27], a[252:255], a[152:155], v[24:27]\n\t"
    "v_exp_f32 v45, v45\n\t"
    "v_fma_f32 v46, v46, s72, v243\n\t"
    "v_add_u32 v231, 0xffff0000, v231\n\t"
    "v_mfma_f32_16x16x32_bf16 v[28:31], a[252:255], a[156:159], 0\n\t"
    "ds_read_b64_tr_b16 a[252:253], v231 offset:0\n\t"
    "v_fma_f32 v47, v47, s72, v243\n\t"
    "v_mfma_f32_16x16x32_bf16 v[28:31], a[248:251], a[140:143], v[28:31]\n\t"
    "v_exp_f32 v46, v46\n\t"
    "v_cvt_pk_bf16_f32 v222, v44, v45\n\t"
    "v_mfma_f32_16x16x32_bf16 v[28:31], a[244:247], v[92:95], v[28:31]\n\t"
    "ds_read_b64_tr_b16 a[254:255], v231 offset:2048\n\t"
    "v_exp_f32 v47, v47\n\t"
    "v_mfma_f32_16x16x32_bf16 v[28:31], a[240:243], v[76:79], v[28:31]\n\t"
    "s_waitcnt lgkmcnt(4)\n\t"
    "v_cvt_pk_bf16_f32 v223, v46, v47\n\t"
    "v_mfma_f32_16x16x32_bf16 v[32:35], a[236:239], v[64:67], 0\n\t"
    "ds_read_b64_tr_b16 a[248:249], v231 offset:4096\n\t"
    "ds_read_b128 a[28:31], v227 offset:51200\n\t"
    "v_mfma_f32_16x16x32_bf16 v[32:35], a[232:235], v[80:83], v[32:35]\n\t"
    "v_mfma_f32_16x16x32_bf16 v[32:35], a[228:231], a[128:131], v[32:35]\n\t"
    "s_waitcnt lgkmcnt(4)\n\t"
    "ds_read_b64_tr_b16 a[250:251], v231 offset:6144\n\t"
    "v_mfma_f32_16x16x32_bf16 v[32:35], a[224:227], a[144:147], v[32:35]\n\t"
    "buffer_load_dwordx4 v234, s[48:51], s71 offen offset:3072 lds\n\t"
    "v_mfma_f32_16x16x32_bf16 v[252:255], v[48:51], v[208:211], v[252:255]\n\t"
    "v_mfma_f32_16x16x32_bf16 v[36:39], a[224:227], a[148:151], 0\n\t"
    "ds_read_b64_tr_b16 a[244:245], v231 offset:8192\n\t"
    "v_mfma_f32_16x16x32_bf16 v[36:39], a[228:231], a[132:135], v[36:39]\n\t"
    "v_mfma_f32_16x16x32_bf16 v[36:39], a[232:235], v[84:87], v[36:39]\n\t"
    "ds_read_b64_tr_b16 a[246:247], v231 offset:10240\n\t"
    "v_mfma_f32_16x16x32_bf16 v[36:39], a[236:239], v[68:71], v[36:39]\n\t"
    "v_mfma_f32_16x16x32_bf16 v[252:255], v[52:55], v[212:215], v[252:255]\n\t"
    "v_mfma_f32_16x16x32_bf16 v[40:43], a[236:239], v[72:75], 0\n\t"
    "ds_read_b64_tr_b16 a[240:241], v231 offset:12288\n\t"
    "v_mfma_f32_16x16x32_bf16 v[40:43], a[232:235], v[88:91], v[40:43]\n\t"
    "v_mfma_f32_16x16x32_bf16 v[40:43], a[228:231], a[136:139], v[40:43]\n\t"
    "ds_read_b64_tr_b16 a[242:243], v231 offset:14336\n\t"
    "v_mfma_f32_16x16x32_bf16 v[40:43], a[224:227], a[152:155], v[40:43]\n\t"
    "s_xor_b32 s71, s67, 0x4100\n\t"
    "s_add_u32 m0, s70, 0x4000\n\t"
    "v_mfma_f32_16x16x32_bf16 v[252:255], v[56:59], v[216:219], v[252:255]\n\t"
    "buffer_load_dwordx4 v232, s[52:55], s71 offen offset:0 nt lds\n\t"
    "v_mfma_f32_16x16x32_bf16 v[44:47], a[224:227], a[156:159], 0\n\t"
    "v_mfma_f32_16x16x32_bf16 v[44:47], a[228:231], a[140:143], v[44:47]\n\t"
    "v_mfma_f32_16x16x32_bf16 v[44:47], a[232:235], v[92:95], v[44:47]\n\t"
    "v_mfma_f32_16x16x32_bf16 v[44:47], a[236:239], v[76:79], v[44:47]\n\t"
    "v_mfma_f32_16x16x32_bf16 v[252:255], v[60:63], v[220:223], v[252:255]\n\t"
    "s_waitcnt lgkmcnt(2)\n\t"
    "v_mfma_f32_16x16x32_bf16 v[48:51], a[252:255], v[64:67], 0\n\t"
    "buffer_load_dwordx4 v233, s[52:55], s71 offen offset:1024 nt lds\n\t"
    "v_mfma_f32_16x16x32_bf16 v[48:51], a[248:251], v[80:83], v[48:51]\n\t"
    "s_waitcnt lgkmcnt(0)\n\t"
    "v_mfma_f32_16x16x32_bf16 v[48:51], a[244:247], a[128:131], v[48:51]\n\t"
    "s_waitcnt vmcnt(14)\n\t"
    "v_add_u32 v228, 0x10000, v228\n\t"
    "v_mfma_f32_16x16x32_bf16 v[48:51], a[240:243], a[144:147], v[48:51]\n\t"
    "s_barrier\n\t"
    "v_mfma_f32_16x16x32_bf16 v[52:55], a[240:243], a[148:151], 0\n\t"
    "ds_read_b64_tr_b16 a[224:225], v228 offset:0\n\t"
    "ds_read_b64_tr_b16 a[226:227], v228 offset:2048\n\t"
    "v_mfma_f32_16x16x32_bf16 v[52:55], a[244:247], a[132:135], v[52:55]\n\t"
    "ds_read_b64_tr_b16 a[228:229], v228 offset:4096\n\t"
    "ds_read_b64_tr_b16 a[230:231], v228 offset:6144\n\t"
    "v_mfma_f32_16x16x32_bf16 v[52:55], a[248:251], v[84:87], v[52:55]\n\t"
    "ds_read_b64_tr_b16 a[232:233], v228 offset:8192\n\t"
    "v_cmp_nge_f32 vcc, s78, v252\n\t"
    "v_mfma_f32_16x16x32_bf16 v[52:55], a[252:255], v[68:71], v[52:55]\n\t"
    "ds_read_b64_tr_b16 a[234:235], v228 offset:10240\n\t"
    "v_mfma_f32_16x16x32_bf16 v[56:59], a[252:255], v[72:75], 0\n\t"
    "ds_read_b64_tr_b16 a[236:237], v228 offset:12288\n\t"
    "v_mfma_f32_16x16x32_bf16 v[56:59], a[248:251], v[88:91], v[56:59]\n\t"
    "v_rcp_f32 v252, v252\n\t"
    "v_mfma_f32_16x16x32_bf16 v[56:59], a[244:247], a[136:139], v[56:59]\n\t"
    "s_or_b64 s[76:77], s[76:77], vcc\n\t"
    "ds_read_b64_tr_b16 a[238:239], v228 offset:14336\n\t"
    "v_mfma_f32_16x16x32_bf16 v[56:59], a[240:243], a[152:155], v[56:59]\n\t"
    "v_add_u32 v229, 0x10000, v229\n\t"
    "v_cmp_nge_f32 vcc, s78, v253\n\t"
    "v_mfma_f32_16x16x32_bf16 v[60:63], a[240:243], a[156:159], 0\n\t"
    "ds_read_b64_tr_b16 a[240:241], v229 offset:0\n\t"
    "v_mfma_f32_16x16x32_bf16 v[60:63], a[244:247], a[140:143], v[60:63]\n\t"
    "buffer_load_dwordx4 v232, s[52:55], s71 offen offset:2048 nt lds\n\t"
    "v_mfma_f32_16x16x32_bf16 v[60:63], a[248:251], v[92:95], v[60:63]\n\t"
    "ds_read_b64_tr_b16 a[242:243], v229 offset:2048\n\t"
    "v_mfma_f32_16x16x32_bf16 v[60:63], a[252:255], v[76:79], v[60:63]\n\t"
    "s_waitcnt lgkmcnt(4)\n\t"
    "v_add_u32 v226, 0x20000, v226\n\t"
    "v_mfma_f32_16x16x32_bf16 v[0:3], a[224:227], a[160:163], v[0:3]\n\t"
    "s_waitcnt vmcnt(43)\n\t"
    "ds_read_b64_tr_b16 a[244:245], v229 offset:4096\n\t"
    "v_mfma_f32_16x16x32_bf16 v[0:3], a[228:231], a[176:179], v[0:3]\n\t"
    "ds_read_b128 a[32:35], v226 offset:0\n\t"
    "v_add_u32 v227, 0x20000, v227\n\t"
    "v_mfma_f32_16x16x32_bf16 v[0:3], a[232:235], a[192:195], v[0:3]\n\t"
    "s_waitcnt lgkmcnt(4)\n\t"
    "ds_read_b64_tr_b16 a[246:247], v229 offset:6144\n\t"
    "v_mfma_f32_16x16x32_bf16 v[0:3], a[236:239], a[208:211], v[0:3]\n\t"
    "ds_read_b128 a[36:39], v227 offset:0\n\t"
    "v_mfma_f32_16x16x32_bf16 v[4:7], a[236:239], a[212:215], v[4:7]\n\t"
    "ds_read_b64_tr_b16 a[248:249], v229 offset:8192\n\t"
    "ds_read_b128 a[40:43], v226 offset:2048\n\t"
    "v_mfma_f32_16x16x32_bf16 v[4:7], a[232:235], a[196:199], v[4:7]\n\t"
    "ds_read_b128 a[44:47], v227 offset:2048\n\t"
    "v_mfma_f32_16x16x32_bf16 v[4:7], a[228:231], a[180:183], v[4:7]\n\t"
    "s_waitcnt vmcnt(31)\n\t"
    "ds_read_b64_tr_b16 a[250:251], v229 offset:10240\n\t"
    "v_mfma_f32_16x16x32_bf16 v[4:7], a[224:227], a[164:167], v[4:7]\n\t"
    "buffer_load_dwordx4 v233, s[52:55], s71 offen offset:3072 nt lds\n\t"
    "v_mfma_f32_16x16x32_bf16 v[8:11], a[224:227], a[168:171], v[8:11]\n\t"
    "ds_read_b64_tr_b16 a[252:253], v229 offset:12288\n\t"
    "ds_read_b128 a[48:51], v226 offset:16384\n\t"
    "v_mfma_f32_16x16x32_bf16 v[8:11], a[228:231], a[184:187], v[8:11]\n\t"
    "ds_read_b128 a[52:55], v227 offset:16384\n\t"
    "v_mfma_f32_16x16x32_bf16 v[8:11], a[232:235], a[200:203], v[8:11]\n\t"
    "ds_read_b64_tr_b16 a[254:255], v229 offset:14336\n\t"
    "ds_read_b128 a[56:59], v226 offset:18432\n\t"
    "v_mfma_f32_16x16x32_bf16 v[8:11], a[236:239], a[216:219], v[8:11]\n\t"
    "ds_read_b128 a[60:63], v227 offset:18432\n\t"
    "v_add_u32 v230, 0x10000, v230\n\t"
    "v_mfma_f32_16x16x32_bf16 v[12:15], a[236:239], a[220:223], v[12:15]\n\t"
    "ds_read_b64_tr_b16 a[236:237], v230 offset:0\n\t"
    "v_mfma_f32_16x16x32_bf16 v[12:15], a[232:235], a[204:207], v[12:15]\n\t"
    "v_rcp_f32 v253, v253\n\t"
    "v_mfma_f32_16x16x32_bf16 v[12:15], a[228:231], a[188:191], v[12:15]\n\t"
    "s_or_b64 s[76:77], s[76:77], vcc\n\t"
    "ds_read_b64_tr_b16 a[238:239], v230 offset:2048\n\t"
    "v_mfma_f32_16x16x32_bf16 v[12:15], a[224:227], a[172:175], v[12:15]\n\t"
    "s_waitcnt lgkmcnt(8)\n\t"
    "v_cmp_nge_f32 vcc, s78, v254\n\t"
    "v_mfma_f32_16x16x32_bf16 v[16:19], a[240:243], a[160:163], v[16:19]\n\t"
    "ds_read_b64_tr_b16 a[232:233], v230 offset:4096\n\t"
    "v_mfma_f32_16x16x32_bf16 v[16:19], a[244:247], a[176:179], v[16:19]\n\t"
    "s_xor_b32 s71, s66, 0x10080\n\t"
    "s_add_u32 m0, s70, 0x0\n\t"
    "v_mfma_f32_16x16x32_bf16 v[16:19], a[248:251], a[192:195], v[16:19]\n\t"
    "s_waitcnt lgkmcnt(5)\n\t"
    "ds_read_b64_tr_b16 a[234:235], v230 offset:6144\n\t"
    "v_mfma_f32_16x16x32_bf16 v[16:19], a[252:255], a[208:211], v[16:19]\n\t"
    "buffer_load_dwordx4 v234, s[48:51], s71 offen offset:0 lds\n\t"
    "v_mfma_f32_16x16x32_bf16 v[20:23], a[252:255], a[212:215], v[20:23]\n\t"
    "ds_read_b64_tr_b16 a[228:229], v230 offset:8192\n\t"
    "v_mfma_f32_16x16x32_bf16 v[20:23], a[248:251], a[196:199], v[20:23]\n\t"
    "v_rcp_f32 v254, v254\n\t"
    "v_mfma_f32_16x16x32_bf16 v[20:23], a[244:247], a[180:183], v[20:23]\n\t"
    "s_or_b64 s[76:77], s[76:77], vcc\n\t"
    "ds_read_b64_tr_b16 a[230:231], v230 offset:10240\n\t"
    "v_mfma_f32_16x16x32_bf16 v[20:23], a[240:243], a[164:167], v[20:23]\n\t"
    "v_cmp_nge_f32 vcc, s78, v255\n\t"
    "v_mfma_f32_16x16x32_bf16 v[24:27], a[240:243], a[168:171], v[24:27]\n\t"
    "ds_read_b64_tr_b16 a[224:225], v230 offset:12288\n\t"
    "v_mfma_f32_16x16x32_bf16 v[24:27], a[244:247], a[184:187], v[24:27]\n\t"
    "v_rcp_f32 v255, v255\n\t"
    "v_mfma_f32_16x16x32_bf16 v[24:27], a[248:251], a[200:203], v[24:27]\n\t"
    "s_or_b64 s[76:77], s[76:77], vcc\n\t"
    "ds_read_b64_tr_b16 a[226:227], v230 offset:14336\n\t"
    "v_mfma_f32_16x16x32_bf16 v[24:27], a[252:255], a[216:219], v[24:27]\n\t"
    "s_cmp_lg_u64 s[76:77], 0\n\t"
    "s_cselect_b32 s75, s73, 0\n\t"
    "v_add_u32 v231, 0x10000, v231\n\t"
    "v_mfma_f32_16x16x32_bf16 v[28:31], a[252:255], a[220:223], v[28:31]\n\t"
    "s_or_b32 s74, s74, s75\n\t"
    "ds_read_b64_tr_b16 a[252:253], v231 offset:0\n\t"
    "v_mfma_f32_16x16x32_bf16 v[28:31], a[248:251], a[204:207], v[28:31]\n\t"
    "buffer_load_dwordx4 v234, s[48:51], s71 offen offset:1024 lds\n\t"
    "v_mfma_f32_16x16x32_bf16 v[28:31], a[244:247], a[188:191], v[28:31]\n\t"
    "s_mov_b64 s[76:77], 0\n\t"
    "ds_read_b64_tr_b16 a[254:255], v231 offset:2048\n\t"
    "v_mfma_f32_16x16x32_bf16 v[28:31], a[240:243], a[172:175], v[28:31]\n\t"
    "s_waitcnt lgkmcnt(4)\n\t"
    "s_lshl_b32 s73, s73, 1\n\t"
    "v_mfma_f32_16x16x32_bf16 v[32:35], a[236:239], a[160:163], v[32:35]\n\t"
    "ds_read_b64_tr_b16 a[248:249], v231 offset:4096\n\t"
    "v_mfma_f32_16x16x32_bf16 v[32:35], a[232:235], a[176:179], v[32:35]\n\t"
    "v_mfma_f32_16x16x32_bf16 v[32:35], a[228:231], a[192:195], v[32:35]\n\t"
    "s_waitcnt lgkmcnt(3)\n\t"
    "ds_read_b64_tr_b16 a[250:251], v231 offset:6144\n\t"
    "v_mfma_f32_16x16x32_bf16 v[32:35], a[224:227], a[208:211], v[32:35]\n\t"
    "v_mfma_f32_16x16x32_bf16 v[36:39], a[224:227], a[212:215], v[36:39]\n\t"
    "ds_read_b64_tr_b16 a[244:245], v231 offset:8192\n\t"
    "v_mfma_f32_16x16x32_bf16 v[36:39], a[228:231], a[196:199], v[36:39]\n\t"
    "v_mfma_f32_16x16x32_bf16 v[36:39], a[232:235], a[180:183], v[36:39]\n\t"
    "ds_read_b64_tr_b16 a[246:247], v231 offset:10240\n\t"
    "v_mfma_f32_16x16x32_bf16 v[36:39], a[236:239], a[164:167], v[36:39]\n\t"
    "buffer_load_dwordx4 v234, s[48:51], s71 offen offset:2048 lds\n\t"
    "v_mfma_f32_16x16x32_bf16 v[40:43], a[236:239], a[168:171], v[40:43]\n\t"
    "ds_read_b64_tr_b16 a[240:241], v231 offset:12288\n\t"
    "v_mfma_f32_16x16x32_bf16 v[40:43], a[232:235], a[184:187], v[40:43]\n\t"
    "v_mfma_f32_16x16x32_bf16 v[40:43], a[228:231], a[200:203], v[40:43]\n\t"
    "ds_read_b64_tr_b16 a[242:243], v231 offset:14336\n\t"
    "v_mfma_f32_16x16x32_bf16 v[40:43], a[224:227], a[216:219], v[40:43]\n\t"
    "v_mfma_f32_16x16x32_bf16 v[44:47], a[224:227], a[220:223], v[44:47]\n\t"
    "v_mfma_f32_16x16x32_bf16 v[44:47], a[228:231], a[204:207], v[44:47]\n\t"
    "v_mfma_f32_16x16x32_bf16 v[44:47], a[232:235], a[188:191], v[44:47]\n\t"
    "v_mfma_f32_16x16x32_bf16 v[44:47], a[236:239], a[172:175], v[44:47]\n\t"
    "s_waitcnt lgkmcnt(2)\n\t"
    "v_mfma_f32_16x16x32_bf16 v[48:51], a[252:255], a[160:163], v[48:51]\n\t"
    "buffer_load_dwordx4 v234, s[48:51], s71 offen offset:3072 lds\n\t"
    "v_mfma_f32_16x16x32_bf16 v[48:51], a[248:251], a[176:179], v[48:51]\n\t"
    "s_waitcnt lgkmcnt(0)\n\t"
    "v_mfma_f32_16x16x32_bf16 v[48:51], a[244:247], a[192:195], v[48:51]\n\t"
    "s_waitcnt vmcnt(16)\n\t"
    "v_mfma_f32_16x16x32_bf16 v[48:51], a[240:243], a[208:211], v[48:51]\n\t"
    "s_barrier\n\t"
    "v_mfma_f32_16x16x32_bf16 v[52:55], a[240:243], a[212:215], v[52:55]\n\t"
    "ds_read_b64_tr_b16 a[224:225], v228 offset:16384\n\t"
    "ds_read_b64_tr_b16 a[226:227], v228 offset:18432\n\t"
    "v_mfma_f32_16x16x32_bf16 v[52:55], a[244:247], a[196:199], v[52:55]\n\t"
    "ds_read_b64_tr_b16 a[228:229], v228 offset:20480\n\t"
    "ds_read_b64_tr_b16 a[230:231], v228 offset:22528\n\t"
    "v_mfma_f32_16x16x32_bf16 v[52:55], a[248:251], a[180:183], v[52:55]\n\t"
    "ds_read_b64_tr_b16 a[232:233], v228 offset:24576\n\t"
    "v_mfma_f32_16x16x32_bf16 v[52:55], a[252:255], a[164:167], v[52:55]\n\t"
    "ds_read_b64_tr_b16 a[234:235], v228 offset:26624\n\t"
    "v_mfma_f32_16x16x32_bf16 v[56:59], a[252:255], a[168:171], v[56:59]\n\t"
    "ds_read_b64_tr_b16 a[236:237], v228 offset:28672\n\t"
    "v_mfma_f32_16x16x32_bf16 v[56:59], a[248:251], a[184:187], v[56:59]\n\t"
    "s_xor_b32 s71, s67, 0x180\n\t"
    "s_add_u32 m0, s70, 0xc000\n\t"
    "v_mfma_f32_16x16x32_bf16 v[56:59], a[244:247], a[200:203], v[56:59]\n\t"
    "ds_read_b64_tr_b16 a[238:239], v228 offset:30720\n\t"
    "v_mfma_f32_16x16x32_bf16 v[56:59], a[240:243], a[216:219], v[56:59]\n\t"
    "buffer_load_dwordx4 v232, s[52:55], s71 offen offset:0 nt lds\n\t"
    "v_mfma_f32_16x16x32_bf16 v[60:63], a[240:243], a[220:223], v[60:63]\n\t"
    "ds_read_b64_tr_b16 a[240:241], v229 offset:16384\n\t"
    "v_mfma_f32_16x16x32_bf16 v[60:63], a[244:247], a[204:207], v[60:63]\n\t"
    "v_mfma_f32_16x16x32_bf16 v[60:63], a[248:251], a[188:191], v[60:63]\n\t"
    "ds_read_b64_tr_b16 a[242:243], v229 offset:18432\n\t"
    "v_mfma_f32_16x16x32_bf16 v[60:63], a[252:255], a[172:175], v[60:63]\n\t"
    "s_waitcnt lgkmcnt(4)\n\t"
    "v_add_u32 v226, 0xfffe0000, v226\n\t"
    "v_mfma_f32_16x16x32_bf16 v[0:3], a[224:227], v[96:99], v[0:3]\n\t"
    "s_waitcnt vmcnt(25)\n\t"
    "ds_read_b64_tr_b16 a[244:245], v229 offset:20480\n\t"
    "v_mfma_f32_16x16x32_bf16 v[0:3], a[228:231], v[112:115], v[0:3]\n\t"
    "ds_read_b128 a[64:67], v226 offset:32768\n\t"
    "v_add_u32 v227, 0xfffe0000, v227\n\t"
    "v_mfma_f32_16x16x32_bf16 v[0:3], a[232:235], v[128:131], v[0:3]\n\t"
    "s_waitcnt lgkmcnt(4)\n\t"
    "ds_read_b64_tr_b16 a[246:247], v229 offset:22528\n\t"
    "v_mfma_f32_16x16x32_bf16 v[0:3], a[236:239], v[144:147], v[0:3]\n\t"
    "ds_read_b128 a[68:71], v227 offset:32768\n\t"
    "v_mfma_f32_16x16x32_bf16 v[4:7], a[236:239], v[148:151], v[4:7]\n\t"
    "ds_read_b64_tr_b16 a[248:249], v229 offset:24576\n\t"
    "ds_read_b128 a[72:75], v226 offset:34816\n\t"
    "v_mfma_f32_16x16x32_bf16 v[4:7], a[232:235], v[132:135], v[4:7]\n\t"
    "buffer_load_dwordx4 v233, s[52:55], s71 offen offset:1024 nt lds\n\t"
    "v_mfma_f32_16x16x32_bf16 v[4:7], a[228:231], v[116:119], v[4:7]\n\t"
    "ds_read_b64_tr_b16 a[250:251], v229 offset:26624\n\t"
    "ds_read_b128 a[76:79], v227 offset:34816\n\t"
    "v_mfma_f32_16x16x32_bf16 v[4:7], a[224:227], v[100:103], v[4:7]\n\t"
    "v_mfma_f32_16x16x32_bf16 v[8:11], a[224:227], v[104:107], v[8:11]\n\t"
    "ds_read_b64_tr_b16 a[252:253], v229 offset:28672\n\t"
    "v_mfma_f32_16x16x32_bf16 v[8:11], a[228:231], v[120:123], v[8:11]\n\t"
    "v_mfma_f32_16x16x32_bf16 v[8:11], a[232:235], v[136:139], v[8:11]\n\t"
    "ds_read_b64_tr_b16 a[254:255], v229 offset:30720\n\t"
    "v_mfma_f32_16x16x32_bf16 v[8:11], a[236:239], v[152:155], v[8:11]\n\t"
    "v_mfma_f32_16x16x32_bf16 v[12:15], a[236:239], v[156:159], v[12:15]\n\t"
    "ds_read_b64_tr_b16 a[236:237], v230 offset:16384\n\t"
    "v_mfma_f32_16x16x32_bf16 v[12:15], a[232:235], v[140:143], v[12:15]\n\t"
    "v_mfma_f32_16x16x32_bf16 v[12:15], a[228:231], v[124:127], v[12:15]\n\t"
    "s_waitcnt lgkmcnt(4)\n\t"
    "ds_read_b64_tr_b16 a[238:239], v230 offset:18432\n\t"
    "v_mfma_f32_16x16x32_bf16 v[12:15], a[224:227], v[108:111], v[12:15]\n\t"
    "buffer_load_dwordx4 v232, s[52:55], s71 offen offset:2048 nt lds\n\t"
    "v_mfma_f32_16x16x32_bf16 v[16:19], a[240:243], v[96:99], v[16:19]\n\t"
    "ds_read_b64_tr_b16 a[232:233], v230 offset:20480\n\t"
    "v_mfma_f32_16x16x32_bf16 v[16:19], a[244:247], v[112:115], v[16:19]\n\t"
    "v_mfma_f32_16x16x32_bf16 v[16:19], a[248:251], v[128:131], v[16:19]\n\t"
    "s_waitcnt lgkmcnt(3)\n\t"
    "ds_read_b64_tr_b16 a[234:235], v230 offset:22528\n\t"
    "v_mfma_f32_16x16x32_bf16 v[16:19], a[252:255], v[144:147], v[16:19]\n\t"
    "v_mfma_f32_16x16x32_bf16 v[20:23], a[252:255], v[148:151], v[20:23]\n\t"
    "ds_read_b64_tr_b16 a[228:229], v230 offset:24576\n\t"
    "v_mfma_f32_16x16x32_bf16 v[20:23], a[248:251], v[132:135], v[20:23]\n\t"
    "v_mfma_f32_16x16x32_bf16 v[20:23], a[244:247], v[116:119], v[20:23]\n\t"
    "ds_read_b64_tr_b16 a[230:231], v230 offset:26624\n\t"
    "v_mfma_f32_16x16x32_bf16 v[20:23], a[240:243], v[100:103], v[20:23]\n\t"
    "v_mfma_f32_16x16x32_bf16 v[24:27], a[240:243], v[104:107], v[24:27]\n\t"
    "ds_read_b64_tr_b16 a[224:225], v230 offset:28672\n\t"
    "v_mfma_f32_16x16x32_bf16 v[24:27], a[244:247], v[120:123], v[24:27]\n\t"
    "buffer_load_dwordx4 v233, s[52:55], s71 offen offset:3072 nt lds\n\t"
    "v_mfma_f32_16x16x32_bf16 v[24:27], a[248:251], v[136:139], v[24:27]\n\t"
    "ds_read_b64_tr_b16 a[226:227], v230 offset:30720\n\t"
    "v_mfma_f32_16x16x32_bf16 v[24:27], a[252:255], v[152:155], v[24:27]\n\t"
    "v_mfma_f32_16x16x32_bf16 v[28:31], a[252:255], v[156:159], v[28:31]\n\t"
    "ds_read_b64_tr_b16 a[252:253], v231 offset:16384\n\t"
    "v_mfma_f32_16x16x32_bf16 v[28:31], a[248:251], v[140:143], v[28:31]\n\t"
    "v_mfma_f32_16x16x32_bf16 v[28:31], a[244:247], v[124:127], v[28:31]\n\t"
    "ds_read_b64_tr_b16 a[254:255], v231 offset:18432\n\t"
    "v_mfma_f32_16x16x32_bf16 v[28:31], a[240:243], v[108:111], v[28:31]\n\t"
    "s_waitcnt lgkmcnt(4)\n\t"
    "v_mfma_f32_16x16x32_bf16 v[32:35], a[236:239], v[96:99], v[32:35]\n\t"
    "ds_read_b64_tr_b16 a[248:249], v231 offset:20480\n\t"
    "v_mfma_f32_16x16x32_bf16 v[32:35], a[232:235], v[112:115], v[32:35]\n\t"
    "s_xor_b32 s71, s66, 0x20080\n\t"
    "s_add_u32 m0, s70, 0x8000\n\t"
    "v_mfma_f32_16x16x32_bf16 v[32:35], a[228:231], v[128:131], v[32:35]\n\t"
    "s_waitcnt lgkmcnt(3)\n\t"
    "ds_read_b64_tr_b16 a[250:251], v231 offset:22528\n\t"
    "v_mfma_f32_16x16x32_bf16 v[32:35], a[224:227], v[144:147], v[32:35]\n\t"
    "buffer_load_dwordx4 v234, s[48:51], s71 offen offset:0 lds\n\t"
    "v_mfma_f32_16x16x32_bf16 v[36:39], a[224:227], v[148:151], v[36:39]\n\t"
    "ds_read_b64_tr_b16 a[244:245], v231 offset:24576\n\t"
    "v_mfma_f32_16x16x32_bf16 v[36:39], a[228:231], v[132:135], v[36:39]\n\t"
    "v_mfma_f32_16x16x32_bf16 v[36:39], a[232:235], v[116:119], v[36:39]\n\t"
    "ds_read_b64_tr_b16 a[246:247], v231 offset:26624\n\t"
    "v_mfma_f32_16x16x32_bf16 v[36:39], a[236:239], v[100:103], v[36:39]\n\t"
    "v_mfma_f32_16x16x32_bf16 v[40:43], a[236:239], v[104:107], v[40:43]\n\t"
    "ds_read_b64_tr_b16 a[240:241], v231 offset:28672\n\t"
    "v_mfma_f32_16x16x32_bf16 v[40:43], a[232:235], v[120:123], v[40:43]\n\t"
    "s_mov_b32 s64, s65\n\t"
    "s_add_u32 s68, s68, 0x2000000\n\t"
    "s_mov_b32 s58, 0x4000000\n\t"
    "v_mfma_f32_16x16x32_bf16 v[40:43], a[228:231], v[136:139], v[40:43]\n\t"
    "ds_read_b64_tr_b16 a[242:243], v231 offset:30720\n\t"
    "v_mfma_f32_16x16x32_bf16 v[40:43], a[224:227], v[152:155], v[40:43]\n\t"
    "v_mfma_f32_16x16x32_bf16 v[44:47], a[224:227], v[156:159], v[44:47]\n\t"
    "v_mfma_f32_16x16x32_bf16 v[44:47], a[228:231], v[140:143], v[44:47]\n\t"
    "buffer_load_dwordx4 v234, s[48:51], s71 offen offset:1024 lds\n\t"
    "v_mfma_f32_16x16x32_bf16 v[44:47], a[232:235], v[124:127], v[44:47]\n\t"
    "v_mfma_f32_16x16x32_bf16 v[44:47], a[236:239], v[108:111], v[44:47]\n\t"
    "s_waitcnt lgkmcnt(2)\n\t"
    "v_mfma_f32_16x16x32_bf16 v[48:51], a[252:255], v[96:99], v[48:51]\n\t"
    "v_mfma_f32_16x16x32_bf16 v[48:51], a[248:251], v[112:115], v[48:51]\n\t"
    "s_waitcnt lgkmcnt(0)\n\t"
    "v_mfma_f32_16x16x32_bf16 v[48:51], a[244:247], v[128:131], v[48:51]\n\t"
    "s_waitcnt vmcnt(18)\n\t"
    "v_mfma_f32_16x16x32_bf16 v[48:51], a[240:243], v[144:147], v[48:51]\n\t"
    "s_barrier\n\t"
    "v_mfma_f32_16x16x32_bf16 v[52:55], a[240:243], v[148:151], v[52:55]\n\t"
    "ds_read_b64_tr_b16 a[224:225], v228 offset:32768\n\t"
    "ds_read_b64_tr_b16 a[226:227], v228 offset:34816\n\t"
    "v_mfma_f32_16x16x32_bf16 v[52:55], a[244:247], v[132:135], v[52:55]\n\t"
    "ds_read_b64_tr_b16 a[228:229], v228 offset:36864\n\t"
    "ds_read_b64_tr_b16 a[230:231], v228 offset:38912\n\t"
    "v_mfma_f32_16x16x32_bf16 v[52:55], a[248:251], v[116:119], v[52:55]\n\t"
    "ds_read_b64_tr_b16 a[232:233], v228 offset:40960\n\t"
    "v_mfma_f32_16x16x32_bf16 v[52:55], a[252:255], v[100:103], v[52:55]\n\t"
    "ds_read_b64_tr_b16 a[234:235], v228 offset:43008\n\t"
    "v_mfma_f32_16x16x32_bf16 v[56:59], a[252:255], v[104:107], v[56:59]\n\t"
    "ds_read_b64_tr_b16 a[236:237], v228 offset:45056\n\t"
    "v_mfma_f32_16x16x32_bf16 v[56:59], a[248:251], v[120:123], v[56:59]\n\t"
    "buffer_load_dwordx4 v234, s[48:51], s71 offen offset:2048 lds\n\t"
    "v_mfma_f32_16x16x32_bf16 v[56:59], a[244:247], v[136:139], v[56:59]\n\t"
    "ds_read_b64_tr_b16 a[238:239], v228 offset:47104\n\t"
    "v_mfma_f32_16x16x32_bf16 v[56:59], a[240:243], v[152:155], v[56:59]\n\t"
    "v_mfma_f32_16x16x32_bf16 v[60:63], a[240:243], v[156:159], v[60:63]\n\t"
    "ds_read_b64_tr_b16 a[240:241], v229 offset:32768\n\t"
    "v_mfma_f32_16x16x32_bf16 v[60:63], a[244:247], v[140:143], v[60:63]\n\t"
    "v_mfma_f32_16x16x32_bf16 v[60:63], a[248:251], v[124:127], v[60:63]\n\t"
    "ds_read_b64_tr_b16 a[242:243], v229 offset:34816\n\t"
    "v_mfma_f32_16x16x32_bf16 v[60:63], a[252:255], v[108:111], v[60:63]\n\t"
    "s_waitcnt lgkmcnt(4)\n\t"
    "v_mfma_f32_16x16x32_bf16 v[0:3], a[224:227], v[160:163], v[0:3]\n\t"
    "ds_read_b64_tr_b16 a[244:245], v229 offset:36864\n\t"
    "v_mfma_f32_16x16x32_bf16 v[0:3], a[228:231], v[176:179], v[0:3]\n\t"
    "buffer_load_dwordx4 v234, s[48:51], s71 offen offset:3072 lds\n\t"
    "v_mfma_f32_16x16x32_bf16 v[0:3], a[232:235], v[192:195], v[0:3]\n\t"
    "s_waitcnt lgkmcnt(3)\n\t"
    "ds_read_b64_tr_b16 a[246:247], v229 offset:38912\n\t"
    "v_mfma_f32_16x16x32_bf16 v[0:3], a[236:239], v[208:211], v[0:3]\n\t"
    "v_mfma_f32_16x16x32_bf16 v[4:7], a[236:239], v[212:215], v[4:7]\n\t"
    "ds_read_b64_tr_b16 a[248:249], v229 offset:40960\n\t"
    "v_mfma_f32_16x16x32_bf16 v[4:7], a[232:235], v[196:199], v[4:7]\n\t"
    "v_mfma_f32_16x16x32_bf16 v[4:7], a[228:231], v[180:183], v[4:7]\n\t"
    "ds_read_b64_tr_b16 a[250:251], v229 offset:43008\n\t"
    "v_mfma_f32_16x16x32_bf16 v[4:7], a[224:227], v[164:167], v[4:7]\n\t"
    "s_xor_b32 s71, s66, 0x30080\n\t"
    "v_mfma_f32_16x16x32_bf16 v[8:11], a[224:227], v[168:171], v[8:11]\n\t"
    "ds_read_b64_tr_b16 a[252:253], v229 offset:45056\n\t"
    "v_mul_f32 v0, v252, v0\n\t"
    "v_mfma_f32_16x16x32_bf16 v[8:11], a[228:231], v[184:187], v[8:11]\n\t"
    "s_add_u32 m0, s70, 0x10000\n\t"
    "v_mul_f32 v1, v252, v1\n\t"
    "v_mul_f32 v2, v252, v2\n\t"
    "v_mfma_f32_16x16x32_bf16 v[8:11], a[232:235], v[200:203], v[8:11]\n\t"
    "ds_read_b64_tr_b16 a[254:255], v229 offset:47104\n\t"
    "v_mul_f32 v3, v252, v3\n\t"
    "v_mfma_f32_16x16x32_bf16 v[8:11], a[236:239], v[216:219], v[8:11]\n\t"
    "buffer_load_dwordx4 v234, s[48:51], s71 offen offset:0 lds\n\t"
    "v_mfma_f32_16x16x32_bf16 v[12:15], a[236:239], v[220:223], v[12:15]\n\t"
    "ds_read_b64_tr_b16 a[236:237], v230 offset:32768\n\t"
    "v_cvt_pk_bf16_f32 v0, v0, v1\n\t"
    "v_mfma_f32_16x16x32_bf16 v[12:15], a[232:235], v[204:207], v[12:15]\n\t"
    "v_cvt_pk_bf16_f32 v1, v2, v3\n\t"
    "v_mul_f32 v4, v253, v4\n\t"
    "v_mfma_f32_16x16x32_bf16 v[12:15], a[228:231], v[188:191], v[12:15]\n\t"
    "ds_read_b64_tr_b16 a[238:239], v230 offset:34816\n\t"
    "v_mul_f32 v5, v253, v5\n\t"
    "v_mfma_f32_16x16x32_bf16 v[12:15], a[224:227], v[172:175], v[12:15]\n\t"
    "s_waitcnt lgkmcnt(4)\n\t"
    "ds_write_b64 v235, v[0:1] offset:0\n\t"
    "v_mul_f32 v6, v253, v6\n\t"
    "v_mul_f32 v7, v253, v7\n\t"
    "v_mfma_f32_16x16x32_bf16 v[16:19], a[240:243], v[160:163], v[16:19]\n\t"
    "ds_read_b64_tr_b16 a[232:233], v230 offset:36864\n\t"
    "v_cvt_pk_bf16_f32 v4, v4, v5\n\t"
    "v_mfma_f32_16x16x32_bf16 v[16:19], a[244:247], v[176:179], v[16:19]\n\t"
    "s_waitcnt lgkmcnt(4)\n\t"
    "v_cvt_pk_bf16_f32 v5, v6, v7\n\t"
    "v_mul_f32 v8, v254, v8\n\t"
    "v_mfma_f32_16x16x32_bf16 v[16:19], a[248:251], v[192:195], v[16:19]\n\t"
    "ds_read_b64_tr_b16 a[234:235], v230 offset:38912\n\t"
    "v_mul_f32 v9, v254, v9\n\t"
    "v_mfma_f32_16x16x32_bf16 v[16:19], a[252:255], v[208:211], v[16:19]\n\t"
    "ds_write_b64 v235, v[4:5] offset:512\n\t"
    "v_mul_f32 v10, v254, v10\n\t"
    "v_mfma_f32_16x16x32_bf16 v[20:23], a[252:255], v[212:215], v[20:23]\n\t"
    "ds_read_b64_tr_b16 a[228:229], v230 offset:40960\n\t"
    "v_mul_f32 v11, v254, v11\n\t"
    "v_mfma_f32_16x16x32_bf16 v[20:23], a[248:251], v[196:199], v[20:23]\n\t"
    "buffer_load_dwordx4 v234, s[48:51], s71 offen offset:1024 lds\n\t"
    "v_mfma_f32_16x16x32_bf16 v[20:23], a[244:247], v[180:183], v[20:23]\n\t"
    "ds_read_b64_tr_b16 a[230:231], v230 offset:43008\n\t"
    "v_cvt_pk_bf16_f32 v8, v8, v9\n\t"
    "v_mfma_f32_16x16x32_bf16 v[20:23], a[240:243], v[164:167], v[20:23]\n\t"
    "v_cvt_pk_bf16_f32 v9, v10, v11\n\t"
    "v_mul_f32 v12, v255, v12\n\t"
    "v_mfma_f32_16x16x32_bf16 v[24:27], a[240:243], v[168:171], v[24:27]\n\t"
    "ds_read_b64_tr_b16 a[224:225], v230 offset:45056\n\t"
    "v_mul_f32 v13, v255, v13\n\t"
    "v_mfma_f32_16x16x32_bf16 v[24:27], a[244:247], v[184:187], v[24:27]\n\t"
    "ds_write_b64 v235, v[8:9] offset:1024\n\t"
    "v_mfma_f32_16x16x32_bf16 v[24:27], a[248:251], v[200:203], v[24:27]\n\t"
    "ds_read_b64_tr_b16 a[226:227], v230 offset:47104\n\t"
    "v_mul_f32 v14, v255, v14\n\t"
    "v_mfma_f32_16x16x32_bf16 v[24:27], a[252:255], v[216:219], v[24:27]\n\t"
    "v_mul_f32 v15, v255, v15\n\t"
    "v_cvt_pk_bf16_f32 v12, v12, v13\n\t"
    "v_mfma_f32_16x16x32_bf16 v[28:31], a[252:255], v[220:223], v[28:31]\n\t"
    "ds_read_b64_tr_b16 a[252:253], v231 offset:32768\n\t"
    "v_cvt_pk_bf16_f32 v13, v14, v15\n\t"
    "v_mfma_f32_16x16x32_bf16 v[28:31], a[248:251], v[204:207], v[28:31]\n\t"
    "ds_write_b64 v235, v[12:13] offset:1536\n\t"
    "v_mfma_f32_16x16x32_bf16 v[28:31], a[244:247], v[188:191], v[28:31]\n\t"
    "ds_read_b64_tr_b16 a[254:255], v231 offset:34816\n\t"
    "v_mul_f32 v16, v252, v16\n\t"
    "v_mfma_f32_16x16x32_bf16 v[28:31], a[240:243], v[172:175], v[28:31]\n\t"
    "s_waitcnt lgkmcnt(6)\n\t"
    "buffer_load_dwordx4 v234, s[48:51], s71 offen offset:2048 lds\n\t"
    "v_mfma_f32_16x16x32_bf16 v[32:35], a[236:239], v[160:163], v[32:35]\n\t"
    "ds_read_b64_tr_b16 a[248:249], v231 offset:36864\n\t"
    "v_mul_f32 v17, v252, v17\n\t"
    "v_mfma_f32_16x16x32_bf16 v[32:35], a[232:235], v[176:179], v[32:35]\n\t"
    "s_waitcnt lgkmcnt(4)\n\t"
    "v_mul_f32 v18, v252, v18\n\t"
    "v_mul_f32 v19, v252, v19\n\t"
    "v_mfma_f32_16x16x32_bf16 v[32:35], a[228:231], v[192:195], v[32:35]\n\t"
    "ds_read_b64_tr_b16 a[250:251], v231 offset:38912\n\t"
    "v_cvt_pk_bf16_f32 v16, v16, v17\n\t"
    "v_mfma_f32_16x16x32_bf16 v[32:35], a[224:227], v[208:211], v[32:35]\n\t"
    "v_cvt_pk_bf16_f32 v17, v18, v19\n\t"
    "v_mul_f32 v20, v253, v20\n\t"
    "v_mfma_f32_16x16x32_bf16 v[36:39], a[224:227], v[212:215], v[36:39]\n\t"
    "ds_read_b64_tr_b16 a[244:245], v231 offset:40960\n\t"
    "v_mul_f32 v21, v253, v21\n\t"
    "v_mfma_f32_16x16x32_bf16 v[36:39], a[228:231], v[196:199], v[36:39]\n\t"
    "ds_write_b64 v235, v[16:17] offset:128\n\t"
    "v_mfma_f32_16x16x32_bf16 v[36:39], a[232:235], v[180:183], v[36:39]\n\t"
    "ds_read_b64_tr_b16 a[246:247], v231 offset:43008\n\t"
    "v_mul_f32 v22, v253, v22\n\t"
    "v_mfma_f32_16x16x32_bf16 v[36:39], a[236:239], v[164:167], v[36:39]\n\t"
    "v_mul_f32 v23, v253, v23\n\t"
    "v_cvt_pk_bf16_f32 v20, v20, v21\n\t"
    "v_mfma_f32_16x16x32_bf16 v[40:43], a[236:239], v[168:171], v[40:43]\n\t"
    "ds_read_b64_tr_b16 a[240:241], v231 offset:45056\n\t"
    "v_cvt_pk_bf16_f32 v21, v22, v23\n\t"
    "v_mfma_f32_16x16x32_bf16 v[40:43], a[232:235], v[184:187], v[40:43]\n\t"
    "buffer_load_dwordx4 v234, s[48:51], s71 offen offset:3072 lds\n\t"
    "v_mfma_f32_16x16x32_bf16 v[40:43], a[228:231], v[200:203], v[40:43]\n\t"
    "ds_read_b64_tr_b16 a[242:243], v231 offset:47104\n\t"
    "v_mul_f32 v24, v254, v24\n\t"
    "v_mfma_f32_16x16x32_bf16 v[40:43], a[224:227], v[216:219], v[40:43]\n\t"
    "v_mul_f32 v25, v254, v25\n\t"
    "v_mfma_f32_16x16x32_bf16 v[44:47], a[224:227], v[220:223], v[44:47]\n\t"
    "s_xor_b32 s71, s67, 0x4180\n\t"
    "v_mfma_f32_16x16x32_bf16 v[44:47], a[228:231], v[204:207], v[44:47]\n\t"
    "ds_write_b64 v235, v[20:21] offset:640\n\t"
    "v_mfma_f32_16x16x32_bf16 v[44:47], a[232:235], v[188:191], v[44:47]\n\t"
    "s_add_u32 m0, s70, 0x14000\n\t"
    "v_mul_f32 v26, v254, v26\n\t"
    "v_mul_f32 v27, v254, v27\n\t"
    "v_mfma_f32_16x16x32_bf16 v[44:47], a[236:239], v[172:175], v[44:47]\n\t"
    "s_waitcnt lgkmcnt(3)\n\t"
    "v_cvt_pk_bf16_f32 v24, v24, v25\n\t"
    "v_cvt_pk_bf16_f32 v25, v26, v27\n\t"
    "v_mfma_f32_16x16x32_bf16 v[48:51], a[252:255], v[160:163], v[48:51]\n\t"
    "buffer_load_dwordx4 v232, s[52:55], s71 offen offset:0 nt lds\n\t"
    "v_mfma_f32_16x16x32_bf16 v[48:51], a[248:251], v[176:179], v[48:51]\n\t"
    "s_waitcnt lgkmcnt(1)\n\t"
    "v_mul_f32 v28, v255, v28\n\t"
    "v_mul_f32 v29, v255, v29\n\t"
    "v_mfma_f32_16x16x32_bf16 v[48:51], a[244:247], v[192:195], v[48:51]\n\t"
    "s_waitcnt vmcnt(21)\n\t"
    "v_mfma_f32_16x16x32_bf16 v[48:51], a[240:243], v[208:211], v[48:51]\n\t"
    "s_barrier\n\t"
    "v_mfma_f32_16x16x32_bf16 v[52:55], a[240:243], v[212:215], v[52:55]\n\t"
    "ds_read_b64_tr_b16 a[224:225], v228 offset:49152\n\t"
    "ds_read_b64_tr_b16 a[226:227], v228 offset:51200\n\t"
    "v_mfma_f32_16x16x32_bf16 v[52:55], a[244:247], v[196:199], v[52:55]\n\t"
    "ds_read_b64_tr_b16 a[228:229], v228 offset:53248\n\t"
    "ds_read_b64_tr_b16 a[230:231], v228 offset:55296\n\t"
    "v_mfma_f32_16x16x32_bf16 v[52:55], a[248:251], v[180:183], v[52:55]\n\t"
    "ds_read_b64_tr_b16 a[232:233], v228 offset:57344\n\t"
    "v_mfma_f32_16x16x32_bf16 v[52:55], a[252:255], v[164:167], v[52:55]\n\t"
    "ds_read_b64_tr_b16 a[234:235], v228 offset:59392\n\t"
    "v_mfma_f32_16x16x32_bf16 v[56:59], a[252:255], v[168:171], v[56:59]\n\t"
    "ds_read_b64_tr_b16 a[236:237], v228 offset:61440\n\t"
    "v_mfma_f32_16x16x32_bf16 v[56:59], a[248:251], v[184:187], v[56:59]\n\t"
    "ds_write_b64 v235, v[24:25] offset:1152\n\t"
    "v_mfma_f32_16x16x32_bf16 v[56:59], a[244:247], v[200:203], v[56:59]\n\t"
    "ds_read_b64_tr_b16 a[238:239], v228 offset:63488\n\t"
    "v_mul_f32 v30, v255, v30\n\t"
    "v_mfma_f32_16x16x32_bf16 v[56:59], a[240:243], v[216:219], v[56:59]\n\t"
    "v_mul_f32 v31, v255, v31\n\t"
    "v_cvt_pk_bf16_f32 v28, v28, v29\n\t"
    "v_mfma_f32_16x16x32_bf16 v[60:63], a[240:243], v[220:223], v[60:63]\n\t"
    "ds_read_b64_tr_b16 a[240:241], v229 offset:49152\n\t"
    "v_cvt_pk_bf16_f32 v29, v30, v31\n\t"
    "v_mfma_f32_16x16x32_bf16 v[60:63], a[244:247], v[204:207], v[60:63]\n\t"
    "ds_write_b64 v235, v[28:29] offset:1664\n\t"
    "v_mfma_f32_16x16x32_bf16 v[60:63], a[248:251], v[188:191], v[60:63]\n\t"
    "ds_read_b64_tr_b16 a[242:243], v229 offset:51200\n\t"
    "v_mul_f32 v32, v252, v32\n\t"
    "v_mfma_f32_16x16x32_bf16 v[60:63], a[252:255], v[172:175], v[60:63]\n\t"
    "s_waitcnt lgkmcnt(6)\n\t"
    "buffer_load_dwordx4 v233, s[52:55], s71 offen offset:1024 nt lds\n\t"
    "v_mfma_f32_16x16x32_bf16 v[0:3], a[224:227], v[64:67], 0\n\t"
    "ds_read_b64_tr_b16 a[244:245], v229 offset:53248\n\t"
    "v_mul_f32 v33, v252, v33\n\t"
    "v_mfma_f32_16x16x32_bf16 v[0:3], a[228:231], v[80:83], v[0:3]\n\t"
    "s_waitcnt lgkmcnt(4)\n\t"
    "v_mul_f32 v34, v252, v34\n\t"
    "v_mul_f32 v35, v252, v35\n\t"
    "v_mfma_f32_16x16x32_bf16 v[0:3], a[232:235], a[128:131], v[0:3]\n\t"
    "ds_read_b64_tr_b16 a[246:247], v229 offset:55296\n\t"
    "v_cvt_pk_bf16_f32 v32, v32, v33\n\t"
    "v_mfma_f32_16x16x32_bf16 v[0:3], a[236:239], a[144:147], v[0:3]\n\t"
    "s_waitcnt vmcnt(18)\n\t"
    "v_cvt_pk_bf16_f32 v33, v34, v35\n\t"
    "v_mul_f32 v36, v253, v36\n\t"
    "v_mfma_f32_16x16x32_bf16 v[4:7], a[236:239], a[148:151], 0\n\t"
    "ds_read_b64_tr_b16 a[248:249], v229 offset:57344\n\t"
    "v_mul_f32 v37, v253, v37\n\t"
    "v_mfma_f32_16x16x32_bf16 v[4:7], a[232:235], a[132:135], v[4:7]\n\t"
    "ds_write_b64 v235, v[32:33] offset:16384\n\t"
    "v_mfma_f32_16x16x32_bf16 v[4:7], a[228:231], v[84:87], v[4:7]\n\t"
    "ds_read_b64_tr_b16 a[250:251], v229 offset:59392\n\t"
    "v_mul_f32 v38, v253, v38\n\t"
    "v_mfma_f32_16x16x32_bf16 v[4:7], a[224:227], v[68:71], v[4:7]\n\t"
    "v_mul_f32 v39, v253, v39\n\t"
    "v_cvt_pk_bf16_f32 v36, v36, v37\n\t"
    "v_mfma_f32_16x16x32_bf16 v[8:11], a[224:227], v[72:75], 0\n\t"
    "ds_read_b64_tr_b16 a[252:253], v229 offset:61440\n\t"
    "v_cvt_pk_bf16_f32 v37, v38, v39\n\t"
    "v_mfma_f32_16x16x32_bf16 v[8:11], a[228:231], v[88:91], v[8:11]\n\t"
    "buffer_load_dwordx4 v232, s[52:55], s71 offen offset:2048 nt lds\n\t"
    "v_mfma_f32_16x16x32_bf16 v[8:11], a[232:235], a[136:139], v[8:11]\n\t"
    "ds_read_b64_tr_b16 a[254:255], v229 offset:63488\n\t"
    "v_mul_f32 v40, v254, v40\n\t"
    "v_mfma_f32_16x16x32_bf16 v[8:11], a[236:239], a[152:155], v[8:11]\n\t"
    "ds_read_b128 a[80:83], v226 offset:16384\n\t"
    "v_mul_f32 v41, v254, v41\n\t"
    "v_mfma_f32_16x16x32_bf16 v[12:15], a[236:239], a[156:159], 0\n\t"
    "ds_read_b64_tr_b16 a[236:237], v230 offset:49152\n\t"
    "ds_read_b128 a[84:87], v227 offset:16384\n\t"
    "v_mfma_f32_16x16x32_bf16 v[12:15], a[232:235], a[140:143], v[12:15]\n\t"
    "ds_write_b64 v235, v[36:37] offset:16896\n\t"
    "v_mfma_f32_16x16x32_bf16 v[12:15], a[228:231], v[92:95], v[12:15]\n\t"
    "ds_read_b64_tr_b16 a[238:239], v230 offset:51200\n\t"
    "v_mul_f32 v42, v254, v42\n\t"
    "v_mfma_f32_16x16x32_bf16 v[12:15], a[224:227], v[76:79], v[12:15]\n\t"
    "s_waitcnt lgkmcnt(7)\n\t"
    "v_mul_f32 v43, v254, v43\n\t"
    "v_cvt_pk_bf16_f32 v40, v40, v41\n\t"
    "v_mfma_f32_16x16x32_bf16 v[16:19], a[240:243], v[64:67], 0\n\t"
    "ds_read_b64_tr_b16 a[232:233], v230 offset:53248\n\t"
    "v_cvt_pk_bf16_f32 v41, v42, v43\n\t"
    "v_mfma_f32_16x16x32_bf16 v[16:19], a[244:247], v[80:83], v[16:19]\n\t"
    "v_mul_f32 v44, v255, v44\n\t"
    "v_mul_f32 v45, v255, v45\n\t"
    "v_mfma_f32_16x16x32_bf16 v[16:19], a[248:251], a[128:131], v[16:19]\n\t"
    "s_waitcnt lgkmcnt(6)\n\t"
    "ds_read_b64_tr_b16 a[234:235], v230 offset:55296\n\t"
    "v_mfma_f32_16x16x32_bf16 v[16:19], a[252:255], a[144:147], v[16:19]\n\t"
    "buffer_load_dwordx4 v233, s[52:55], s71 offen offset:3072 nt lds\n\t"
    "v_mfma_f32_16x16x32_bf16 v[20:23], a[252:255], a[148:151], 0\n\t"
    "ds_read_b64_tr_b16 a[228:229], v230 offset:57344\n\t"
    "ds_read_b128 a[88:91], v226 offset:18432\n\t"
    "v_mfma_f32_16x16x32_bf16 v[20:23], a[248:251], a[132:135], v[20:23]\n\t"
    "ds_read_b128 a[92:95], v227 offset:18432\n\t"
    "v_mfma_f32_16x16x32_bf16 v[20:23], a[244:247], v[84:87], v[20:23]\n\t"
    "s_xor_b32 s71, s66, 0x100\n\t"
    "ds_read_b64_tr_b16 a[230:231], v230 offset:59392\n\t"
    "v_mfma_f32_16x16x32_bf16 v[20:23], a[240:243], v[68:71], v[20:23]\n\t"
    "ds_write_b64 v235, v[40:41] offset:17408\n\t"
    "v_mfma_f32_16x16x32_bf16 v[24:27], a[240:243], v[72:75], 0\n\t"
    "ds_read_b64_tr_b16 a[224:225], v230 offset:61440\n\t"
    "v_mul_f32 v46, v255, v46\n\t"
    "v_mfma_f32_16x16x32_bf16 v[24:27], a[244:247], v[88:91], v[24:27]\n\t"
    "s_add_u32 m0, s70, 0x18000\n\t"
    "v_mul_f32 v47, v255, v47\n\t"
    "v_cvt_pk_bf16_f32 v44, v44, v45\n\t"
    "v_mfma_f32_16x16x32_bf16 v[24:27], a[248:251], a[136:139], v[24:27]\n\t"
    "ds_read_b64_tr_b16 a[226:227], v230 offset:63488\n\t"
    "v_cvt_pk_bf16_f32 v45, v46, v47\n\t"
    "v_mfma_f32_16x16x32_bf16 v[24:27], a[252:255], a[152:155], v[24:27]\n\t"
    "ds_write_b64 v235, v[44:45] offset:17920\n\t"
    "v_mfma_f32_16x16x32_bf16 v[28:31], a[252:255], a[156:159], 0\n\t"
    "ds_read_b64_tr_b16 a[252:253], v231 offset:49152\n\t"
    "v_mul_f32 v48, v252, v48\n\t"
    "v_mfma_f32_16x16x32_bf16 v[28:31], a[248:251], a[140:143], v[28:31]\n\t"
    "buffer_load_dwordx4 v234, s[48:51], s71 offen offset:0 lds\n\t"
    "v_mfma_f32_16x16x32_bf16 v[28:31], a[244:247], v[92:95], v[28:31]\n\t"
    "ds_read_b64_tr_b16 a[254:255], v231 offset:51200\n\t"
    "v_mul_f32 v49, v252, v49\n\t"
    "v_mfma_f32_16x16x32_bf16 v[28:31], a[240:243], v[76:79], v[28:31]\n\t"
    "s_waitcnt lgkmcnt(6)\n\t"
    "v_mul_f32 v50, v252, v50\n\t"
    "v_mul_f32 v51, v252, v51\n\t"
    "v_mfma_f32_16x16x32_bf16 v[32:35], a[236:239], v[64:67], 0\n\t"
    "ds_read_b64_tr_b16 a[248:249], v231 offset:53248\n\t"
    "v_cvt_pk_bf16_f32 v48, v48, v49\n\t"
    "v_mfma_f32_16x16x32_bf16 v[32:35], a[232:235], v[80:83], v[32:35]\n\t"
    "s_waitcnt lgkmcnt(4)\n\t"
    "v_cvt_pk_bf16_f32 v49, v50, v51\n\t"
    "v_mul_f32 v52, v253, v52\n\t"
    "v_mfma_f32_16x16x32_bf16 v[32:35], a[228:231], a[128:131], v[32:35]\n\t"
    "ds_read_b64_tr_b16 a[250:251], v231 offset:55296\n\t"
    "v_mul_f32 v53, v253, v53\n\t"
    "v_mfma_f32_16x16x32_bf16 v[32:35], a[224:227], a[144:147], v[32:35]\n\t"
    "ds_write_b64 v235, v[48:49] offset:16512\n\t"
    "v_mfma_f32_16x16x32_bf16 v[36:39], a[224:227], a[148:151], 0\n\t"
    "ds_read_b64_tr_b16 a[244:245], v231 offset:57344\n\t"
    "v_mul_f32 v54, v253, v54\n\t"
    "v_mfma_f32_16x16x32_bf16 v[36:39], a[228:231], a[132:135], v[36:39]\n\t"
    "v_mul_f32 v55, v253, v55\n\t"
    "v_cvt_pk_bf16_f32 v52, v52, v53\n\t"
    "v_mfma_f32_16x16x32_bf16 v[36:39], a[232:235], v[84:87], v[36:39]\n\t"
    "ds_read_b64_tr_b16 a[246:247], v231 offset:59392\n\t"
    "v_cvt_pk_bf16_f32 v53, v54, v55\n\t"
    "v_mfma_f32_16x16x32_bf16 v[36:39], a[236:239], v[68:71], v[36:39]\n\t"
    "buffer_load_dwordx4 v234, s[48:51], s71 offen offset:1024 lds\n\t"
    "v_mfma_f32_16x16x32_bf16 v[40:43], a[236:239], v[72:75], 0\n\t"
    "ds_read_b64_tr_b16 a[240:241], v231 offset:61440\n\t"
    "v_mul_f32 v56, v254, v56\n\t"
    "v_mfma_f32_16x16x32_bf16 v[40:43], a[232:235], v[88:91], v[40:43]\n\t"
    "ds_read_b128 v[244:247], v236 offset:0\n\t"
    "v_mul_f32 v57, v254, v57\n\t"
    "v_mfma_f32_16x16x32_bf16 v[40:43], a[228:231], a[136:139], v[40:43]\n\t"
    "ds_read_b64_tr_b16 a[242:243], v231 offset:63488\n\t"
    "v_add_u32 v228, 0xffff0000, v228\n\t"
    "v_mfma_f32_16x16x32_bf16 v[40:43], a[224:227], a[152:155], v[40:43]\n\t"
    "ds_write_b64 v235, v[52:53] offset:17024\n\t"
    "v_mfma_f32_16x16x32_bf16 v[44:47], a[224:227], a[156:159], 0\n\t"
    "v_mul_f32 v58, v254, v58\n\t"
    "v_mul_f32 v59, v254, v59\n\t"
    "v_mfma_f32_16x16x32_bf16 v[44:47], a[228:231], a[140:143], v[44:47]\n\t"
    "v_cvt_pk_bf16_f32 v56, v56, v57\n\t"
    "v_cvt_pk_bf16_f32 v57, v58, v59\n\t"
    "v_mfma_f32_16x16x32_bf16 v[44:47], a[232:235], v[92:95], v[44:47]\n\t"
    "s_xor_b32 s83, s68, 0x0\n\t"
    "v_mul_f32 v60, v255, v60\n\t"
    "v_mul_f32 v61, v255, v61\n\t"
    "v_mfma_f32_16x16x32_bf16 v[44:47], a[236:239], v[76:79], v[44:47]\n\t"
    "s_waitcnt lgkmcnt(4)\n\t"
    "ds_write_b64 v235, v[56:57] offset:17536\n\t"
    "v_mfma_f32_16x16x32_bf16 v[48:51], a[252:255], v[64:67], 0\n\t"
    "buffer_load_dwordx4 v234, s[48:51], s71 offen offset:2048 lds\n\t"
    "v_mfma_f32_16x16x32_bf16 v[48:51], a[248:251], v[80:83], v[48:51]\n\t"
    "s_waitcnt lgkmcnt(2)\n\t"
    "v_mul_f32 v62, v255, v62\n\t"
    "v_mul_f32 v63, v255, v63\n\t"
    "v_mfma_f32_16x16x32_bf16 v[48:51], a[244:247], a[128:131], v[48:51]\n\t"
    "s_waitcnt vmcnt(19)\n\t"
    "v_cvt_pk_bf16_f32 v60, v60, v61\n\t"
    "v_cvt_pk_bf16_f32 v61, v62, v63\n\t"
    "v_mfma_f32_16x16x32_bf16 v[48:51], a[240:243], a[144:147], v[48:51]\n\t"
    "s_barrier\n\t"
    "v_mfma_f32_16x16x32_bf16 v[52:55], a[240:243], a[148:151], 0\n\t"
    "ds_read_b64_tr_b16 a[224:225], v228 offset:0\n\t"
    "ds_read_b64_tr_b16 a[226:227], v228 offset:2048\n\t"
    "v_mfma_f32_16x16x32_bf16 v[52:55], a[244:247], a[132:135], v[52:55]\n\t"
    "ds_read_b64_tr_b16 a[228:229], v228 offset:4096\n\t"
    "ds_read_b64_tr_b16 a[230:231], v228 offset:6144\n\t"
    "v_mfma_f32_16x16x32_bf16 v[52:55], a[248:251], v[84:87], v[52:55]\n\t"
    "ds_read_b64_tr_b16 a[232:233], v228 offset:8192\n\t"
    "buffer_store_dwordx4 v[244:247], v237, s[56:59], s83 offen offset:0 sc1\n\t"
    "v_mfma_f32_16x16x32_bf16 v[52:55], a[252:255], v[68:71], v[52:55]\n\t"
    "ds_read_b64_tr_b16 a[234:235], v228 offset:10240\n\t"
    "v_mfma_f32_16x16x32_bf16 v[56:59], a[252:255], v[72:75], 0\n\t"
    "ds_read_b64_tr_b16 a[236:237], v228 offset:12288\n\t"
    "v_mfma_f32_16x16x32_bf16 v[56:59], a[248:251], v[88:91], v[56:59]\n\t"
    "ds_write_b64 v235, v[60:61] offset:18048\n\t"
    "v_mfma_f32_16x16x32_bf16 v[56:59], a[244:247], a[136:139], v[56:59]\n\t"
    "ds_read_b64_tr_b16 a[238:239], v228 offset:14336\n\t"
    "v_add_u32 v229, 0xffff0000, v229\n\t"
    "v_mfma_f32_16x16x32_bf16 v[56:59], a[240:243], a[152:155], v[56:59]\n\t"
    "buffer_load_dwordx4 v234, s[48:51], s71 offen offset:3072 lds\n\t"
    "v_mfma_f32_16x16x32_bf16 v[60:63], a[240:243], a[156:159], 0\n\t"
    "ds_read_b64_tr_b16 a[240:241], v229 offset:0\n\t"
    "v_mfma_f32_16x16x32_bf16 v[60:63], a[244:247], a[140:143], v[60:63]\n\t"
    "v_mfma_f32_16x16x32_bf16 v[60:63], a[248:251], v[92:95], v[60:63]\n\t"
    "ds_read_b64_tr_b16 a[242:243], v229 offset:2048\n\t"
    "v_mfma_f32_16x16x32_bf16 v[60:63], a[252:255], v[76:79], v[60:63]\n\t"
    "s_waitcnt lgkmcnt(5)\n\t"
    "v_mfma_f32_16x16x32_bf16 v[0:3], a[224:227], a[160:163], v[0:3]\n\t"
    "ds_read_b64_tr_b16 a[244:245], v229 offset:4096\n\t"
    "v_mfma_f32_16x16x32_bf16 v[0:3], a[228:231], a[176:179], v[0:3]\n\t"
    "v_mfma_f32_16x16x32_bf16 v[0:3], a[232:235], a[192:195], v[0:3]\n\t"
    "s_waitcnt lgkmcnt(3)\n\t"
    "ds_read_b64_tr_b16 a[246:247], v229 offset:6144\n\t"
    "v_mfma_f32_16x16x32_bf16 v[0:3], a[236:239], a[208:211], v[0:3]\n\t"
    "v_mfma_f32_16x16x32_bf16 v[4:7], a[236:239], a[212:215], v[4:7]\n\t"
    "ds_read_b64_tr_b16 a[248:249], v229 offset:8192\n\t"
    "ds_read_b128 v[248:251], v236 offset:256\n\t"
    "v_mfma_f32_16x16x32_bf16 v[4:7], a[232:235], a[196:199], v[4:7]\n\t"
    "s_xor_b32 s71, s66, 0x10100\n\t"
    "v_mfma_f32_16x16x32_bf16 v[4:7], a[228:231], a[180:183], v[4:7]\n\t"
    "s_add_u32 m0, s70, 0x1c000\n\t"
    "ds_read_b64_tr_b16 a[250:251], v229 offset:10240\n\t"
    "v_mfma_f32_16x16x32_bf16 v[4:7], a[224:227], a[164:167], v[4:7]\n\t"
    "buffer_load_dwordx4 v234, s[48:51], s71 offen offset:0 lds\n\t"
    "v_mfma_f32_16x16x32_bf16 v[8:11], a[224:227], a[168:171], v[8:11]\n\t"
    "ds_read_b64_tr_b16 a[252:253], v229 offset:12288\n\t"
    "v_mfma_f32_16x16x32_bf16 v[8:11], a[228:231], a[184:187], v[8:11]\n\t"
    "v_add_u32 v230, 0xffff0000, v230\n\t"
    "v_mfma_f32_16x16x32_bf16 v[8:11], a[232:235], a[200:203], v[8:11]\n\t"
    "s_waitcnt lgkmcnt(2)\n\t"
    "ds_read_b64_tr_b16 a[254:255], v229 offset:14336\n\t"
    "v_mfma_f32_16x16x32_bf16 v[8:11], a[236:239], a[216:219], v[8:11]\n\t"
    "buffer_store_dwordx4 v[248:251], v237, s[56:59], s83 offen offset:1024 sc1\n\t"
    "v_mfma_f32_16x16x32_bf16 v[12:15], a[236:239], a[220:223], v[12:15]\n\t"
    "ds_read_b64_tr_b16 a[236:237], v230 offset:0\n\t"
    "v_mfma_f32_16x16x32_bf16 v[12:15], a[232:235], a[204:207], v[12:15]\n\t"
    "v_mfma_f32_16x16x32_bf16 v[12:15], a[228:231], a[188:191], v[12:15]\n\t"
    "ds_read_b64_tr_b16 a[238:239], v230 offset:2048\n\t"
    "v_mfma_f32_16x16x32_bf16 v[12:15], a[224:227], a[172:175], v[12:15]\n\t"
    "s_waitcnt lgkmcnt(4)\n\t"
    "v_mfma_f32_16x16x32_bf16 v[16:19], a[240:243], a[160:163], v[16:19]\n\t"
    "ds_read_b64_tr_b16 a[232:233], v230 offset:4096\n\t"
    "v_mfma_f32_16x16x32_bf16 v[16:19], a[244:247], a[176:179], v[16:19]\n\t"
    "v_mfma_f32_16x16x32_bf16 v[16:19], a[248:251], a[192:195], v[16:19]\n\t"
    "s_waitcnt lgkmcnt(3)\n\t"
    "ds_read_b64_tr_b16 a[234:235], v230 offset:6144\n\t"
    "v_mfma_f32_16x16x32_bf16 v[16:19], a[252:255], a[208:211], v[16:19]\n\t"
    "s_waitcnt vmcnt(19)\n\t"
    "v_mfma_f32_16x16x32_bf16 v[20:23], a[252:255], a[212:215], v[20:23]\n\t"
    "ds_read_b64_tr_b16 a[228:229], v230 offset:8192\n\t"
    "ds_read_b128 a[96:99], v226 offset:49152\n\t"
    "v_mfma_f32_16x16x32_bf16 v[20:23], a[248:251], a[196:199], v[20:23]\n\t"
    "ds_read_b128 a[100:103], v227 offset:49152\n\t"
    "v_mfma_f32_16x16x32_bf16 v[20:23], a[244:247], a[180:183], v[20:23]\n\t"
    "ds_read_b64_tr_b16 a[230:231], v230 offset:10240\n\t"
    "ds_read_b128 a[104:107], v226 offset:51200\n\t"
    "v_mfma_f32_16x16x32_bf16 v[20:23], a[240:243], a[164:167], v[20:23]\n\t"
    "buffer_load_dwordx4 v234, s[48:51], s71 offen offset:1024 lds\n\t"
    "v_mfma_f32_16x16x32_bf16 v[24:27], a[240:243], a[168:171], v[24:27]\n\t"
    "ds_read_b64_tr_b16 a[224:225], v230 offset:12288\n\t"
    "ds_read_b128 a[108:111], v227 offset:51200\n\t"
    "v_mfma_f32_16x16x32_bf16 v[24:27], a[244:247], a[184:187], v[24:27]\n\t"
    "v_mfma_f32_16x16x32_bf16 v[24:27], a[248:251], a[200:203], v[24:27]\n\t"
    "ds_read_b64_tr_b16 a[226:227], v230 offset:14336\n\t"
    "v_mfma_f32_16x16x32_bf16 v[24:27], a[252:255], a[216:219], v[24:27]\n\t"
    "v_add_u32 v231, 0xffff0000, v231\n\t"
    "v_mfma_f32_16x16x32_bf16 v[28:31], a[252:255], a[220:223], v[28:31]\n\t"
    "ds_read_b64_tr_b16 a[252:253], v231 offset:0\n\t"
    "v_mfma_f32_16x16x32_bf16 v[28:31], a[248:251], a[204:207], v[28:31]\n\t"
    "v_mfma_f32_16x16x32_bf16 v[28:31], a[244:247], a[188:191], v[28:31]\n\t"
    "ds_read_b64_tr_b16 a[254:255], v231 offset:2048\n\t"
    "v_mfma_f32_16x16x32_bf16 v[28:31], a[240:243], a[172:175], v[28:31]\n\t"
    "s_waitcnt lgkmcnt(6)\n\t"
    "v_mfma_f32_16x16x32_bf16 v[32:35], a[236:239], a[160:163], v[32:35]\n\t"
    "ds_read_b64_tr_b16 a[248:249], v231 offset:4096\n\t"
    "v_mfma_f32_16x16x32_bf16 v[32:35], a[232:235], a[176:179], v[32:35]\n\t"
    "v_mfma_f32_16x16x32_bf16 v[32:35], a[228:231], a[192:195], v[32:35]\n\t"
    "s_waitcnt lgkmcnt(3)\n\t"
    "ds_read_b64_tr_b16 a[250:251], v231 offset:6144\n\t"
    "v_mfma_f32_16x16x32_bf16 v[32:35], a[224:227], a[208:211], v[32:35]\n\t"
    "v_mfma_f32_16x16x32_bf16 v[36:39], a[224:227], a[212:215], v[36:39]\n\t"
    "ds_read_b64_tr_b16 a[244:245], v231 offset:8192\n\t"
    "ds_read_b128 v[244:247], v236 offset:512\n\t"
    "v_mfma_f32_16x16x32_bf16 v[36:39], a[228:231], a[196:199], v[36:39]\n\t"
    "v_mfma_f32_16x16x32_bf16 v[36:39], a[232:235], a[180:183], v[36:39]\n\t"
    "ds_read_b64_tr_b16 a[246:247], v231 offset:10240\n\t"
    "v_mfma_f32_16x16x32_bf16 v[36:39], a[236:239], a[164:167], v[36:39]\n\t"
    "buffer_load_dwordx4 v234, s[48:51], s71 offen offset:2048 lds\n\t"
    "v_mfma_f32_16x16x32_bf16 v[40:43], a[236:239], a[168:171], v[40:43]\n\t"
    "ds_read_b64_tr_b16 a[240:241], v231 offset:12288\n\t"
    "v_mfma_f32_16x16x32_bf16 v[40:43], a[232:235], a[184:187], v[40:43]\n\t"
    "v_mfma_f32_16x16x32_bf16 v[40:43], a[228:231], a[200:203], v[40:43]\n\t"
    "s_waitcnt lgkmcnt(2)\n\t"
    "ds_read_b64_tr_b16 a[242:243], v231 offset:14336\n\t"
    "v_mfma_f32_16x16x32_bf16 v[40:43], a[224:227], a[216:219], v[40:43]\n\t"
    "buffer_store_dwordx4 v[244:247], v237, s[56:59], s83 offen offset:2048 sc1\n\t"
    "v_mfma_f32_16x16x32_bf16 v[44:47], a[224:227], a[220:223], v[44:47]\n\t"
    "v_mfma_f32_16x16x32_bf16 v[44:47], a[228:231], a[204:207], v[44:47]\n\t"
    "v_mfma_f32_16x16x32_bf16 v[44:47], a[232:235], a[188:191], v[44:47]\n\t"
    "v_mfma_f32_16x16x32_bf16 v[44:47], a[236:239], a[172:175], v[44:47]\n\t"
    "s_waitcnt lgkmcnt(2)\n\t"
    "v_mfma_f32_16x16x32_bf16 v[48:51], a[252:255], a[160:163], v[48:51]\n\t"
    "v_mfma_f32_16x16x32_bf16 v[48:51], a[248:251], a[176:179], v[48:51]\n\t"
    "s_waitcnt lgkmcnt(0)\n\t"
    "v_mfma_f32_16x16x32_bf16 v[48:51], a[244:247], a[192:195], v[48:51]\n\t"
    "s_waitcnt vmcnt(18)\n\t"
    "v_mfma_f32_16x16x32_bf16 v[48:51], a[240:243], a[208:211], v[48:51]\n\t"
    "s_barrier\n\t"
    "v_mfma_f32_16x16x32_bf16 v[52:55], a[240:243], a[212:215], v[52:55]\n\t"
    "ds_read_b64_tr_b16 a[224:225], v228 offset:32768\n\t"
    "ds_read_b64_tr_b16 a[226:227], v228 offset:34816\n\t"
    "v_mfma_f32_16x16x32_bf16 v[52:55], a[244:247], a[196:199], v[52:55]\n\t"
    "ds_read_b64_tr_b16 a[228:229], v228 offset:36864\n\t"
    "ds_read_b64_tr_b16 a[230:231], v228 offset:38912\n\t"
    "v_mfma_f32_16x16x32_bf16 v[52:55], a[248:251], a[180:183], v[52:55]\n\t"
    "ds_read_b64_tr_b16 a[232:233], v228 offset:40960\n\t"
    "v_mfma_f32_16x16x32_bf16 v[52:55], a[252:255], a[164:167], v[52:55]\n\t"
    "ds_read_b64_tr_b16 a[234:235], v228 offset:43008\n\t"
    "v_mfma_f32_16x16x32_bf16 v[56:59], a[252:255], a[168:171], v[56:59]\n\t"
    "ds_read_b64_tr_b16 a[236:237], v228 offset:45056\n\t"
    "v_mfma_f32_16x16x32_bf16 v[56:59], a[248:251], a[184:187], v[56:59]\n\t"
    "buffer_load_dwordx4 v234, s[48:51], s71 offen offset:3072 lds\n\t"
    "v_mfma_f32_16x16x32_bf16 v[56:59], a[244:247], a[200:203], v[56:59]\n\t"
    "ds_read_b64_tr_b16 a[238:239], v228 offset:47104\n\t"
    "v_mfma_f32_16x16x32_bf16 v[56:59], a[240:243], a[216:219], v[56:59]\n\t"
    "v_mfma_f32_16x16x32_bf16 v[60:63], a[240:243], a[220:223], v[60:63]\n\t"
    "ds_read_b64_tr_b16 a[240:241], v229 offset:32768\n\t"
    "v_mfma_f32_16x16x32_bf16 v[60:63], a[244:247], a[204:207], v[60:63]\n\t"
    "v_mfma_f32_16x16x32_bf16 v[60:63], a[248:251], a[188:191], v[60:63]\n\t"
    "ds_read_b64_tr_b16 a[242:243], v229 offset:34816\n\t"
    "v_mfma_f32_16x16x32_bf16 v[60:63], a[252:255], a[172:175], v[60:63]\n\t"
    "s_waitcnt lgkmcnt(4)\n\t"
    "v_mfma_f32_16x16x32_bf16 v[0:3], a[224:227], v[96:99], v[0:3]\n\t"
    "ds_read_b64_tr_b16 a[244:245], v229 offset:36864\n\t"
    "v_mfma_f32_16x16x32_bf16 v[0:3], a[228:231], v[112:115], v[0:3]\n\t"
    "v_mfma_f32_16x16x32_bf16 v[0:3], a[232:235], v[128:131], v[0:3]\n\t"
    "s_waitcnt lgkmcnt(3)\n\t"
    "ds_read_b64_tr_b16 a[246:247], v229 offset:38912\n\t"
    "v_mfma_f32_16x16x32_bf16 v[0:3], a[236:239], v[144:147], v[0:3]\n\t"
    "v_mfma_f32_16x16x32_bf16 v[4:7], a[236:239], v[148:151], v[4:7]\n\t"
    "ds_read_b64_tr_b16 a[248:249], v229 offset:40960\n\t"
    "ds_read_b128 v[248:251], v236 offset:768\n\t"
    "v_mfma_f32_16x16x32_bf16 v[4:7], a[232:235], v[132:135], v[4:7]\n\t"
    "s_xor_b32 s71, s66, 0x20100\n\t"
    "v_mfma_f32_16x16x32_bf16 v[4:7], a[228:231], v[116:119], v[4:7]\n\t"
    "s_add_u32 m0, s70, 0x0\n\t"
    "ds_read_b64_tr_b16 a[250:251], v229 offset:43008\n\t"
    "v_mfma_f32_16x16x32_bf16 v[4:7], a[224:227], v[100:103], v[4:7]\n\t"
    "buffer_load_dwordx4 v234, s[48:51], s71 offen offset:0 lds\n\t"
    "v_mfma_f32_16x16x32_bf16 v[8:11], a[224:227], v[104:107], v[8:11]\n\t"
    "ds_read_b64_tr_b16 a[252:253], v229 offset:45056\n\t"
    "v_mfma_f32_16x16x32_bf16 v[8:11], a[228:231], v[120:123], v[8:11]\n\t"
    "v_mfma_f32_16x16x32_bf16 v[8:11], a[232:235], v[136:139], v[8:11]\n\t"
    "s_waitcnt lgkmcnt(2)\n\t"
    "ds_read_b64_tr_b16 a[254:255], v229 offset:47104\n\t"
    "v_mfma_f32_16x16x32_bf16 v[8:11], a[236:239], v[152:155], v[8:11]\n\t"
    "buffer_store_dwordx4 v[248:251], v237, s[56:59], s83 offen offset:3072 sc1\n\t"
    "v_mfma_f32_16x16x32_bf16 v[12:15], a[236:239], v[156:159], v[12:15]\n\t"
    "ds_read_b64_tr_b16 a[236:237], v230 offset:32768\n\t"
    "v_mfma_f32_16x16x32_bf16 v[12:15], a[232:235], v[140:143], v[12:15]\n\t"
    "v_mfma_f32_16x16x32_bf16 v[12:15], a[228:231], v[124:127], v[12:15]\n\t"
    "ds_read_b64_tr_b16 a[238:239], v230 offset:34816\n\t"
    "v_mfma_f32_16x16x32_bf16 v[12:15], a[224:227], v[108:111], v[12:15]\n\t"
    "s_waitcnt lgkmcnt(4)\n\t"
    "v_mfma_f32_16x16x32_bf16 v[16:19], a[240:243], v[96:99], v[16:19]\n\t"
    "ds_read_b64_tr_b16 a[232:233], v230 offset:36864\n\t"
    "v_mfma_f32_16x16x32_bf16 v[16:19], a[244:247], v[112:115], v[16:19]\n\t"
    "v_mfma_f32_16x16x32_bf16 v[16:19], a[248:251], v[128:131], v[16:19]\n\t"
    "s_waitcnt lgkmcnt(3)\n\t"
    "ds_read_b64_tr_b16 a[234:235], v230 offset:38912\n\t"
    "v_mfma_f32_16x16x32_bf16 v[16:19], a[252:255], v[144:147], v[16:19]\n\t"
    "v_mfma_f32_16x16x32_bf16 v[20:23], a[252:255], v[148:151], v[20:23]\n\t"
    "ds_read_b64_tr_b16 a[228:229], v230 offset:40960\n\t"
    "v_mfma_f32_16x16x32_bf16 v[20:23], a[248:251], v[132:135], v[20:23]\n\t"
    "v_mfma_f32_16x16x32_bf16 v[20:23], a[244:247], v[116:119], v[20:23]\n\t"
    "ds_read_b64_tr_b16 a[230:231], v230 offset:43008\n\t"
    "v_mfma_f32_16x16x32_bf16 v[20:23], a[240:243], v[100:103], v[20:23]\n\t"
    "buffer_load_dwordx4 v234, s[48:51], s71 offen offset:1024 lds\n\t"
    "v_mfma_f32_16x16x32_bf16 v[24:27], a[240:243], v[104:107], v[24:27]\n\t"
    "ds_read_b64_tr_b16 a[224:225], v230 offset:45056\n\t"
    "v_mfma_f32_16x16x32_bf16 v[24:27], a[244:247], v[120:123], v[24:27]\n\t"
    "v_mfma_f32_16x16x32_bf16 v[24:27], a[248:251], v[136:139], v[24:27]\n\t"
    "ds_read_b64_tr_b16 a[226:227], v230 offset:47104\n\t"
    "v_mfma_f32_16x16x32_bf16 v[24:27], a[252:255], v[152:155], v[24:27]\n\t"
    "v_mfma_f32_16x16x32_bf16 v[28:31], a[252:255], v[156:159], v[28:31]\n\t"
    "ds_read_b64_tr_b16 a[252:253], v231 offset:32768\n\t"
    "v_mfma_f32_16x16x32_bf16 v[28:31], a[248:251], v[140:143], v[28:31]\n\t"
    "v_mfma_f32_16x16x32_bf16 v[28:31], a[244:247], v[124:127], v[28:31]\n\t"
    "ds_read_b64_tr_b16 a[254:255], v231 offset:34816\n\t"
    "v_mfma_f32_16x16x32_bf16 v[28:31], a[240:243], v[108:111], v[28:31]\n\t"
    "s_waitcnt lgkmcnt(4)\n\t"
    "v_mfma_f32_16x16x32_bf16 v[32:35], a[236:239], v[96:99], v[32:35]\n\t"
    "ds_read_b64_tr_b16 a[248:249], v231 offset:36864\n\t"
    "v_mfma_f32_16x16x32_bf16 v[32:35], a[232:235], v[112:115], v[32:35]\n\t"
    "v_mfma_f32_16x16x32_bf16 v[32:35], a[228:231], v[128:131], v[32:35]\n\t"
    "s_waitcnt lgkmcnt(3)\n\t"
    "ds_read_b64_tr_b16 a[250:251], v231 offset:38912\n\t"
    "v_mfma_f32_16x16x32_bf16 v[32:35], a[224:227], v[144:147], v[32:35]\n\t"
    "v_mfma_f32_16x16x32_bf16 v[36:39], a[224:227], v[148:151], v[36:39]\n\t"
    "ds_read_b64_tr_b16 a[244:245], v231 offset:40960\n\t"
    "ds_read_b128 v[244:247], v236 offset:1024\n\t"
    "v_mfma_f32_16x16x32_bf16 v[36:39], a[228:231], v[132:135], v[36:39]\n\t"
    "v_mfma_f32_16x16x32_bf16 v[36:39], a[232:235], v[116:119], v[36:39]\n\t"
    "ds_read_b64_tr_b16 a[246:247], v231 offset:43008\n\t"
    "v_mfma_f32_16x16x32_bf16 v[36:39], a[236:239], v[100:103], v[36:39]\n\t"
    "buffer_load_dwordx4 v234, s[48:51], s71 offen offset:2048 lds\n\t"
    "v_mfma_f32_16x16x32_bf16 v[40:43], a[236:239], v[104:107], v[40:43]\n\t"
    "ds_read_b64_tr_b16 a[240:241], v231 offset:45056\n\t"
    "v_mfma_f32_16x16x32_bf16 v[40:43], a[232:235], v[120:123], v[40:43]\n\t"
    "s_xor_b32 s83, s68, 0x4000\n\t"
    "v_mfma_f32_16x16x32_bf16 v[40:43], a[228:231], v[136:139], v[40:43]\n\t"
    "s_waitcnt lgkmcnt(2)\n\t"
    "ds_read_b64_tr_b16 a[242:243], v231 offset:47104\n\t"
    "v_mfma_f32_16x16x32_bf16 v[40:43], a[224:227], v[152:155], v[40:43]\n\t"
    "buffer_store_dwordx4 v[244:247], v237, s[56:59], s83 offen offset:0 sc1\n\t"
    "v_mfma_f32_16x16x32_bf16 v[44:47], a[224:227], v[156:159], v[44:47]\n\t"
    "v_mfma_f32_16x16x32_bf16 v[44:47], a[228:231], v[140:143], v[44:47]\n\t"
    "v_mfma_f32_16x16x32_bf16 v[44:47], a[232:235], v[124:127], v[44:47]\n\t"
    "v_mfma_f32_16x16x32_bf16 v[44:47], a[236:239], v[108:111], v[44:47]\n\t"
    "s_waitcnt lgkmcnt(2)\n\t"
    "v_mfma_f32_16x16x32_bf16 v[48:51], a[252:255], v[96:99], v[48:51]\n\t"
    "v_mfma_f32_16x16x32_bf16 v[48:51], a[248:251], v[112:115], v[48:51]\n\t"
    "s_waitcnt lgkmcnt(0)\n\t"
    "v_mfma_f32_16x16x32_bf16 v[48:51], a[244:247], v[128:131], v[48:51]\n\t"
    "s_waitcnt vmcnt(20)\n\t"
    "v_add_u32 v228, 0x10000, v228\n\t"
    "v_mfma_f32_16x16x32_bf16 v[48:51], a[240:243], v[144:147], v[48:51]\n\t"
    "s_barrier\n\t"
    "v_mfma_f32_16x16x32_bf16 v[52:55], a[240:243], v[148:151], v[52:55]\n\t"
    "ds_read_b64_tr_b16 a[224:225], v228 offset:0\n\t"
    "ds_read_b64_tr_b16 a[226:227], v228 offset:2048\n\t"
    "v_mfma_f32_16x16x32_bf16 v[52:55], a[244:247], v[132:135], v[52:55]\n\t"
    "ds_read_b64_tr_b16 a[228:229], v228 offset:4096\n\t"
    "ds_read_b64_tr_b16 a[230:231], v228 offset:6144\n\t"
    "v_mfma_f32_16x16x32_bf16 v[52:55], a[248:251], v[116:119], v[52:55]\n\t"
    "ds_read_b64_tr_b16 a[232:233], v228 offset:8192\n\t"
    "v_mfma_f32_16x16x32_bf16 v[52:55], a[252:255], v[100:103], v[52:55]\n\t"
    "ds_read_b64_tr_b16 a[234:235], v228 offset:10240\n\t"
    "v_mfma_f32_16x16x32_bf16 v[56:59], a[252:255], v[104:107], v[56:59]\n\t"
    "ds_read_b64_tr_b16 a[236:237], v228 offset:12288\n\t"
    "v_mfma_f32_16x16x32_bf16 v[56:59], a[248:251], v[120:123], v[56:59]\n\t"
    "buffer_load_dwordx4 v234, s[48:51], s71 offen offset:3072 lds\n\t"
    "v_mfma_f32_16x16x32_bf16 v[56:59], a[244:247], v[136:139], v[56:59]\n\t"
    "ds_read_b64_tr_b16 a[238:239], v228 offset:14336\n\t"
    "v_mfma_f32_16x16x32_bf16 v[56:59], a[240:243], v[152:155], v[56:59]\n\t"
    "v_add_u32 v229, 0x10000, v229\n\t"
    "v_mfma_f32_16x16x32_bf16 v[60:63], a[240:243], v[156:159], v[60:63]\n\t"
    "ds_read_b64_tr_b16 a[240:241], v229 offset:0\n\t"
    "v_mfma_f32_16x16x32_bf16 v[60:63], a[244:247], v[140:143], v[60:63]\n\t"
    "v_mfma_f32_16x16x32_bf16 v[60:63], a[248:251], v[124:127], v[60:63]\n\t"
    "ds_read_b64_tr_b16 a[242:243], v229 offset:2048\n\t"
    "v_add_u32 v227, 0x10000, v227\n\t"
    "v_mfma_f32_16x16x32_bf16 v[60:63], a[252:255], v[108:111], v[60:63]\n\t"
    "s_waitcnt lgkmcnt(4)\n\t"
    "v_add_u32 v230, 0x10000, v230\n\t"
    "v_add_u32 v226, 0x10000, v226\n\t"
    "v_mfma_f32_16x16x32_bf16 v[0:3], a[224:227], v[160:163], v[0:3]\n\t"
    "s_waitcnt vmcnt(17)\n\t"
    "ds_read_b64_tr_b16 a[244:245], v229 offset:4096\n\t"
    "v_mfma_f32_16x16x32_bf16 v[0:3], a[228:231], v[176:179], v[0:3]\n\t"
    "s_xor_b32 s71, s66, 0x30100\n\t"
    "ds_read_b128 a[112:115], v226 offset:16384\n\t"
    "v_mfma_f32_16x16x32_bf16 v[0:3], a[232:235], v[192:195], v[0:3]\n\t"
    "s_waitcnt lgkmcnt(4)\n\t"
    "ds_read_b64_tr_b16 a[246:247], v229 offset:6144\n\t"
    "v_mfma_f32_16x16x32_bf16 v[0:3], a[236:239], v[208:211], v[0:3]\n\t"
    "s_add_u32 m0, s70, 0x4000\n\t"
    "ds_read_b128 a[116:119], v227 offset:16384\n\t"
    "v_mfma_f32_16x16x32_bf16 v[4:7], a[236:239], v[212:215], v[4:7]\n\t"
    "ds_read_b64_tr_b16 a[248:249], v229 offset:8192\n\t"
    "ds_read_b128 v[248:251], v236 offset:1280\n\t"
    "v_mfma_f32_16x16x32_bf16 v[4:7], a[232:235], v[196:199], v[4:7]\n\t"
    "ds_read_b128 a[120:123], v226 offset:18432\n\t"
    "v_mfma_f32_16x16x32_bf16 v[4:7], a[228:231], v[180:183], v[4:7]\n\t"
    "ds_read_b64_tr_b16 a[250:251], v229 offset:10240\n\t"
    "ds_read_b128 a[124:127], v227 offset:18432\n\t"
    "v_mfma_f32_16x16x32_bf16 v[4:7], a[224:227], v[164:167], v[4:7]\n\t"
    "buffer_load_dwordx4 v234, s[48:51], s71 offen offset:0 lds\n\t"
    "v_mfma_f32_16x16x32_bf16 v[8:11], a[224:227], v[168:171], v[8:11]\n\t"
    "ds_read_b64_tr_b16 a[252:253], v229 offset:12288\n\t"
    "v_mul_f32 v0, v252, v0\n\t"
    "v_mfma_f32_16x16x32_bf16 v[8:11], a[228:231], v[184:187], v[8:11]\n\t"
    "v_mul_f32 v1, v252, v1\n\t"
    "v_mul_f32 v2, v252, v2\n\t"
    "v_mfma_f32_16x16x32_bf16 v[8:11], a[232:235], v[200:203], v[8:11]\n\t"
    "ds_read_b64_tr_b16 a[254:255], v229 offset:14336\n\t"
    "v_mul_f32 v3, v252, v3\n\t"
    "v_mfma_f32_16x16x32_bf16 v[8:11], a[236:239], v[216:219], v[8:11]\n\t"
    "s_waitcnt lgkmcnt(5)\n\t"
    "v_cvt_pk_bf16_f32 v0, v0, v1\n\t"
    "v_cvt_pk_bf16_f32 v1, v2, v3\n\t"
    "v_mfma_f32_16x16x32_bf16 v[12:15], a[236:239], v[220:223], v[12:15]\n\t"
    "ds_read_b64_tr_b16 a[236:237], v230 offset:0\n\t"
    "v_mul_f32 v4, v253, v4\n\t"
    "v_mfma_f32_16x16x32_bf16 v[12:15], a[232:235], v[204:207], v[12:15]\n\t"
    "buffer_store_dwordx4 v[248:251], v237, s[56:59], s83 offen offset:1024 sc1\n\t"
    "v_mul_f32 v5, v253, v5\n\t"
    "v_mfma_f32_16x16x32_bf16 v[12:15], a[228:231], v[188:191], v[12:15]\n\t"
    "s_waitcnt lgkmcnt(4)\n\t"
    "ds_read_b64_tr_b16 a[238:239], v230 offset:2048\n\t"
    "v_mfma_f32_16x16x32_bf16 v[12:15], a[224:227], v[172:175], v[12:15]\n\t"
    "ds_write_b64 v235, v[0:1] offset:0\n\t"
    "v_mfma_f32_16x16x32_bf16 v[16:19], a[240:243], v[160:163], v[16:19]\n\t"
    "ds_read_b64_tr_b16 a[232:233], v230 offset:4096\n\t"
    "v_mul_f32 v6, v253, v6\n\t"
    "v_mfma_f32_16x16x32_bf16 v[16:19], a[244:247], v[176:179], v[16:19]\n\t"
    "s_waitcnt lgkmcnt(4)\n\t"
    "v_mul_f32 v7, v253, v7\n\t"
    "v_cvt_pk_bf16_f32 v4, v4, v5\n\t"
    "v_mfma_f32_16x16x32_bf16 v[16:19], a[248:251], v[192:195], v[16:19]\n\t"
    "ds_read_b64_tr_b16 a[234:235], v230 offset:6144\n\t"
    "v_cvt_pk_bf16_f32 v5, v6, v7\n\t"
    "v_mfma_f32_16x16x32_bf16 v[16:19], a[252:255], v[208:211], v[16:19]\n\t"
    "v_mul_f32 v8, v254, v8\n\t"
    "v_mul_f32 v9, v254, v9\n\t"
    "v_mfma_f32_16x16x32_bf16 v[20:23], a[252:255], v[212:215], v[20:23]\n\t"
    "ds_read_b64_tr_b16 a[228:229], v230 offset:8192\n\t"
    "v_mfma_f32_16x16x32_bf16 v[20:23], a[248:251], v[196:199], v[20:23]\n\t"
    "ds_write_b64 v235, v[4:5] offset:512\n\t"
    "v_mfma_f32_16x16x32_bf16 v[20:23], a[244:247], v[180:183], v[20:23]\n\t"
    "ds_read_b64_tr_b16 a[230:231], v230 offset:10240\n\t"
    "v_mul_f32 v10, v254, v10\n\t"
    "v_mfma_f32_16x16x32_bf16 v[20:23], a[240:243], v[164:167], v[20:23]\n\t"
    "buffer_load_dwordx4 v234, s[48:51], s71 offen offset:1024 lds\n\t"
    "v_mfma_f32_16x16x32_bf16 v[24:27], a[240:243], v[168:171], v[24:27]\n\t"
    "ds_read_b64_tr_b16 a[224:225], v230 offset:12288\n\t"
    "v_mul_f32 v11, v254, v11\n\t"
    "v_mfma_f32_16x16x32_bf16 v[24:27], a[244:247], v[184:187], v[24:27]\n\t"
    "v_cvt_pk_bf16_f32 v8, v8, v9\n\t"
    "v_cvt_pk_bf16_f32 v9, v10, v11\n\t"
    "v_mfma_f32_16x16x32_bf16 v[24:27], a[248:251], v[200:203], v[24:27]\n\t"
    "ds_read_b64_tr_b16 a[226:227], v230 offset:14336\n\t"
    "v_mul_f32 v12, v255, v12\n\t"
    "v_mfma_f32_16x16x32_bf16 v[24:27], a[252:255], v[216:219], v[24:27]\n\t"
    "v_mul_f32 v13, v255, v13\n\t"
    "v_add_u32 v231, 0x10000, v231\n\t"
    "v_mfma_f32_16x16x32_bf16 v[28:31], a[252:255], v[220:223], v[28:31]\n\t"
    "ds_read_b64_tr_b16 a[252:253], v231 offset:0\n\t"
    "ds_read_b128 v[244:247], v236 offset:1536\n\t"
    "v_mfma_f32_16x16x32_bf16 v[28:31], a[248:251], v[204:207], v[28:31]\n\t"
    "ds_write_b64 v235, v[8:9] offset:1024\n\t"
    "v_mfma_f32_16x16x32_bf16 v[28:31], a[244:247], v[188:191], v[28:31]\n\t"
    "ds_read_b64_tr_b16 a[254:255], v231 offset:2048\n\t"
    "v_mul_f32 v14, v255, v14\n\t"
    "v_mfma_f32_16x16x32_bf16 v[28:31], a[240:243], v[172:175], v[28:31]\n\t"
    "s_waitcnt lgkmcnt(6)\n\t"
    "v_mul_f32 v15, v255, v15\n\t"
    "v_cvt_pk_bf16_f32 v12, v12, v13\n\t"
    "v_mfma_f32_16x16x32_bf16 v[32:35], a[236:239], v[160:163], v[32:35]\n\t"
    "ds_read_b64_tr_b16 a[248:249], v231 offset:4096\n\t"
    "ds_read_b128 v[248:251], v236 offset:1792\n\t"
    "v_cvt_pk_bf16_f32 v13, v14, v15\n\t"
    "v_mfma_f32_16x16x32_bf16 v[32:35], a[232:235], v[176:179], v[32:35]\n\t"
    "ds_write_b64 v235, v[12:13] offset:1536\n\t"
    "v_mfma_f32_16x16x32_bf16 v[32:35], a[228:231], v[192:195], v[32:35]\n\t"
    "s_waitcnt lgkmcnt(7)\n\t"
    "ds_read_b64_tr_b16 a[250:251], v231 offset:6144\n\t"
    "v_mul_f32 v16, v252, v16\n\t"
    "v_mfma_f32_16x16x32_bf16 v[32:35], a[224:227], v[208:211], v[32:35]\n\t"
    "v_mul_f32 v17, v252, v17\n\t"
    "v_mul_f32 v18, v252, v18\n\t"
    "v_mfma_f32_16x16x32_bf16 v[36:39], a[224:227], v[212:215], v[36:39]\n\t"
    "ds_read_b64_tr_b16 a[244:245], v231 offset:8192\n\t"
    "v_mul_f32 v19, v252, v19\n\t"
    "v_mfma_f32_16x16x32_bf16 v[36:39], a[228:231], v[196:199], v[36:39]\n\t"
    "s_waitcnt lgkmcnt(7)\n\t"
    "v_cvt_pk_bf16_f32 v16, v16, v17\n\t"
    "v_cvt_pk_bf16_f32 v17, v18, v19\n\t"
    "v_mfma_f32_16x16x32_bf16 v[36:39], a[232:235], v[180:183], v[36:39]\n\t"
    "ds_read_b64_tr_b16 a[246:247], v231 offset:10240\n\t"
    "v_mul_f32 v20, v253, v20\n\t"
    "v_mfma_f32_16x16x32_bf16 v[36:39], a[236:239], v[164:167], v[36:39]\n\t"
    "buffer_load_dwordx4 v234, s[48:51], s71 offen offset:2048 lds\n\t"
    "v_mfma_f32_16x16x32_bf16 v[40:43], a[236:239], v[168:171], v[40:43]\n\t"
    "ds_read_b64_tr_b16 a[240:241], v231 offset:12288\n\t"
    "v_mul_f32 v21, v253, v21\n\t"
    "v_mfma_f32_16x16x32_bf16 v[40:43], a[232:235], v[184:187], v[40:43]\n\t"
    "buffer_store_dwordx4 v[244:247], v237, s[56:59], s83 offen offset:2048 sc1\n\t"
    "v_mfma_f32_16x16x32_bf16 v[40:43], a[228:231], v[200:203], v[40:43]\n\t"
    "ds_read_b64_tr_b16 a[242:243], v231 offset:14336\n\t"
    "v_mfma_f32_16x16x32_bf16 v[40:43], a[224:227], v[216:219], v[40:43]\n\t"
    "ds_write_b64 v235, v[16:17] offset:128\n\t"
    "v_mfma_f32_16x16x32_bf16 v[44:47], a[224:227], v[220:223], v[44:47]\n\t"
    "v_mul_f32 v22, v253, v22\n\t"
    "v_mul_f32 v23, v253, v23\n\t"
    "v_mfma_f32_16x16x32_bf16 v[44:47], a[228:231], v[204:207], v[44:47]\n\t"
    "v_cvt_pk_bf16_f32 v20, v20, v21\n\t"
    "v_cvt_pk_bf16_f32 v21, v22, v23\n\t"
    "v_mfma_f32_16x16x32_bf16 v[44:47], a[232:235], v[188:191], v[44:47]\n\t"
    "s_waitcnt lgkmcnt(3)\n\t"
    "v_mul_f32 v24, v254, v24\n\t"
    "v_mul_f32 v25, v254, v25\n\t"
    "v_mfma_f32_16x16x32_bf16 v[44:47], a[236:239], v[172:175], v[44:47]\n\t"
    "ds_write_b64 v235, v[20:21] offset:640\n\t"
    "v_mfma_f32_16x16x32_bf16 v[48:51], a[252:255], v[160:163], v[48:51]\n\t"
    "v_mul_f32 v26, v254, v26\n\t"
    "v_mul_f32 v27, v254, v27\n\t"
    "v_mfma_f32_16x16x32_bf16 v[48:51], a[248:251], v[176:179], v[48:51]\n\t"
    "s_waitcnt lgkmcnt(2)\n\t"
    "v_cvt_pk_bf16_f32 v24, v24, v25\n\t"
    "v_cvt_pk_bf16_f32 v25, v26, v27\n\t"
    "v_mfma_f32_16x16x32_bf16 v[48:51], a[244:247], v[192:195], v[48:51]\n\t"
    "s_waitcnt vmcnt(17)\n\t"
    "v_mul_f32 v28, v255, v28\n\t"
    "v_mul_f32 v29, v255, v29\n\t"
    "v_mfma_f32_16x16x32_bf16 v[48:51], a[240:243], v[208:211], v[48:51]\n\t"
    "s_barrier\n\t"
    "v_mfma_f32_16x16x32_bf16 v[52:55], a[240:243], v[212:215], v[52:55]\n\t"
    "ds_read_b64_tr_b16 a[224:225], v228 offset:32768\n\t"
    "ds_read_b64_tr_b16 a[226:227], v228 offset:34816\n\t"
    "v_mfma_f32_16x16x32_bf16 v[52:55], a[244:247], v[196:199], v[52:55]\n\t"
    "ds_read_b64_tr_b16 a[228:229], v228 offset:36864\n\t"
    "ds_read_b64_tr_b16 a[230:231], v228 offset:38912\n\t"
    "v_mfma_f32_16x16x32_bf16 v[52:55], a[248:251], v[180:183], v[52:55]\n\t"
    "ds_read_b64_tr_b16 a[232:233], v228 offset:40960\n\t"
    "ds_write_b64 v235, v[24:25] offset:1152\n\t"
    "v_mfma_f32_16x16x32_bf16 v[52:55], a[252:255], v[164:167], v[52:55]\n\t"
    "ds_read_b64_tr_b16 a[234:235], v228 offset:43008\n\t"
    "v_mul_f32 v30, v255, v30\n\t"
    "v_mfma_f32_16x16x32_bf16 v[56:59], a[252:255], v[168:171], v[56:59]\n\t"
    "ds_read_b64_tr_b16 a[236:237], v228 offset:45056\n\t"
    "v_mul_f32 v31, v255, v31\n\t"
    "v_mfma_f32_16x16x32_bf16 v[56:59], a[248:251], v[184:187], v[56:59]\n\t"
    "buffer_load_dwordx4 v234, s[48:51], s71 offen offset:3072 lds\n\t"
    "v_mfma_f32_16x16x32_bf16 v[56:59], a[244:247], v[200:203], v[56:59]\n\t"
    "ds_read_b64_tr_b16 a[238:239], v228 offset:47104\n\t"
    "v_cvt_pk_bf16_f32 v28, v28, v29\n\t"
    "v_mfma_f32_16x16x32_bf16 v[56:59], a[240:243], v[216:219], v[56:59]\n\t"
    "s_xor_b32 s71, s66, 0x180\n\t"
    "v_cvt_pk_bf16_f32 v29, v30, v31\n\t"
    "v_mfma_f32_16x16x32_bf16 v[60:63], a[240:243], v[220:223], v[60:63]\n\t"
    "s_add_u32 m0, s70, 0x8000\n\t"
    "ds_read_b64_tr_b16 a[240:241], v229 offset:32768\n\t"
    "v_mfma_f32_16x16x32_bf16 v[60:63], a[244:247], v[204:207], v[60:63]\n\t"
    "ds_write_b64 v235, v[28:29] offset:1664\n\t"
    "v_mul_f32 v32, v252, v32\n\t"
    "v_mfma_f32_16x16x32_bf16 v[60:63], a[248:251], v[188:191], v[60:63]\n\t"
    "ds_read_b64_tr_b16 a[242:243], v229 offset:34816\n\t"
    "v_mul_f32 v33, v252, v33\n\t"
    "v_mfma_f32_16x16x32_bf16 v[60:63], a[252:255], v[172:175], v[60:63]\n\t"
    "s_waitcnt lgkmcnt(5)\n\t"
    "v_mul_f32 v34, v252, v34\n\t"
    "v_mul_f32 v35, v252, v35\n\t"
    "v_mfma_f32_16x16x32_bf16 v[0:3], a[224:227], v[64:67], 0\n\t"
    "ds_read_b64_tr_b16 a[244:245], v229 offset:36864\n\t"
    "v_cvt_pk_bf16_f32 v32, v32, v33\n\t"
    "v_mfma_f32_16x16x32_bf16 v[0:3], a[228:231], v[80:83], v[0:3]\n\t"
    "s_waitcnt lgkmcnt(4)\n\t"
    "v_cvt_pk_bf16_f32 v33, v34, v35\n\t"
    "v_mul_f32 v36, v253, v36\n\t"
    "v_mfma_f32_16x16x32_bf16 v[0:3], a[232:235], a[128:131], v[0:3]\n\t"
    "ds_read_b64_tr_b16 a[246:247], v229 offset:38912\n\t"
    "v_mul_f32 v37, v253, v37\n\t"
    "v_mfma_f32_16x16x32_bf16 v[0:3], a[236:239], a[144:147], v[0:3]\n\t"
    "ds_write_b64 v235, v[32:33] offset:16384\n\t"
    "v_mfma_f32_16x16x32_bf16 v[4:7], a[236:239], a[148:151], 0\n\t"
    "ds_read_b64_tr_b16 a[248:249], v229 offset:40960\n\t"
    "v_mul_f32 v38, v253, v38\n\t"
    "v_mfma_f32_16x16x32_bf16 v[4:7], a[232:235], a[132:135], v[4:7]\n\t"
    "v_mul_f32 v39, v253, v39\n\t"
    "v_cvt_pk_bf16_f32 v36, v36, v37\n\t"
    "v_mfma_f32_16x16x32_bf16 v[4:7], a[228:231], v[84:87], v[4:7]\n\t"
    "ds_read_b64_tr_b16 a[250:251], v229 offset:43008\n\t"
    "v_cvt_pk_bf16_f32 v37, v38, v39\n\t"
    "v_mfma_f32_16x16x32_bf16 v[4:7], a[224:227], v[68:71], v[4:7]\n\t"
    "buffer_load_dwordx4 v234, s[48:51], s71 offen offset:0 lds\n\t"
    "v_mfma_f32_16x16x32_bf16 v[8:11], a[224:227], v[72:75], 0\n\t"
    "ds_read_b64_tr_b16 a[252:253], v229 offset:45056\n\t"
    "v_mul_f32 v40, v254, v40\n\t"
    "v_mfma_f32_16x16x32_bf16 v[8:11], a[228:231], v[88:91], v[8:11]\n\t"
    "v_mul_f32 v41, v254, v41\n\t"
    "v_mfma_f32_16x16x32_bf16 v[8:11], a[232:235], a[136:139], v[8:11]\n\t"
    "ds_read_b64_tr_b16 a[254:255], v229 offset:47104\n\t"
    "buffer_store_dwordx4 v[248:251], v237, s[56:59], s83 offen offset:3072 sc1\n\t"
    "v_mfma_f32_16x16x32_bf16 v[8:11], a[236:239], a[152:155], v[8:11]\n\t"
    "ds_write_b64 v235, v[36:37] offset:16896\n\t"
    "v_mfma_f32_16x16x32_bf16 v[12:15], a[236:239], a[156:159], 0\n\t"
    "ds_read_b64_tr_b16 a[236:237], v230 offset:32768\n\t"
    "v_mul_f32 v42, v254, v42\n\t"
    "v_mfma_f32_16x16x32_bf16 v[12:15], a[232:235], a[140:143], v[12:15]\n\t"
    "v_mul_f32 v43, v254, v43\n\t"
    "v_cvt_pk_bf16_f32 v40, v40, v41\n\t"
    "v_mfma_f32_16x16x32_bf16 v[12:15], a[228:231], v[92:95], v[12:15]\n\t"
    "ds_read_b64_tr_b16 a[238:239], v230 offset:34816\n\t"
    "v_cvt_pk_bf16_f32 v41, v42, v43\n\t"
    "v_mfma_f32_16x16x32_bf16 v[12:15], a[224:227], v[76:79], v[12:15]\n\t"
    "s_waitcnt lgkmcnt(5)\n\t"
    "v_mul_f32 v44, v255, v44\n\t"
    "v_mul_f32 v45, v255, v45\n\t"
    "v_mfma_f32_16x16x32_bf16 v[16:19], a[240:243], v[64:67], 0\n\t"
    "ds_read_b64_tr_b16 a[232:233], v230 offset:36864\n\t"
    "v_mfma_f32_16x16x32_bf16 v[16:19], a[244:247], v[80:83], v[16:19]\n\t"
    "ds_write_b64 v235, v[40:41] offset:17408\n\t"
    "v_mfma_f32_16x16x32_bf16 v[16:19], a[248:251], a[128:131], v[16:19]\n\t"
    "s_waitcnt lgkmcnt(5)\n\t"
    "ds_read_b64_tr_b16 a[234:235], v230 offset:38912\n\t"
    "v_mul_f32 v46, v255, v46\n\t"
    "v_mfma_f32_16x16x32_bf16 v[16:19], a[252:255], a[144:147], v[16:19]\n\t"
    "v_mul_f32 v47, v255, v47\n\t"
    "v_cvt_pk_bf16_f32 v44, v44, v45\n\t"
    "v_mfma_f32_16x16x32_bf16 v[20:23], a[252:255], a[148:151], 0\n\t"
    "ds_read_b64_tr_b16 a[228:229], v230 offset:40960\n\t"
    "v_cvt_pk_bf16_f32 v45, v46, v47\n\t"
    "v_mfma_f32_16x16x32_bf16 v[20:23], a[248:251], a[132:135], v[20:23]\n\t"
    "ds_write_b64 v235, v[44:45] offset:17920\n\t"
    "v_mfma_f32_16x16x32_bf16 v[20:23], a[244:247], v[84:87], v[20:23]\n\t"
    "ds_read_b64_tr_b16 a[230:231], v230 offset:43008\n\t"
    "v_mul_f32 v48, v252, v48\n\t"
    "v_mfma_f32_16x16x32_bf16 v[20:23], a[240:243], v[68:71], v[20:23]\n\t"
    "buffer_load_dwordx4 v234, s[48:51], s71 offen offset:1024 lds\n\t"
    "v_mfma_f32_16x16x32_bf16 v[24:27], a[240:243], v[72:75], 0\n\t"
    "ds_read_b64_tr_b16 a[224:225], v230 offset:45056\n\t"
    "v_mul_f32 v49, v252, v49\n\t"
    "v_mfma_f32_16x16x32_bf16 v[24:27], a[244:247], v[88:91], v[24:27]\n\t"
    "v_mul_f32 v50, v252, v50\n\t"
    "v_mul_f32 v51, v252, v51\n\t"
    "v_mfma_f32_16x16x32_bf16 v[24:27], a[248:251], a[136:139], v[24:27]\n\t"
    "ds_read_b64_tr_b16 a[226:227], v230 offset:47104\n\t"
    "v_cvt_pk_bf16_f32 v48, v48, v49\n\t"
    "v_mfma_f32_16x16x32_bf16 v[24:27], a[252:255], a[152:155], v[24:27]\n\t"
    "v_cvt_pk_bf16_f32 v49, v50, v51\n\t"
    "v_mul_f32 v52, v253, v52\n\t"
    "v_mfma_f32_16x16x32_bf16 v[28:31], a[252:255], a[156:159], 0\n\t"
    "ds_read_b64_tr_b16 a[252:253], v231 offset:32768\n\t"
    "v_mul_f32 v53, v253, v53\n\t"
    "v_mfma_f32_16x16x32_bf16 v[28:31], a[248:251], a[140:143], v[28:31]\n\t"
    "ds_write_b64 v235, v[48:49] offset:16512\n\t"
    "v_mfma_f32_16x16x32_bf16 v[28:31], a[244:247], v[92:95], v[28:31]\n\t"
    "ds_read_b64_tr_b16 a[254:255], v231 offset:34816\n\t"
    "v_mul_f32 v54, v253, v54\n\t"
    "v_mfma_f32_16x16x32_bf16 v[28:31], a[240:243], v[76:79], v[28:31]\n\t"
    "s_waitcnt lgkmcnt(5)\n\t"
    "v_mul_f32 v55, v253, v55\n\t"
    "v_cvt_pk_bf16_f32 v52, v52, v53\n\t"
    "v_mfma_f32_16x16x32_bf16 v[32:35], a[236:239], v[64:67], 0\n\t"
    "ds_read_b64_tr_b16 a[248:249], v231 offset:36864\n\t"
    "v_cvt_pk_bf16_f32 v53, v54, v55\n\t"
    "v_mfma_f32_16x16x32_bf16 v[32:35], a[232:235], v[80:83], v[32:35]\n\t"
    "s_waitcnt lgkmcnt(4)\n\t"
    "v_mul_f32 v56, v254, v56\n\t"
    "v_mul_f32 v57, v254, v57\n\t"
    "v_mfma_f32_16x16x32_bf16 v[32:35], a[228:231], a[128:131], v[32:35]\n\t"
    "ds_read_b64_tr_b16 a[250:251], v231 offset:38912\n\t"
    "ds_read_b128 v[244:247], v236 offset:0\n\t"
    "v_mfma_f32_16x16x32_bf16 v[32:35], a[224:227], a[144:147], v[32:35]\n\t"
    "ds_write_b64 v235, v[52:53] offset:17024\n\t"
    "v_mfma_f32_16x16x32_bf16 v[36:39], a[224:227], a[148:151], 0\n\t"
    "ds_read_b64_tr_b16 a[244:245], v231 offset:40960\n\t"
    "v_mul_f32 v58, v254, v58\n\t"
    "v_mfma_f32_16x16x32_bf16 v[36:39], a[228:231], a[132:135], v[36:39]\n\t"
    "v_mul_f32 v59, v254, v59\n\t"
    "v_cvt_pk_bf16_f32 v56, v56, v57\n\t"
    "v_mfma_f32_16x16x32_bf16 v[36:39], a[232:235], v[84:87], v[36:39]\n\t"
    "ds_read_b64_tr_b16 a[246:247], v231 offset:43008\n\t"
    "v_cvt_pk_bf16_f32 v57, v58, v59\n\t"
    "v_mfma_f32_16x16x32_bf16 v[36:39], a[236:239], v[68:71], v[36:39]\n\t"
    "buffer_load_dwordx4 v234, s[48:51], s71 offen offset:2048 lds\n\t"
    "v_mfma_f32_16x16x32_bf16 v[40:43], a[236:239], v[72:75], 0\n\t"
    "ds_read_b64_tr_b16 a[240:241], v231 offset:45056\n\t"
    "v_mul_f32 v60, v255, v60\n\t"
    "v_mfma_f32_16x16x32_bf16 v[40:43], a[232:235], v[88:91], v[40:43]\n\t"
    "v_mul_f32 v61, v255, v61\n\t"
    "v_mfma_f32_16x16x32_bf16 v[40:43], a[228:231], a[136:139], v[40:43]\n\t"
    "ds_read_b64_tr_b16 a[242:243], v231 offset:47104\n\t"
    "v_mfma_f32_16x16x32_bf16 v[40:43], a[224:227], a[152:155], v[40:43]\n\t"
    "ds_write_b64 v235, v[56:57] offset:17536\n\t"
    "v_mfma_f32_16x16x32_bf16 v[44:47], a[224:227], a[156:159], 0\n\t"
    "s_xor_b32 s83, s68, 0x80\n\t"
    "v_mul_f32 v62, v255, v62\n\t"
    "v_mul_f32 v63, v255, v63\n\t"
    "v_mfma_f32_16x16x32_bf16 v[44:47], a[228:231], a[140:143], v[44:47]\n\t"
    "s_waitcnt lgkmcnt(6)\n\t"
    "v_cvt_pk_bf16_f32 v60, v60, v61\n\t"
    "v_cvt_pk_bf16_f32 v61, v62, v63\n\t"
    "v_mfma_f32_16x16x32_bf16 v[44:47], a[232:235], v[92:95], v[44:47]\n\t"
    "ds_write_b64 v235, v[60:61] offset:18048\n\t"
    "v_mfma_f32_16x16x32_bf16 v[44:47], a[236:239], v[76:79], v[44:47]\n\t"
    "s_waitcnt lgkmcnt(4)\n\t"
    "buffer_store_dwordx4 v[244:247], v237, s[56:59], s83 offen offset:0 sc1\n\t"
    "v_mfma_f32_16x16x32_bf16 v[48:51], a[252:255], v[64:67], 0\n\t"
    "v_mfma_f32_16x16x32_bf16 v[48:51], a[248:251], v[80:83], v[48:51]\n\t"
    "s_waitcnt lgkmcnt(2)\n\t"
    "v_mfma_f32_16x16x32_bf16 v[48:51], a[244:247], a[128:131], v[48:51]\n\t"
    "s_waitcnt vmcnt(17)\n\t"
    "v_mfma_f32_16x16x32_bf16 v[48:51], a[240:243], a[144:147], v[48:51]\n\t"
    "s_barrier\n\t"
    "v_mfma_f32_16x16x32_bf16 v[52:55], a[240:243], a[148:151], 0\n\t"
    "ds_read_b64_tr_b16 a[224:225], v228 offset:49152\n\t"
    "ds_read_b64_tr_b16 a[226:227], v228 offset:51200\n\t"
    "v_mfma_f32_16x16x32_bf16 v[52:55], a[244:247], a[132:135], v[52:55]\n\t"
    "ds_read_b64_tr_b16 a[228:229], v228 offset:53248\n\t"
    "ds_read_b64_tr_b16 a[230:231], v228 offset:55296\n\t"
    "v_mfma_f32_16x16x32_bf16 v[52:55], a[248:251], v[84:87], v[52:55]\n\t"
    "ds_read_b64_tr_b16 a[232:233], v228 offset:57344\n\t"
    "v_mfma_f32_16x16x32_bf16 v[52:55], a[252:255], v[68:71], v[52:55]\n\t"
    "ds_read_b64_tr_b16 a[234:235], v228 offset:59392\n\t"
    "v_mfma_f32_16x16x32_bf16 v[56:59], a[252:255], v[72:75], 0\n\t"
    "ds_read_b64_tr_b16 a[236:237], v228 offset:61440\n\t"
    "v_mfma_f32_16x16x32_bf16 v[56:59], a[248:251], v[88:91], v[56:59]\n\t"
    "buffer_load_dwordx4 v234, s[48:51], s71 offen offset:3072 lds\n\t"
    "v_mfma_f32_16x16x32_bf16 v[56:59], a[244:247], a[136:139], v[56:59]\n\t"
    "ds_read_b64_tr_b16 a[238:239], v228 offset:63488\n\t"
    "v_mfma_f32_16x16x32_bf16 v[56:59], a[240:243], a[152:155], v[56:59]\n\t"
    "v_mfma_f32_16x16x32_bf16 v[60:63], a[240:243], a[156:159], 0\n\t"
    "ds_read_b64_tr_b16 a[240:241], v229 offset:49152\n\t"
    "v_mfma_f32_16x16x32_bf16 v[60:63], a[244:247], a[140:143], v[60:63]\n\t"
    "v_mfma_f32_16x16x32_bf16 v[60:63], a[248:251], v[92:95], v[60:63]\n\t"
    "ds_read_b64_tr_b16 a[242:243], v229 offset:51200\n\t"
    "v_mfma_f32_16x16x32_bf16 v[60:63], a[252:255], v[76:79], v[60:63]\n\t"
    "s_waitcnt lgkmcnt(4)\n\t"
    "v_mfma_f32_16x16x32_bf16 v[0:3], a[224:227], a[160:163], v[0:3]\n\t"
    "ds_read_b64_tr_b16 a[244:245], v229 offset:53248\n\t"
    "v_mfma_f32_16x16x32_bf16 v[0:3], a[228:231], a[176:179], v[0:3]\n\t"
    "v_mfma_f32_16x16x32_bf16 v[0:3], a[232:235], a[192:195], v[0:3]\n\t"
    "s_waitcnt lgkmcnt(3)\n\t"
    "ds_read_b64_tr_b16 a[246:247], v229 offset:55296\n\t"
    "v_mfma_f32_16x16x32_bf16 v[0:3], a[236:239], a[208:211], v[0:3]\n\t"
    "v_mfma_f32_16x16x32_bf16 v[4:7], a[236:239], a[212:215], v[4:7]\n\t"
    "ds_read_b64_tr_b16 a[248:249], v229 offset:57344\n\t"
    "ds_read_b128 v[248:251], v236 offset:256\n\t"
    "v_mfma_f32_16x16x32_bf16 v[4:7], a[232:235], a[196:199], v[4:7]\n\t"
    "s_xor_b32 s71, s66, 0x10180\n\t"
    "s_add_u32 m0, s70, 0xc000\n\t"
    "v_mfma_f32_16x16x32_bf16 v[4:7], a[228:231], a[180:183], v[4:7]\n\t"
    "ds_read_b64_tr_b16 a[250:251], v229 offset:59392\n\t"
    "v_mfma_f32_16x16x32_bf16 v[4:7], a[224:227], a[164:167], v[4:7]\n\t"
    "buffer_load_dwordx4 v234, s[48:51], s71 offen offset:0 lds\n\t"
    "v_mfma_f32_16x16x32_bf16 v[8:11], a[224:227], a[168:171], v[8:11]\n\t"
    "ds_read_b64_tr_b16 a[252:253], v229 offset:61440\n\t"
    "v_mfma_f32_16x16x32_bf16 v[8:11], a[228:231], a[184:187], v[8:11]\n\t"
    "v_mfma_f32_16x16x32_bf16 v[8:11], a[232:235], a[200:203], v[8:11]\n\t"
    "s_waitcnt lgkmcnt(2)\n\t"
    "ds_read_b64_tr_b16 a[254:255], v229 offset:63488\n\t"
    "v_mfma_f32_16x16x32_bf16 v[8:11], a[236:239], a[216:219], v[8:11]\n\t"
    "buffer_store_dwordx4 v[248:251], v237, s[56:59], s83 offen offset:1024 sc1\n\t"
    "v_mfma_f32_16x16x32_bf16 v[12:15], a[236:239], a[220:223], v[12:15]\n\t"
    "ds_read_b64_tr_b16 a[236:237], v230 offset:49152\n\t"
    "v_mfma_f32_16x16x32_bf16 v[12:15], a[232:235], a[204:207], v[12:15]\n\t"
    "v_mfma_f32_16x16x32_bf16 v[12:15], a[228:231], a[188:191], v[12:15]\n\t"
    "ds_read_b64_tr_b16 a[238:239], v230 offset:51200\n\t"
    "v_mfma_f32_16x16x32_bf16 v[12:15], a[224:227], a[172:175], v[12:15]\n\t"
    "s_waitcnt lgkmcnt(4)\n\t"
    "v_mfma_f32_16x16x32_bf16 v[16:19], a[240:243], a[160:163], v[16:19]\n\t"
    "ds_read_b64_tr_b16 a[232:233], v230 offset:53248\n\t"
    "v_mfma_f32_16x16x32_bf16 v[16:19], a[244:247], a[176:179], v[16:19]\n\t"
    "v_mfma_f32_16x16x32_bf16 v[16:19], a[248:251], a[192:195], v[16:19]\n\t"
    "s_waitcnt lgkmcnt(3)\n\t"
    "ds_read_b64_tr_b16 a[234:235], v230 offset:55296\n\t"
    "v_mfma_f32_16x16x32_bf16 v[16:19], a[252:255], a[208:211], v[16:19]\n\t"
    "v_mfma_f32_16x16x32_bf16 v[20:23], a[252:255], a[212:215], v[20:23]\n\t"
    "ds_read_b64_tr_b16 a[228:229], v230 offset:57344\n\t"
    "v_mfma_f32_16x16x32_bf16 v[20:23], a[248:251], a[196:199], v[20:23]\n\t"
    "v_mfma_f32_16x16x32_bf16 v[20:23], a[244:247], a[180:183], v[20:23]\n\t"
    "ds_read_b64_tr_b16 a[230:231], v230 offset:59392\n\t"
    "v_mfma_f32_16x16x32_bf16 v[20:23], a[240:243], a[164:167], v[20:23]\n\t"
    "buffer_load_dwordx4 v234, s[48:51], s71 offen offset:1024 lds\n\t"
    "v_mfma_f32_16x16x32_bf16 v[24:27], a[240:243], a[168:171], v[24:27]\n\t"
    "ds_read_b64_tr_b16 a[224:225], v230 offset:61440\n\t"
    "v_mfma_f32_16x16x32_bf16 v[24:27], a[244:247], a[184:187], v[24:27]\n\t"
    "v_mfma_f32_16x16x32_bf16 v[24:27], a[248:251], a[200:203], v[24:27]\n\t"
    "ds_read_b64_tr_b16 a[226:227], v230 offset:63488\n\t"
    "v_mfma_f32_16x16x32_bf16 v[24:27], a[252:255], a[216:219], v[24:27]\n\t"
    "v_mfma_f32_16x16x32_bf16 v[28:31], a[252:255], a[220:223], v[28:31]\n\t"
    "ds_read_b64_tr_b16 a[252:253], v231 offset:49152\n\t"
    "v_mfma_f32_16x16x32_bf16 v[28:31], a[248:251], a[204:207], v[28:31]\n\t"
    "v_mfma_f32_16x16x32_bf16 v[28:31], a[244:247], a[188:191], v[28:31]\n\t"
    "ds_read_b64_tr_b16 a[254:255], v231 offset:51200\n\t"
    "v_mfma_f32_16x16x32_bf16 v[28:31], a[240:243], a[172:175], v[28:31]\n\t"
    "s_waitcnt lgkmcnt(4)\n\t"
    "v_mfma_f32_16x16x32_bf16 v[32:35], a[236:239], a[160:163], v[32:35]\n\t"
    "ds_read_b64_tr_b16 a[248:249], v231 offset:53248\n\t"
    "v_mfma_f32_16x16x32_bf16 v[32:35], a[232:235], a[176:179], v[32:35]\n\t"
    "v_mfma_f32_16x16x32_bf16 v[32:35], a[228:231], a[192:195], v[32:35]\n\t"
    "s_waitcnt lgkmcnt(3)\n\t"
    "ds_read_b64_tr_b16 a[250:251], v231 offset:55296\n\t"
    "v_mfma_f32_16x16x32_bf16 v[32:35], a[224:227], a[208:211], v[32:35]\n\t"
    "v_mfma_f32_16x16x32_bf16 v[36:39], a[224:227], a[212:215], v[36:39]\n\t"
    "ds_read_b64_tr_b16 a[244:245], v231 offset:57344\n\t"
    "ds_read_b128 v[244:247], v236 offset:512\n\t"
    "v_mfma_f32_16x16x32_bf16 v[36:39], a[228:231], a[196:199], v[36:39]\n\t"
    "v_mfma_f32_16x16x32_bf16 v[36:39], a[232:235], a[180:183], v[36:39]\n\t"
    "ds_read_b64_tr_b16 a[246:247], v231 offset:59392\n\t"
    "v_mfma_f32_16x16x32_bf16 v[36:39], a[236:239], a[164:167], v[36:39]\n\t"
    "buffer_load_dwordx4 v234, s[48:51], s71 offen offset:2048 lds\n\t"
    "v_mfma_f32_16x16x32_bf16 v[40:43], a[236:239], a[168:171], v[40:43]\n\t"
    "ds_read_b64_tr_b16 a[240:241], v231 offset:61440\n\t"
    "v_mfma_f32_16x16x32_bf16 v[40:43], a[232:235], a[184:187], v[40:43]\n\t"
    "v_mfma_f32_16x16x32_bf16 v[40:43], a[228:231], a[200:203], v[40:43]\n\t"
    "s_waitcnt lgkmcnt(2)\n\t"
    "ds_read_b64_tr_b16 a[242:243], v231 offset:63488\n\t"
    "v_mfma_f32_16x16x32_bf16 v[40:43], a[224:227], a[216:219], v[40:43]\n\t"
    "buffer_store_dwordx4 v[244:247], v237, s[56:59], s83 offen offset:2048 sc1\n\t"
    "v_mfma_f32_16x16x32_bf16 v[44:47], a[224:227], a[220:223], v[44:47]\n\t"
    "v_mfma_f32_16x16x32_bf16 v[44:47], a[228:231], a[204:207], v[44:47]\n\t"
    "v_mfma_f32_16x16x32_bf16 v[44:47], a[232:235], a[188:191], v[44:47]\n\t"
    "v_mfma_f32_16x16x32_bf16 v[44:47], a[236:239], a[172:175], v[44:47]\n\t"
    "s_waitcnt lgkmcnt(2)\n\t"
    "v_mfma_f32_16x16x32_bf16 v[48:51], a[252:255], a[160:163], v[48:51]\n\t"
    "v_mfma_f32_16x16x32_bf16 v[48:51], a[248:251], a[176:179], v[48:51]\n\t"
    "s_waitcnt lgkmcnt(0)\n\t"
    "v_mfma_f32_16x16x32_bf16 v[48:51], a[244:247], a[192:195], v[48:51]\n\t"
    "s_waitcnt vmcnt(17)\n\t"
    "v_add_u32 v228, 0xffff0000, v228\n\t"
    "v_mfma_f32_16x16x32_bf16 v[48:51], a[240:243], a[208:211], v[48:51]\n\t"
    "s_barrier\n\t"
    "v_mfma_f32_16x16x32_bf16 v[52:55], a[240:243], a[212:215], v[52:55]\n\t"
    "ds_read_b64_tr_b16 a[224:225], v228 offset:0\n\t"
    "ds_read_b64_tr_b16 a[226:227], v228 offset:2048\n\t"
    "v_mfma_f32_16x16x32_bf16 v[52:55], a[244:247], a[196:199], v[52:55]\n\t"
    "ds_read_b64_tr_b16 a[228:229], v228 offset:4096\n\t"
    "ds_read_b64_tr_b16 a[230:231], v228 offset:6144\n\t"
    "v_mfma_f32_16x16x32_bf16 v[52:55], a[248:251], a[180:183], v[52:55]\n\t"
    "ds_read_b64_tr_b16 a[232:233], v228 offset:8192\n\t"
    "v_mfma_f32_16x16x32_bf16 v[52:55], a[252:255], a[164:167], v[52:55]\n\t"
    "ds_read_b64_tr_b16 a[234:235], v228 offset:10240\n\t"
    "v_mfma_f32_16x16x32_bf16 v[56:59], a[252:255], a[168:171], v[56:59]\n\t"
    "ds_read_b64_tr_b16 a[236:237], v228 offset:12288\n\t"
    "v_mfma_f32_16x16x32_bf16 v[56:59], a[248:251], a[184:187], v[56:59]\n\t"
    "buffer_load_dwordx4 v234, s[48:51], s71 offen offset:3072 lds\n\t"
    "v_mfma_f32_16x16x32_bf16 v[56:59], a[244:247], a[200:203], v[56:59]\n\t"
    "ds_read_b64_tr_b16 a[238:239], v228 offset:14336\n\t"
    "v_mfma_f32_16x16x32_bf16 v[56:59], a[240:243], a[216:219], v[56:59]\n\t"
    "v_add_u32 v229, 0xffff0000, v229\n\t"
    "v_mfma_f32_16x16x32_bf16 v[60:63], a[240:243], a[220:223], v[60:63]\n\t"
    "ds_read_b64_tr_b16 a[240:241], v229 offset:0\n\t"
    "v_mfma_f32_16x16x32_bf16 v[60:63], a[244:247], a[204:207], v[60:63]\n\t"
    "v_mfma_f32_16x16x32_bf16 v[60:63], a[248:251], a[188:191], v[60:63]\n\t"
    "ds_read_b64_tr_b16 a[242:243], v229 offset:2048\n\t"
    "v_mfma_f32_16x16x32_bf16 v[60:63], a[252:255], a[172:175], v[60:63]\n\t"
    "s_waitcnt lgkmcnt(4)\n\t"
    "v_mfma_f32_16x16x32_bf16 v[0:3], a[224:227], v[96:99], v[0:3]\n\t"
    "ds_read_b64_tr_b16 a[244:245], v229 offset:4096\n\t"
    "v_mfma_f32_16x16x32_bf16 v[0:3], a[228:231], v[112:115], v[0:3]\n\t"
    "v_mfma_f32_16x16x32_bf16 v[0:3], a[232:235], v[128:131], v[0:3]\n\t"
    "s_waitcnt lgkmcnt(3)\n\t"
    "ds_read_b64_tr_b16 a[246:247], v229 offset:6144\n\t"
    "v_mfma_f32_16x16x32_bf16 v[0:3], a[236:239], v[144:147], v[0:3]\n\t"
    "v_mfma_f32_16x16x32_bf16 v[4:7], a[236:239], v[148:151], v[4:7]\n\t"
    "ds_read_b64_tr_b16 a[248:249], v229 offset:8192\n\t"
    "ds_read_b128 v[248:251], v236 offset:768\n\t"
    "v_mfma_f32_16x16x32_bf16 v[4:7], a[232:235], v[132:135], v[4:7]\n\t"
    "s_xor_b32 s71, s66, 0x20180\n\t"
    "v_mfma_f32_16x16x32_bf16 v[4:7], a[228:231], v[116:119], v[4:7]\n\t"
    "s_add_u32 m0, s70, 0x10000\n\t"
    "ds_read_b64_tr_b16 a[250:251], v229 offset:10240\n\t"
    "v_mfma_f32_16x16x32_bf16 v[4:7], a[224:227], v[100:103], v[4:7]\n\t"
    "buffer_load_dwordx4 v234, s[48:51], s71 offen offset:0 lds\n\t"
    "v_mfma_f32_16x16x32_bf16 v[8:11], a[224:227], v[104:107], v[8:11]\n\t"
    "ds_read_b64_tr_b16 a[252:253], v229 offset:12288\n\t"
    "v_mfma_f32_16x16x32_bf16 v[8:11], a[228:231], v[120:123], v[8:11]\n\t"
    "v_add_u32 v230, 0xffff0000, v230\n\t"
    "v_mfma_f32_16x16x32_bf16 v[8:11], a[232:235], v[136:139], v[8:11]\n\t"
    "s_waitcnt lgkmcnt(2)\n\t"
    "ds_read_b64_tr_b16 a[254:255], v229 offset:14336\n\t"
    "v_mfma_f32_16x16x32_bf16 v[8:11], a[236:239], v[152:155], v[8:11]\n\t"
    "buffer_store_dwordx4 v[248:251], v237, s[56:59], s83 offen offset:3072 sc1\n\t"
    "v_mfma_f32_16x16x32_bf16 v[12:15], a[236:239], v[156:159], v[12:15]\n\t"
    "ds_read_b64_tr_b16 a[236:237], v230 offset:0\n\t"
    "v_mfma_f32_16x16x32_bf16 v[12:15], a[232:235], v[140:143], v[12:15]\n\t"
    "v_mfma_f32_16x16x32_bf16 v[12:15], a[228:231], v[124:127], v[12:15]\n\t"
    "ds_read_b64_tr_b16 a[238:239], v230 offset:2048\n\t"
    "v_mfma_f32_16x16x32_bf16 v[12:15], a[224:227], v[108:111], v[12:15]\n\t"
    "s_waitcnt lgkmcnt(4)\n\t"
    "v_mfma_f32_16x16x32_bf16 v[16:19], a[240:243], v[96:99], v[16:19]\n\t"
    "ds_read_b64_tr_b16 a[232:233], v230 offset:4096\n\t"
    "v_mfma_f32_16x16x32_bf16 v[16:19], a[244:247], v[112:115], v[16:19]\n\t"
    "v_mfma_f32_16x16x32_bf16 v[16:19], a[248:251], v[128:131], v[16:19]\n\t"
    "s_waitcnt lgkmcnt(3)\n\t"
    "ds_read_b64_tr_b16 a[234:235], v230 offset:6144\n\t"
    "v_mfma_f32_16x16x32_bf16 v[16:19], a[252:255], v[144:147], v[16:19]\n\t"
    "v_mfma_f32_16x16x32_bf16 v[20:23], a[252:255], v[148:151], v[20:23]\n\t"
    "ds_read_b64_tr_b16 a[228:229], v230 offset:8192\n\t"
    "v_mfma_f32_16x16x32_bf16 v[20:23], a[248:251], v[132:135], v[20:23]\n\t"
    "v_mfma_f32_16x16x32_bf16 v[20:23], a[244:247], v[116:119], v[20:23]\n\t"
    "ds_read_b64_tr_b16 a[230:231], v230 offset:10240\n\t"
    "v_mfma_f32_16x16x32_bf16 v[20:23], a[240:243], v[100:103], v[20:23]\n\t"
    "buffer_load_dwordx4 v234, s[48:51], s71 offen offset:1024 lds\n\t"
    "v_mfma_f32_16x16x32_bf16 v[24:27], a[240:243], v[104:107], v[24:27]\n\t"
    "ds_read_b64_tr_b16 a[224:225], v230 offset:12288\n\t"
    "v_mfma_f32_16x16x32_bf16 v[24:27], a[244:247], v[120:123], v[24:27]\n\t"
    "v_mfma_f32_16x16x32_bf16 v[24:27], a[248:251], v[136:139], v[24:27]\n\t"
    "ds_read_b64_tr_b16 a[226:227], v230 offset:14336\n\t"
    "v_mfma_f32_16x16x32_bf16 v[24:27], a[252:255], v[152:155], v[24:27]\n\t"
    "v_add_u32 v231, 0xffff0000, v231\n\t"
    "v_mfma_f32_16x16x32_bf16 v[28:31], a[252:255], v[156:159], v[28:31]\n\t"
    "ds_read_b64_tr_b16 a[252:253], v231 offset:0\n\t"
    "v_mfma_f32_16x16x32_bf16 v[28:31], a[248:251], v[140:143], v[28:31]\n\t"
    "v_mfma_f32_16x16x32_bf16 v[28:31], a[244:247], v[124:127], v[28:31]\n\t"
    "ds_read_b64_tr_b16 a[254:255], v231 offset:2048\n\t"
    "v_mfma_f32_16x16x32_bf16 v[28:31], a[240:243], v[108:111], v[28:31]\n\t"
    "s_waitcnt lgkmcnt(4)\n\t"
    "v_mfma_f32_16x16x32_bf16 v[32:35], a[236:239], v[96:99], v[32:35]\n\t"
    "ds_read_b64_tr_b16 a[248:249], v231 offset:4096\n\t"
    "v_mfma_f32_16x16x32_bf16 v[32:35], a[232:235], v[112:115], v[32:35]\n\t"
    "v_mfma_f32_16x16x32_bf16 v[32:35], a[228:231], v[128:131], v[32:35]\n\t"
    "s_waitcnt lgkmcnt(3)\n\t"
    "ds_read_b64_tr_b16 a[250:251], v231 offset:6144\n\t"
    "v_mfma_f32_16x16x32_bf16 v[32:35], a[224:227], v[144:147], v[32:35]\n\t"
    "v_mfma_f32_16x16x32_bf16 v[36:39], a[224:227], v[148:151], v[36:39]\n\t"
    "ds_read_b64_tr_b16 a[244:245], v231 offset:8192\n\t"
    "ds_read_b128 v[244:247], v236 offset:1024\n\t"
    "v_mfma_f32_16x16x32_bf16 v[36:39], a[228:231], v[132:135], v[36:39]\n\t"
    "v_mfma_f32_16x16x32_bf16 v[36:39], a[232:235], v[116:119], v[36:39]\n\t"
    "ds_read_b64_tr_b16 a[246:247], v231 offset:10240\n\t"
    "v_mfma_f32_16x16x32_bf16 v[36:39], a[236:239], v[100:103], v[36:39]\n\t"
    "buffer_load_dwordx4 v234, s[48:51], s71 offen offset:2048 lds\n\t"
    "v_mfma_f32_16x16x32_bf16 v[40:43], a[236:239], v[104:107], v[40:43]\n\t"
    "ds_read_b64_tr_b16 a[240:241], v231 offset:12288\n\t"
    "v_mfma_f32_16x16x32_bf16 v[40:43], a[232:235], v[120:123], v[40:43]\n\t"
    "s_xor_b32 s83, s68, 0x4080\n\t"
    "v_mfma_f32_16x16x32_bf16 v[40:43], a[228:231], v[136:139], v[40:43]\n\t"
    "s_waitcnt lgkmcnt(2)\n\t"
    "ds_read_b64_tr_b16 a[242:243], v231 offset:14336\n\t"
    "v_mfma_f32_16x16x32_bf16 v[40:43], a[224:227], v[152:155], v[40:43]\n\t"
    "buffer_store_dwordx4 v[244:247], v237, s[56:59], s83 offen offset:0 sc1\n\t"
    "v_mfma_f32_16x16x32_bf16 v[44:47], a[224:227], v[156:159], v[44:47]\n\t"
    "v_mfma_f32_16x16x32_bf16 v[44:47], a[228:231], v[140:143], v[44:47]\n\t"
    "v_mfma_f32_16x16x32_bf16 v[44:47], a[232:235], v[124:127], v[44:47]\n\t"
    "v_mfma_f32_16x16x32_bf16 v[44:47], a[236:239], v[108:111], v[44:47]\n\t"
    "s_waitcnt lgkmcnt(2)\n\t"
    "v_mfma_f32_16x16x32_bf16 v[48:51], a[252:255], v[96:99], v[48:51]\n\t"
    "v_mfma_f32_16x16x32_bf16 v[48:51], a[248:251], v[112:115], v[48:51]\n\t"
    "s_waitcnt lgkmcnt(0)\n\t"
    "v_mfma_f32_16x16x32_bf16 v[48:51], a[244:247], v[128:131], v[48:51]\n\t"
    "s_waitcnt vmcnt(17)\n\t"
    "v_mfma_f32_16x16x32_bf16 v[48:51], a[240:243], v[144:147], v[48:51]\n\t"
    "s_barrier\n\t"
    "v_mfma_f32_16x16x32_bf16 v[52:55], a[240:243], v[148:151], v[52:55]\n\t"
    "ds_read_b64_tr_b16 a[224:225], v228 offset:16384\n\t"
    "ds_read_b64_tr_b16 a[226:227], v228 offset:18432\n\t"
    "v_mfma_f32_16x16x32_bf16 v[52:55], a[244:247], v[132:135], v[52:55]\n\t"
    "ds_read_b64_tr_b16 a[228:229], v228 offset:20480\n\t"
    "ds_read_b64_tr_b16 a[230:231], v228 offset:22528\n\t"
    "v_mfma_f32_16x16x32_bf16 v[52:55], a[248:251], v[116:119], v[52:55]\n\t"
    "ds_read_b64_tr_b16 a[232:233], v228 offset:24576\n\t"
    "v_mfma_f32_16x16x32_bf16 v[52:55], a[252:255], v[100:103], v[52:55]\n\t"
    "ds_read_b64_tr_b16 a[234:235], v228 offset:26624\n\t"
    "v_mfma_f32_16x16x32_bf16 v[56:59], a[252:255], v[104:107], v[56:59]\n\t"
    "ds_read_b64_tr_b16 a[236:237], v228 offset:28672\n\t"
    "v_mfma_f32_16x16x32_bf16 v[56:59], a[248:251], v[120:123], v[56:59]\n\t"
    "buffer_load_dwordx4 v234, s[48:51], s71 offen offset:3072 lds\n\t"
    "v_mfma_f32_16x16x32_bf16 v[56:59], a[244:247], v[136:139], v[56:59]\n\t"
    "ds_read_b64_tr_b16 a[238:239], v228 offset:30720\n\t"
    "v_mfma_f32_16x16x32_bf16 v[56:59], a[240:243], v[152:155], v[56:59]\n\t"
    "v_mfma_f32_16x16x32_bf16 v[60:63], a[240:243], v[156:159], v[60:63]\n\t"
    "ds_read_b64_tr_b16 a[240:241], v229 offset:16384\n\t"
    "v_mfma_f32_16x16x32_bf16 v[60:63], a[244:247], v[140:143], v[60:63]\n\t"
    "v_mfma_f32_16x16x32_bf16 v[60:63], a[248:251], v[124:127], v[60:63]\n\t"
    "ds_read_b64_tr_b16 a[242:243], v229 offset:18432\n\t"
    "v_mfma_f32_16x16x32_bf16 v[60:63], a[252:255], v[108:111], v[60:63]\n\t"
    "s_waitcnt lgkmcnt(4)\n\t"
    "v_mfma_f32_16x16x32_bf16 v[0:3], a[224:227], v[160:163], v[0:3]\n\t"
    "ds_read_b64_tr_b16 a[244:245], v229 offset:20480\n\t"
    "v_mfma_f32_16x16x32_bf16 v[0:3], a[228:231], v[176:179], v[0:3]\n\t"
    "v_mfma_f32_16x16x32_bf16 v[0:3], a[232:235], v[192:195], v[0:3]\n\t"
    "s_waitcnt lgkmcnt(3)\n\t"
    "ds_read_b64_tr_b16 a[246:247], v229 offset:22528\n\t"
    "v_mfma_f32_16x16x32_bf16 v[0:3], a[236:239], v[208:211], v[0:3]\n\t"
    "v_mfma_f32_16x16x32_bf16 v[4:7], a[236:239], v[212:215], v[4:7]\n\t"
    "ds_read_b64_tr_b16 a[248:249], v229 offset:24576\n\t"
    "ds_read_b128 v[248:251], v236 offset:1280\n\t"
    "v_mfma_f32_16x16x32_bf16 v[4:7], a[232:235], v[196:199], v[4:7]\n\t"
    "s_xor_b32 s71, s66, 0x30180\n\t"
    "s_add_u32 m0, s70, 0x14000\n\t"
    "v_mfma_f32_16x16x32_bf16 v[4:7], a[228:231], v[180:183], v[4:7]\n\t"
    "ds_read_b64_tr_b16 a[250:251], v229 offset:26624\n\t"
    "v_mfma_f32_16x16x32_bf16 v[4:7], a[224:227], v[164:167], v[4:7]\n\t"
    "buffer_load_dwordx4 v234, s[48:51], s71 offen offset:0 lds\n\t"
    "v_mfma_f32_16x16x32_bf16 v[8:11], a[224:227], v[168:171], v[8:11]\n\t"
    "ds_read_b64_tr_b16 a[252:253], v229 offset:28672\n\t"
    "v_mul_f32 v0, v252, v0\n\t"
    "v_mfma_f32_16x16x32_bf16 v[8:11], a[228:231], v[184:187], v[8:11]\n\t"
    "v_mul_f32 v1, v252, v1\n\t"
    "v_mul_f32 v2, v252, v2\n\t"
    "v_mfma_f32_16x16x32_bf16 v[8:11], a[232:235], v[200:203], v[8:11]\n\t"
    "ds_read_b64_tr_b16 a[254:255], v229 offset:30720\n\t"
    "v_mul_f32 v3, v252, v3\n\t"
    "v_mfma_f32_16x16x32_bf16 v[8:11], a[236:239], v[216:219], v[8:11]\n\t"
    "s_waitcnt lgkmcnt(3)\n\t"
    "v_cvt_pk_bf16_f32 v0, v0, v1\n\t"
    "v_cvt_pk_bf16_f32 v1, v2, v3\n\t"
    "v_mfma_f32_16x16x32_bf16 v[12:15], a[236:239], v[220:223], v[12:15]\n\t"
    "ds_read_b64_tr_b16 a[236:237], v230 offset:16384\n\t"
    "v_mul_f32 v4, v253, v4\n\t"
    "v_mfma_f32_16x16x32_bf16 v[12:15], a[232:235], v[204:207], v[12:15]\n\t"
    "buffer_store_dwordx4 v[248:251], v237, s[56:59], s83 offen offset:1024 sc1\n\t"
    "v_mul_f32 v5, v253, v5\n\t"
    "v_mfma_f32_16x16x32_bf16 v[12:15], a[228:231], v[188:191], v[12:15]\n\t"
    "s_waitcnt lgkmcnt(3)\n\t"
    "ds_read_b64_tr_b16 a[238:239], v230 offset:18432\n\t"
    "v_mfma_f32_16x16x32_bf16 v[12:15], a[224:227], v[172:175], v[12:15]\n\t"
    "ds_write_b64 v235, v[0:1] offset:0\n\t"
    "v_mfma_f32_16x16x32_bf16 v[16:19], a[240:243], v[160:163], v[16:19]\n\t"
    "ds_read_b64_tr_b16 a[232:233], v230 offset:20480\n\t"
    "v_mul_f32 v6, v253, v6\n\t"
    "v_mfma_f32_16x16x32_bf16 v[16:19], a[244:247], v[176:179], v[16:19]\n\t"
    "s_waitcnt lgkmcnt(4)\n\t"
    "v_mul_f32 v7, v253, v7\n\t"
    "v_cvt_pk_bf16_f32 v4, v4, v5\n\t"
    "v_mfma_f32_16x16x32_bf16 v[16:19], a[248:251], v[192:195], v[16:19]\n\t"
    "ds_read_b64_tr_b16 a[234:235], v230 offset:22528\n\t"
    "v_cvt_pk_bf16_f32 v5, v6, v7\n\t"
    "v_mfma_f32_16x16x32_bf16 v[16:19], a[252:255], v[208:211], v[16:19]\n\t"
    "v_mul_f32 v8, v254, v8\n\t"
    "v_mul_f32 v9, v254, v9\n\t"
    "v_mfma_f32_16x16x32_bf16 v[20:23], a[252:255], v[212:215], v[20:23]\n\t"
    "ds_read_b64_tr_b16 a[228:229], v230 offset:24576\n\t"
    "v_mfma_f32_16x16x32_bf16 v[20:23], a[248:251], v[196:199], v[20:23]\n\t"
    "ds_write_b64 v235, v[4:5] offset:512\n\t"
    "v_mfma_f32_16x16x32_bf16 v[20:23], a[244:247], v[180:183], v[20:23]\n\t"
    "ds_read_b64_tr_b16 a[230:231], v230 offset:26624\n\t"
    "v_mul_f32 v10, v254, v10\n\t"
    "v_mfma_f32_16x16x32_bf16 v[20:23], a[240:243], v[164:167], v[20:23]\n\t"
    "buffer_load_dwordx4 v234, s[48:51], s71 offen offset:1024 lds\n\t"
    "v_mfma_f32_16x16x32_bf16 v[24:27], a[240:243], v[168:171], v[24:27]\n\t"
    "ds_read_b64_tr_b16 a[224:225], v230 offset:28672\n\t"
    "v_mul_f32 v11, v254, v11\n\t"
    "v_mfma_f32_16x16x32_bf16 v[24:27], a[244:247], v[184:187], v[24:27]\n\t"
    "v_cvt_pk_bf16_f32 v8, v8, v9\n\t"
    "v_cvt_pk_bf16_f32 v9, v10, v11\n\t"
    "v_mfma_f32_16x16x32_bf16 v[24:27], a[248:251], v[200:203], v[24:27]\n\t"
    "ds_read_b64_tr_b16 a[226:227], v230 offset:30720\n\t"
    "v_mul_f32 v12, v255, v12\n\t"
    "v_mfma_f32_16x16x32_bf16 v[24:27], a[252:255], v[216:219], v[24:27]\n\t"
    "v_mul_f32 v13, v255, v13\n\t"
    "v_mfma_f32_16x16x32_bf16 v[28:31], a[252:255], v[220:223], v[28:31]\n\t"
    "ds_read_b64_tr_b16 a[252:253], v231 offset:16384\n\t"
    "ds_read_b128 v[244:247], v236 offset:1536\n\t"
    "v_mfma_f32_16x16x32_bf16 v[28:31], a[248:251], v[204:207], v[28:31]\n\t"
    "ds_write_b64 v235, v[8:9] offset:1024\n\t"
    "v_mfma_f32_16x16x32_bf16 v[28:31], a[244:247], v[188:191], v[28:31]\n\t"
    "ds_read_b64_tr_b16 a[254:255], v231 offset:18432\n\t"
    "v_mul_f32 v14, v255, v14\n\t"
    "v_mfma_f32_16x16x32_bf16 v[28:31], a[240:243], v[172:175], v[28:31]\n\t"
    "s_waitcnt lgkmcnt(6)\n\t"
    "v_mul_f32 v15, v255, v15\n\t"
    "v_cvt_pk_bf16_f32 v12, v12, v13\n\t"
    "v_mfma_f32_16x16x32_bf16 v[32:35], a[236:239], v[160:163], v[32:35]\n\t"
    "ds_read_b64_tr_b16 a[248:249], v231 offset:20480\n\t"
    "ds_read_b128 v[248:251], v236 offset:1792\n\t"
    "v_cvt_pk_bf16_f32 v13, v14, v15\n\t"
    "v_mfma_f32_16x16x32_bf16 v[32:35], a[232:235], v[176:179], v[32:35]\n\t"
    "ds_write_b64 v235, v[12:13] offset:1536\n\t"
    "v_mfma_f32_16x16x32_bf16 v[32:35], a[228:231], v[192:195], v[32:35]\n\t"
    "s_waitcnt lgkmcnt(7)\n\t"
    "ds_read_b64_tr_b16 a[250:251], v231 offset:22528\n\t"
    "v_mul_f32 v16, v252, v16\n\t"
    "v_mfma_f32_16x16x32_bf16 v[32:35], a[224:227], v[208:211], v[32:35]\n\t"
    "v_mul_f32 v17, v252, v17\n\t"
    "v_mul_f32 v18, v252, v18\n\t"
    "v_mfma_f32_16x16x32_bf16 v[36:39], a[224:227], v[212:215], v[36:39]\n\t"
    "ds_read_b64_tr_b16 a[244:245], v231 offset:24576\n\t"
    "v_mul_f32 v19, v252, v19\n\t"
    "v_mfma_f32_16x16x32_bf16 v[36:39], a[228:231], v[196:199], v[36:39]\n\t"
    "s_waitcnt lgkmcnt(7)\n\t"
    "v_cvt_pk_bf16_f32 v16, v16, v17\n\t"
    "v_cvt_pk_bf16_f32 v17, v18, v19\n\t"
    "v_mfma_f32_16x16x32_bf16 v[36:39], a[232:235], v[180:183], v[36:39]\n\t"
    "ds_read_b64_tr_b16 a[246:247], v231 offset:26624\n\t"
    "v_mul_f32 v20, v253, v20\n\t"
    "v_mfma_f32_16x16x32_bf16 v[36:39], a[236:239], v[164:167], v[36:39]\n\t"
    "buffer_load_dwordx4 v234, s[48:51], s71 offen offset:2048 lds\n\t"
    "v_mfma_f32_16x16x32_bf16 v[40:43], a[236:239], v[168:171], v[40:43]\n\t"
    "ds_read_b64_tr_b16 a[240:241], v231 offset:28672\n\t"
    "v_mul_f32 v21, v253, v21\n\t"
    "v_mfma_f32_16x16x32_bf16 v[40:43], a[232:235], v[184:187], v[40:43]\n\t"
    "buffer_store_dwordx4 v[244:247], v237, s[56:59], s83 offen offset:2048 sc1\n\t"
    "v_mfma_f32_16x16x32_bf16 v[40:43], a[228:231], v[200:203], v[40:43]\n\t"
    "ds_read_b64_tr_b16 a[242:243], v231 offset:30720\n\t"
    "v_mfma_f32_16x16x32_bf16 v[40:43], a[224:227], v[216:219], v[40:43]\n\t"
    "ds_write_b64 v235, v[16:17] offset:128\n\t"
    "v_mfma_f32_16x16x32_bf16 v[44:47], a[224:227], v[220:223], v[44:47]\n\t"
    "v_mul_f32 v22, v253, v22\n\t"
    "v_mul_f32 v23, v253, v23\n\t"
    "v_mfma_f32_16x16x32_bf16 v[44:47], a[228:231], v[204:207], v[44:47]\n\t"
    "v_cvt_pk_bf16_f32 v20, v20, v21\n\t"
    "v_cvt_pk_bf16_f32 v21, v22, v23\n\t"
    "v_mfma_f32_16x16x32_bf16 v[44:47], a[232:235], v[188:191], v[44:47]\n\t"
    "s_waitcnt lgkmcnt(3)\n\t"
    "v_mul_f32 v24, v254, v24\n\t"
    "v_mul_f32 v25, v254, v25\n\t"
    "v_mfma_f32_16x16x32_bf16 v[44:47], a[236:239], v[172:175], v[44:47]\n\t"
    "ds_write_b64 v235, v[20:21] offset:640\n\t"
    "v_mfma_f32_16x16x32_bf16 v[48:51], a[252:255], v[160:163], v[48:51]\n\t"
    "v_mul_f32 v26, v254, v26\n\t"
    "v_mul_f32 v27, v254, v27\n\t"
    "v_mfma_f32_16x16x32_bf16 v[48:51], a[248:251], v[176:179], v[48:51]\n\t"
    "s_waitcnt lgkmcnt(2)\n\t"
    "v_cvt_pk_bf16_f32 v24, v24, v25\n\t"
    "v_cvt_pk_bf16_f32 v25, v26, v27\n\t"
    "v_mfma_f32_16x16x32_bf16 v[48:51], a[244:247], v[192:195], v[48:51]\n\t"
    "s_waitcnt vmcnt(17)\n\t"
    "v_mul_f32 v28, v255, v28\n\t"
    "v_mul_f32 v29, v255, v29\n\t"
    "v_mfma_f32_16x16x32_bf16 v[48:51], a[240:243], v[208:211], v[48:51]\n\t"
    "s_barrier\n\t"
    "v_mfma_f32_16x16x32_bf16 v[52:55], a[240:243], v[212:215], v[52:55]\n\t"
    "ds_read_b64_tr_b16 a[224:225], v228 offset:32768\n\t"
    "ds_read_b64_tr_b16 a[226:227], v228 offset:34816\n\t"
    "v_mfma_f32_16x16x32_bf16 v[52:55], a[244:247], v[196:199], v[52:55]\n\t"
    "ds_read_b64_tr_b16 a[228:229], v228 offset:36864\n\t"
    "ds_read_b64_tr_b16 a[230:231], v228 offset:38912\n\t"
    "v_mfma_f32_16x16x32_bf16 v[52:55], a[248:251], v[180:183], v[52:55]\n\t"
    "ds_read_b64_tr_b16 a[232:233], v228 offset:40960\n\t"
    "ds_write_b64 v235, v[24:25] offset:1152\n\t"
    "v_mfma_f32_16x16x32_bf16 v[52:55], a[252:255], v[164:167], v[52:55]\n\t"
    "ds_read_b64_tr_b16 a[234:235], v228 offset:43008\n\t"
    "v_mul_f32 v30, v255, v30\n\t"
    "v_mfma_f32_16x16x32_bf16 v[56:59], a[252:255], v[168:171], v[56:59]\n\t"
    "ds_read_b64_tr_b16 a[236:237], v228 offset:45056\n\t"
    "v_mul_f32 v31, v255, v31\n\t"
    "v_mfma_f32_16x16x32_bf16 v[56:59], a[248:251], v[184:187], v[56:59]\n\t"
    "buffer_load_dwordx4 v234, s[48:51], s71 offen offset:3072 lds\n\t"
    "v_mfma_f32_16x16x32_bf16 v[56:59], a[244:247], v[200:203], v[56:59]\n\t"
    "ds_read_b64_tr_b16 a[238:239], v228 offset:47104\n\t"
    "v_cvt_pk_bf16_f32 v28, v28, v29\n\t"
    "v_mfma_f32_16x16x32_bf16 v[56:59], a[240:243], v[216:219], v[56:59]\n\t"
    "v_cvt_pk_bf16_f32 v29, v30, v31\n\t"
    "v_mfma_f32_16x16x32_bf16 v[60:63], a[240:243], v[220:223], v[60:63]\n\t"
    "s_xor_b32 s71, s65, 0x0\n\t"
    "ds_read_b64_tr_b16 a[240:241], v229 offset:32768\n\t"
    "v_mfma_f32_16x16x32_bf16 v[60:63], a[244:247], v[204:207], v[60:63]\n\t"
    "ds_write_b64 v235, v[28:29] offset:1664\n\t"
    "v_mul_f32 v32, v252, v32\n\t"
    "v_mfma_f32_16x16x32_bf16 v[60:63], a[248:251], v[188:191], v[60:63]\n\t"
    "ds_read_b64_tr_b16 a[242:243], v229 offset:34816\n\t"
    "v_mul_f32 v33, v252, v33\n\t"
    "v_mfma_f32_16x16x32_bf16 v[60:63], a[252:255], v[172:175], v[60:63]\n\t"
    "s_waitcnt lgkmcnt(5)\n\t"
    "v_mul_f32 v34, v252, v34\n\t"
    "v_mul_f32 v35, v252, v35\n\t"
    "v_mfma_f32_16x16x32_bf16 v[0:3], a[224:227], v[64:67], 0\n\t"
    "ds_read_b64_tr_b16 a[244:245], v229 offset:36864\n\t"
    "v_cvt_pk_bf16_f32 v32, v32, v33\n\t"
    "v_mfma_f32_16x16x32_bf16 v[0:3], a[228:231], v[80:83], v[0:3]\n\t"
    "s_waitcnt lgkmcnt(4)\n\t"
    "v_cvt_pk_bf16_f32 v33, v34, v35\n\t"
    "v_mul_f32 v36, v253, v36\n\t"
    "v_mfma_f32_16x16x32_bf16 v[0:3], a[232:235], a[128:131], v[0:3]\n\t"
    "ds_read_b64_tr_b16 a[246:247], v229 offset:38912\n\t"
    "v_mul_f32 v37, v253, v37\n\t"
    "v_mfma_f32_16x16x32_bf16 v[0:3], a[236:239], a[144:147], v[0:3]\n\t"
    "ds_write_b64 v235, v[32:33] offset:16384\n\t"
    "v_mfma_f32_16x16x32_bf16 v[4:7], a[236:239], a[148:151], 0\n\t"
    "ds_read_b64_tr_b16 a[248:249], v229 offset:40960\n\t"
    "v_mul_f32 v38, v253, v38\n\t"
    "v_mfma_f32_16x16x32_bf16 v[4:7], a[232:235], a[132:135], v[4:7]\n\t"
    "s_add_u32 m0, s70, 0x0\n\t"
    "v_mul_f32 v39, v253, v39\n\t"
    "v_cvt_pk_bf16_f32 v36, v36, v37\n\t"
    "v_mfma_f32_16x16x32_bf16 v[4:7], a[228:231], v[84:87], v[4:7]\n\t"
    "ds_read_b64_tr_b16 a[250:251], v229 offset:43008\n\t"
    "v_cvt_pk_bf16_f32 v37, v38, v39\n\t"
    "v_mfma_f32_16x16x32_bf16 v[4:7], a[224:227], v[68:71], v[4:7]\n\t"
    "buffer_load_dwordx4 v232, s[44:47], s71 offen offset:0 nt lds\n\t"
    "v_mfma_f32_16x16x32_bf16 v[8:11], a[224:227], v[72:75], 0\n\t"
    "ds_read_b64_tr_b16 a[252:253], v229 offset:45056\n\t"
    "v_mul_f32 v40, v254, v40\n\t"
    "v_mfma_f32_16x16x32_bf16 v[8:11], a[228:231], v[88:91], v[8:11]\n\t"
    "v_mul_f32 v41, v254, v41\n\t"
    "v_mfma_f32_16x16x32_bf16 v[8:11], a[232:235], a[136:139], v[8:11]\n\t"
    "ds_read_b64_tr_b16 a[254:255], v229 offset:47104\n\t"
    "buffer_store_dwordx4 v[248:251], v237, s[56:59], s83 offen offset:3072 sc1\n\t"
    "v_mfma_f32_16x16x32_bf16 v[8:11], a[236:239], a[152:155], v[8:11]\n\t"
    "ds_write_b64 v235, v[36:37] offset:16896\n\t"
    "v_mfma_f32_16x16x32_bf16 v[12:15], a[236:239], a[156:159], 0\n\t"
    "ds_read_b64_tr_b16 a[236:237], v230 offset:32768\n\t"
    "v_mul_f32 v42, v254, v42\n\t"
    "v_mfma_f32_16x16x32_bf16 v[12:15], a[232:235], a[140:143], v[12:15]\n\t"
    "v_mul_f32 v43, v254, v43\n\t"
    "v_cvt_pk_bf16_f32 v40, v40, v41\n\t"
    "v_mfma_f32_16x16x32_bf16 v[12:15], a[228:231], v[92:95], v[12:15]\n\t"
    "ds_read_b64_tr_b16 a[238:239], v230 offset:34816\n\t"
    "v_cvt_pk_bf16_f32 v41, v42, v43\n\t"
    "v_mfma_f32_16x16x32_bf16 v[12:15], a[224:227], v[76:79], v[12:15]\n\t"
    "s_waitcnt lgkmcnt(5)\n\t"
    "v_mul_f32 v44, v255, v44\n\t"
    "v_mul_f32 v45, v255, v45\n\t"
    "v_mfma_f32_16x16x32_bf16 v[16:19], a[240:243], v[64:67], 0\n\t"
    "ds_read_b64_tr_b16 a[232:233], v230 offset:36864\n\t"
    "v_mfma_f32_16x16x32_bf16 v[16:19], a[244:247], v[80:83], v[16:19]\n\t"
    "ds_write_b64 v235, v[40:41] offset:17408\n\t"
    "v_mfma_f32_16x16x32_bf16 v[16:19], a[248:251], a[128:131], v[16:19]\n\t"
    "s_waitcnt lgkmcnt(5)\n\t"
    "ds_read_b64_tr_b16 a[234:235], v230 offset:38912\n\t"
    "v_mul_f32 v46, v255, v46\n\t"
    "v_mfma_f32_16x16x32_bf16 v[16:19], a[252:255], a[144:147], v[16:19]\n\t"
    "v_mul_f32 v47, v255, v47\n\t"
    "v_cvt_pk_bf16_f32 v44, v44, v45\n\t"
    "v_mfma_f32_16x16x32_bf16 v[20:23], a[252:255], a[148:151], 0\n\t"
    "ds_read_b64_tr_b16 a[228:229], v230 offset:40960\n\t"
    "v_cvt_pk_bf16_f32 v45, v46, v47\n\t"
    "v_mfma_f32_16x16x32_bf16 v[20:23], a[248:251], a[132:135], v[20:23]\n\t"
    "ds_write_b64 v235, v[44:45] offset:17920\n\t"
    "v_mfma_f32_16x16x32_bf16 v[20:23], a[244:247], v[84:87], v[20:23]\n\t"
    "ds_read_b64_tr_b16 a[230:231], v230 offset:43008\n\t"
    "v_mul_f32 v48, v252, v48\n\t"
    "v_mfma_f32_16x16x32_bf16 v[20:23], a[240:243], v[68:71], v[20:23]\n\t"
    "buffer_load_dwordx4 v233, s[44:47], s71 offen offset:1024 nt lds\n\t"
    "v_mfma_f32_16x16x32_bf16 v[24:27], a[240:243], v[72:75], 0\n\t"
    "ds_read_b64_tr_b16 a[224:225], v230 offset:45056\n\t"
    "v_mul_f32 v49, v252, v49\n\t"
    "v_mfma_f32_16x16x32_bf16 v[24:27], a[244:247], v[88:91], v[24:27]\n\t"
    "v_mul_f32 v50, v252, v50\n\t"
    "v_mul_f32 v51, v252, v51\n\t"
    "v_mfma_f32_16x16x32_bf16 v[24:27], a[248:251], a[136:139], v[24:27]\n\t"
    "ds_read_b64_tr_b16 a[226:227], v230 offset:47104\n\t"
    "v_cvt_pk_bf16_f32 v48, v48, v49\n\t"
    "v_mfma_f32_16x16x32_bf16 v[24:27], a[252:255], a[152:155], v[24:27]\n\t"
    "v_cvt_pk_bf16_f32 v49, v50, v51\n\t"
    "v_mul_f32 v52, v253, v52\n\t"
    "v_mfma_f32_16x16x32_bf16 v[28:31], a[252:255], a[156:159], 0\n\t"
    "ds_read_b64_tr_b16 a[252:253], v231 offset:32768\n\t"
    "v_mul_f32 v53, v253, v53\n\t"
    "v_mfma_f32_16x16x32_bf16 v[28:31], a[248:251], a[140:143], v[28:31]\n\t"
    "ds_write_b64 v235, v[48:49] offset:16512\n\t"
    "v_mfma_f32_16x16x32_bf16 v[28:31], a[244:247], v[92:95], v[28:31]\n\t"
    "ds_read_b64_tr_b16 a[254:255], v231 offset:34816\n\t"
    "v_mul_f32 v54, v253, v54\n\t"
    "v_mfma_f32_16x16x32_bf16 v[28:31], a[240:243], v[76:79], v[28:31]\n\t"
    "s_waitcnt lgkmcnt(5)\n\t"
    "v_mul_f32 v55, v253, v55\n\t"
    "v_cvt_pk_bf16_f32 v52, v52, v53\n\t"
    "v_mfma_f32_16x16x32_bf16 v[32:35], a[236:239], v[64:67], 0\n\t"
    "ds_read_b64_tr_b16 a[248:249], v231 offset:36864\n\t"
    "v_cvt_pk_bf16_f32 v53, v54, v55\n\t"
    "v_mfma_f32_16x16x32_bf16 v[32:35], a[232:235], v[80:83], v[32:35]\n\t"
    "s_waitcnt lgkmcnt(4)\n\t"
    "v_mul_f32 v56, v254, v56\n\t"
    "v_mul_f32 v57, v254, v57\n\t"
    "v_mfma_f32_16x16x32_bf16 v[32:35], a[228:231], a[128:131], v[32:35]\n\t"
    "ds_read_b64_tr_b16 a[250:251], v231 offset:38912\n\t"
    "ds_read_b128 v[244:247], v236 offset:0\n\t"
    "v_mfma_f32_16x16x32_bf16 v[32:35], a[224:227], a[144:147], v[32:35]\n\t"
    "ds_write_b64 v235, v[52:53] offset:17024\n\t"
    "v_mfma_f32_16x16x32_bf16 v[36:39], a[224:227], a[148:151], 0\n\t"
    "ds_read_b64_tr_b16 a[244:245], v231 offset:40960\n\t"
    "v_mul_f32 v58, v254, v58\n\t"
    "v_mfma_f32_16x16x32_bf16 v[36:39], a[228:231], a[132:135], v[36:39]\n\t"
    "v_mul_f32 v59, v254, v59\n\t"
    "v_cvt_pk_bf16_f32 v56, v56, v57\n\t"
    "v_mfma_f32_16x16x32_bf16 v[36:39], a[232:235], v[84:87], v[36:39]\n\t"
    "ds_read_b64_tr_b16 a[246:247], v231 offset:43008\n\t"
    "v_cvt_pk_bf16_f32 v57, v58, v59\n\t"
    "v_mfma_f32_16x16x32_bf16 v[36:39], a[236:239], v[68:71], v[36:39]\n\t"
    "buffer_load_dwordx4 v232, s[44:47], s71 offen offset:2048 nt lds\n\t"
    "v_mfma_f32_16x16x32_bf16 v[40:43], a[236:239], v[72:75], 0\n\t"
    "ds_read_b64_tr_b16 a[240:241], v231 offset:45056\n\t"
    "v_mul_f32 v60, v255, v60\n\t"
    "v_mfma_f32_16x16x32_bf16 v[40:43], a[232:235], v[88:91], v[40:43]\n\t"
    "v_mul_f32 v61, v255, v61\n\t"
    "v_mfma_f32_16x16x32_bf16 v[40:43], a[228:231], a[136:139], v[40:43]\n\t"
    "ds_read_b64_tr_b16 a[242:243], v231 offset:47104\n\t"
    "v_mfma_f32_16x16x32_bf16 v[40:43], a[224:227], a[152:155], v[40:43]\n\t"
    "ds_write_b64 v235, v[56:57] offset:17536\n\t"
    "v_mfma_f32_16x16x32_bf16 v[44:47], a[224:227], a[156:159], 0\n\t"
    "s_xor_b32 s83, s68, 0x100\n\t"
    "v_mul_f32 v62, v255, v62\n\t"
    "v_mul_f32 v63, v255, v63\n\t"
    "v_mfma_f32_16x16x32_bf16 v[44:47], a[228:231], a[140:143], v[44:47]\n\t"
    "s_waitcnt lgkmcnt(6)\n\t"
    "v_cvt_pk_bf16_f32 v60, v60, v61\n\t"
    "v_cvt_pk_bf16_f32 v61, v62, v63\n\t"
    "v_mfma_f32_16x16x32_bf16 v[44:47], a[232:235], v[92:95], v[44:47]\n\t"
    "ds_write_b64 v235, v[60:61] offset:18048\n\t"
    "v_mfma_f32_16x16x32_bf16 v[44:47], a[236:239], v[76:79], v[44:47]\n\t"
    "s_waitcnt lgkmcnt(4)\n\t"
    "buffer_store_dwordx4 v[244:247], v237, s[56:59], s83 offen offset:0 sc1\n\t"
    "v_mfma_f32_16x16x32_bf16 v[48:51], a[252:255], v[64:67], 0\n\t"
    "v_mfma_f32_16x16x32_bf16 v[48:51], a[248:251], v[80:83], v[48:51]\n\t"
    "s_waitcnt lgkmcnt(2)\n\t"
    "v_mfma_f32_16x16x32_bf16 v[48:51], a[244:247], a[128:131], v[48:51]\n\t"
    "s_waitcnt vmcnt(17)\n\t"
    "v_mfma_f32_16x16x32_bf16 v[48:51], a[240:243], a[144:147], v[48:51]\n\t"
    "s_barrier\n\t"
    "v_mfma_f32_16x16x32_bf16 v[52:55], a[240:243], a[148:151], 0\n\t"
    "ds_read_b64_tr_b16 a[224:225], v228 offset:49152\n\t"
    "ds_read_b64_tr_b16 a[226:227], v228 offset:51200\n\t"
    "v_mfma_f32_16x16x32_bf16 v[52:55], a[244:247], a[132:135], v[52:55]\n\t"
    "ds_read_b64_tr_b16 a[228:229], v228 offset:53248\n\t"
    "ds_read_b64_tr_b16 a[230:231], v228 offset:55296\n\t"
    "v_mfma_f32_16x16x32_bf16 v[52:55], a[248:251], v[84:87], v[52:55]\n\t"
    "ds_read_b64_tr_b16 a[232:233], v228 offset:57344\n\t"
    "v_mfma_f32_16x16x32_bf16 v[52:55], a[252:255], v[68:71], v[52:55]\n\t"
    "ds_read_b64_tr_b16 a[234:235], v228 offset:59392\n\t"
    "v_mfma_f32_16x16x32_bf16 v[56:59], a[252:255], v[72:75], 0\n\t"
    "ds_read_b64_tr_b16 a[236:237], v228 offset:61440\n\t"
    "v_mfma_f32_16x16x32_bf16 v[56:59], a[248:251], v[88:91], v[56:59]\n\t"
    "buffer_load_dwordx4 v233, s[44:47], s71 offen offset:3072 nt lds\n\t"
    "v_mfma_f32_16x16x32_bf16 v[56:59], a[244:247], a[136:139], v[56:59]\n\t"
    "ds_read_b64_tr_b16 a[238:239], v228 offset:63488\n\t"
    "v_mfma_f32_16x16x32_bf16 v[56:59], a[240:243], a[152:155], v[56:59]\n\t"
    "v_mfma_f32_16x16x32_bf16 v[60:63], a[240:243], a[156:159], 0\n\t"
    "ds_read_b64_tr_b16 a[240:241], v229 offset:49152\n\t"
    "v_mfma_f32_16x16x32_bf16 v[60:63], a[244:247], a[140:143], v[60:63]\n\t"
    "v_mfma_f32_16x16x32_bf16 v[60:63], a[248:251], v[92:95], v[60:63]\n\t"
    "ds_read_b64_tr_b16 a[242:243], v229 offset:51200\n\t"
    "v_mfma_f32_16x16x32_bf16 v[60:63], a[252:255], v[76:79], v[60:63]\n\t"
    "s_waitcnt lgkmcnt(4)\n\t"
    "v_mfma_f32_16x16x32_bf16 v[0:3], a[224:227], a[160:163], v[0:3]\n\t"
    "ds_read_b64_tr_b16 a[244:245], v229 offset:53248\n\t"
    "v_mfma_f32_16x16x32_bf16 v[0:3], a[228:231], a[176:179], v[0:3]\n\t"
    "v_mfma_f32_16x16x32_bf16 v[0:3], a[232:235], a[192:195], v[0:3]\n\t"
    "s_waitcnt lgkmcnt(3)\n\t"
    "ds_read_b64_tr_b16 a[246:247], v229 offset:55296\n\t"
    "v_mfma_f32_16x16x32_bf16 v[0:3], a[236:239], a[208:211], v[0:3]\n\t"
    "v_mfma_f32_16x16x32_bf16 v[4:7], a[236:239], a[212:215], v[4:7]\n\t"
    "ds_read_b64_tr_b16 a[248:249], v229 offset:57344\n\t"
    "ds_read_b128 v[248:251], v236 offset:256\n\t"
    "v_mfma_f32_16x16x32_bf16 v[4:7], a[232:235], a[196:199], v[4:7]\n\t"
    "s_xor_b32 s71, s65, 0x4000\n\t"
    "s_add_u32 m0, s70, 0x4000\n\t"
    "v_mfma_f32_16x16x32_bf16 v[4:7], a[228:231], a[180:183], v[4:7]\n\t"
    "ds_read_b64_tr_b16 a[250:251], v229 offset:59392\n\t"
    "v_mfma_f32_16x16x32_bf16 v[4:7], a[224:227], a[164:167], v[4:7]\n\t"
    "buffer_load_dwordx4 v232, s[44:47], s71 offen offset:0 nt lds\n\t"
    "v_mfma_f32_16x16x32_bf16 v[8:11], a[224:227], a[168:171], v[8:11]\n\t"
    "ds_read_b64_tr_b16 a[252:253], v229 offset:61440\n\t"
    "v_mfma_f32_16x16x32_bf16 v[8:11], a[228:231], a[184:187], v[8:11]\n\t"
    "v_mfma_f32_16x16x32_bf16 v[8:11], a[232:235], a[200:203], v[8:11]\n\t"
    "s_waitcnt lgkmcnt(2)\n\t"
    "ds_read_b64_tr_b16 a[254:255], v229 offset:63488\n\t"
    "v_mfma_f32_16x16x32_bf16 v[8:11], a[236:239], a[216:219], v[8:11]\n\t"
    "buffer_store_dwordx4 v[248:251], v237, s[56:59], s83 offen offset:1024 sc1\n\t"
    "v_mfma_f32_16x16x32_bf16 v[12:15], a[236:239], a[220:223], v[12:15]\n\t"
    "ds_read_b64_tr_b16 a[236:237], v230 offset:49152\n\t"
    "v_mfma_f32_16x16x32_bf16 v[12:15], a[232:235], a[204:207], v[12:15]\n\t"
    "v_mfma_f32_16x16x32_bf16 v[12:15], a[228:231], a[188:191], v[12:15]\n\t"
    "ds_read_b64_tr_b16 a[238:239], v230 offset:51200\n\t"
    "v_mfma_f32_16x16x32_bf16 v[12:15], a[224:227], a[172:175], v[12:15]\n\t"
    "s_waitcnt lgkmcnt(4)\n\t"
    "v_mfma_f32_16x16x32_bf16 v[16:19], a[240:243], a[160:163], v[16:19]\n\t"
    "ds_read_b64_tr_b16 a[232:233], v230 offset:53248\n\t"
    "v_mfma_f32_16x16x32_bf16 v[16:19], a[244:247], a[176:179], v[16:19]\n\t"
    "v_mfma_f32_16x16x32_bf16 v[16:19], a[248:251], a[192:195], v[16:19]\n\t"
    "s_waitcnt lgkmcnt(3)\n\t"
    "ds_read_b64_tr_b16 a[234:235], v230 offset:55296\n\t"
    "v_mfma_f32_16x16x32_bf16 v[16:19], a[252:255], a[208:211], v[16:19]\n\t"
    "v_mfma_f32_16x16x32_bf16 v[20:23], a[252:255], a[212:215], v[20:23]\n\t"
    "ds_read_b64_tr_b16 a[228:229], v230 offset:57344\n\t"
    "v_mfma_f32_16x16x32_bf16 v[20:23], a[248:251], a[196:199], v[20:23]\n\t"
    "v_mfma_f32_16x16x32_bf16 v[20:23], a[244:247], a[180:183], v[20:23]\n\t"
    "ds_read_b64_tr_b16 a[230:231], v230 offset:59392\n\t"
    "v_mfma_f32_16x16x32_bf16 v[20:23], a[240:243], a[164:167], v[20:23]\n\t"
    "buffer_load_dwordx4 v233, s[44:47], s71 offen offset:1024 nt lds\n\t"
    "v_mfma_f32_16x16x32_bf16 v[24:27], a[240:243], a[168:171], v[24:27]\n\t"
    "ds_read_b64_tr_b16 a[224:225], v230 offset:61440\n\t"
    "v_mfma_f32_16x16x32_bf16 v[24:27], a[244:247], a[184:187], v[24:27]\n\t"
    "v_mfma_f32_16x16x32_bf16 v[24:27], a[248:251], a[200:203], v[24:27]\n\t"
    "ds_read_b64_tr_b16 a[226:227], v230 offset:63488\n\t"
    "v_mfma_f32_16x16x32_bf16 v[24:27], a[252:255], a[216:219], v[24:27]\n\t"
    "v_mfma_f32_16x16x32_bf16 v[28:31], a[252:255], a[220:223], v[28:31]\n\t"
    "ds_read_b64_tr_b16 a[252:253], v231 offset:49152\n\t"
    "v_mfma_f32_16x16x32_bf16 v[28:31], a[248:251], a[204:207], v[28:31]\n\t"
    "v_mfma_f32_16x16x32_bf16 v[28:31], a[244:247], a[188:191], v[28:31]\n\t"
    "ds_read_b64_tr_b16 a[254:255], v231 offset:51200\n\t"
    "v_mfma_f32_16x16x32_bf16 v[28:31], a[240:243], a[172:175], v[28:31]\n\t"
    "s_waitcnt lgkmcnt(4)\n\t"
    "v_mfma_f32_16x16x32_bf16 v[32:35], a[236:239], a[160:163], v[32:35]\n\t"
    "ds_read_b64_tr_b16 a[248:249], v231 offset:53248\n\t"
    "v_mfma_f32_16x16x32_bf16 v[32:35], a[232:235], a[176:179], v[32:35]\n\t"
    "v_mfma_f32_16x16x32_bf16 v[32:35], a[228:231], a[192:195], v[32:35]\n\t"
    "s_waitcnt lgkmcnt(3)\n\t"
    "ds_read_b64_tr_b16 a[250:251], v231 offset:55296\n\t"
    "v_mfma_f32_16x16x32_bf16 v[32:35], a[224:227], a[208:211], v[32:35]\n\t"
    "v_mfma_f32_16x16x32_bf16 v[36:39], a[224:227], a[212:215], v[36:39]\n\t"
    "ds_read_b64_tr_b16 a[244:245], v231 offset:57344\n\t"
    "ds_read_b128 v[244:247], v236 offset:512\n\t"
    "v_mfma_f32_16x16x32_bf16 v[36:39], a[228:231], a[196:199], v[36:39]\n\t"
    "v_mfma_f32_16x16x32_bf16 v[36:39], a[232:235], a[180:183], v[36:39]\n\t"
    "ds_read_b64_tr_b16 a[246:247], v231 offset:59392\n\t"
    "v_mfma_f32_16x16x32_bf16 v[36:39], a[236:239], a[164:167], v[36:39]\n\t"
    "buffer_load_dwordx4 v232, s[44:47], s71 offen offset:2048 nt lds\n\t"
    "v_mfma_f32_16x16x32_bf16 v[40:43], a[236:239], a[168:171], v[40:43]\n\t"
    "ds_read_b64_tr_b16 a[240:241], v231 offset:61440\n\t"
    "v_mfma_f32_16x16x32_bf16 v[40:43], a[232:235], a[184:187], v[40:43]\n\t"
    "v_mfma_f32_16x16x32_bf16 v[40:43], a[228:231], a[200:203], v[40:43]\n\t"
    "s_waitcnt lgkmcnt(2)\n\t"
    "ds_read_b64_tr_b16 a[242:243], v231 offset:63488\n\t"
    "v_mfma_f32_16x16x32_bf16 v[40:43], a[224:227], a[216:219], v[40:43]\n\t"
    "buffer_store_dwordx4 v[244:247], v237, s[56:59], s83 offen offset:2048 sc1\n\t"
    "v_mfma_f32_16x16x32_bf16 v[44:47], a[224:227], a[220:223], v[44:47]\n\t"
    "v_mfma_f32_16x16x32_bf16 v[44:47], a[228:231], a[204:207], v[44:47]\n\t"
    "v_mfma_f32_16x16x32_bf16 v[44:47], a[232:235], a[188:191], v[44:47]\n\t"
    "v_mfma_f32_16x16x32_bf16 v[44:47], a[236:239], a[172:175], v[44:47]\n\t"
    "s_waitcnt lgkmcnt(2)\n\t"
    "v_mfma_f32_16x16x32_bf16 v[48:51], a[252:255], a[160:163], v[48:51]\n\t"
    "v_mfma_f32_16x16x32_bf16 v[48:51], a[248:251], a[176:179], v[48:51]\n\t"
    "s_waitcnt lgkmcnt(0)\n\t"
    "v_mfma_f32_16x16x32_bf16 v[48:51], a[244:247], a[192:195], v[48:51]\n\t"
    "s_waitcnt vmcnt(17)\n\t"
    "v_add_u32 v228, 0x10000, v228\n\t"
    "v_mfma_f32_16x16x32_bf16 v[48:51], a[240:243], a[208:211], v[48:51]\n\t"
    "s_barrier\n\t"
    "v_mfma_f32_16x16x32_bf16 v[52:55], a[240:243], a[212:215], v[52:55]\n\t"
    "ds_read_b64_tr_b16 a[224:225], v228 offset:0\n\t"
    "ds_read_b64_tr_b16 a[226:227], v228 offset:2048\n\t"
    "v_mfma_f32_16x16x32_bf16 v[52:55], a[244:247], a[196:199], v[52:55]\n\t"
    "ds_read_b64_tr_b16 a[228:229], v228 offset:4096\n\t"
    "ds_read_b64_tr_b16 a[230:231], v228 offset:6144\n\t"
    "v_mfma_f32_16x16x32_bf16 v[52:55], a[248:251], a[180:183], v[52:55]\n\t"
    "ds_read_b64_tr_b16 a[232:233], v228 offset:8192\n\t"
    "v_mfma_f32_16x16x32_bf16 v[52:55], a[252:255], a[164:167], v[52:55]\n\t"
    "ds_read_b64_tr_b16 a[234:235], v228 offset:10240\n\t"
    "v_mfma_f32_16x16x32_bf16 v[56:59], a[252:255], a[168:171], v[56:59]\n\t"
    "ds_read_b64_tr_b16 a[236:237], v228 offset:12288\n\t"
    "v_mfma_f32_16x16x32_bf16 v[56:59], a[248:251], a[184:187], v[56:59]\n\t"
    "buffer_load_dwordx4 v233, s[44:47], s71 offen offset:3072 nt lds\n\t"
    "v_mfma_f32_16x16x32_bf16 v[56:59], a[244:247], a[200:203], v[56:59]\n\t"
    "ds_read_b64_tr_b16 a[238:239], v228 offset:14336\n\t"
    "v_mfma_f32_16x16x32_bf16 v[56:59], a[240:243], a[216:219], v[56:59]\n\t"
    "v_add_u32 v229, 0x10000, v229\n\t"
    "v_mfma_f32_16x16x32_bf16 v[60:63], a[240:243], a[220:223], v[60:63]\n\t"
    "ds_read_b64_tr_b16 a[240:241], v229 offset:0\n\t"
    "v_mfma_f32_16x16x32_bf16 v[60:63], a[244:247], a[204:207], v[60:63]\n\t"
    "v_mfma_f32_16x16x32_bf16 v[60:63], a[248:251], a[188:191], v[60:63]\n\t"
    "ds_read_b64_tr_b16 a[242:243], v229 offset:2048\n\t"
    "v_mfma_f32_16x16x32_bf16 v[60:63], a[252:255], a[172:175], v[60:63]\n\t"
    "s_waitcnt lgkmcnt(4)\n\t"
    "v_mfma_f32_16x16x32_bf16 v[0:3], a[224:227], v[96:99], v[0:3]\n\t"
    "s_add_u32 s66, s66, 0x2000000\n\t"
    "ds_read_b64_tr_b16 a[244:245], v229 offset:4096\n\t"
    "v_mfma_f32_16x16x32_bf16 v[0:3], a[228:231], v[112:115], v[0:3]\n\t"
    "s_mov_b64 s[52:53], s[48:49]\n\t"
    "s_xor_b32 s67, s35, 0x100\n\t"
    "v_mfma_f32_16x16x32_bf16 v[0:3], a[232:235], v[128:131], v[0:3]\n\t"
    "s_waitcnt lgkmcnt(3)\n\t"
    "ds_read_b64_tr_b16 a[246:247], v229 offset:6144\n\t"
    "v_mfma_f32_16x16x32_bf16 v[0:3], a[236:239], v[144:147], v[0:3]\n\t"
    "v_mfma_f32_16x16x32_bf16 v[4:7], a[236:239], v[148:151], v[4:7]\n\t"
    "ds_read_b64_tr_b16 a[248:249], v229 offset:8192\n\t"
    "ds_read_b128 v[248:251], v236 offset:768\n\t"
    "v_mfma_f32_16x16x32_bf16 v[4:7], a[232:235], v[132:135], v[4:7]\n\t"
    "s_xor_b32 s71, s65, 0x8000\n\t"
    "s_add_u32 m0, s70, 0x8000\n\t"
    "v_mfma_f32_16x16x32_bf16 v[4:7], a[228:231], v[116:119], v[4:7]\n\t"
    "ds_read_b64_tr_b16 a[250:251], v229 offset:10240\n\t"
    "v_mfma_f32_16x16x32_bf16 v[4:7], a[224:227], v[100:103], v[4:7]\n\t"
    "buffer_load_dwordx4 v232, s[44:47], s71 offen offset:0 nt lds\n\t"
    "v_mfma_f32_16x16x32_bf16 v[8:11], a[224:227], v[104:107], v[8:11]\n\t"
    "ds_read_b64_tr_b16 a[252:253], v229 offset:12288\n\t"
    "v_mfma_f32_16x16x32_bf16 v[8:11], a[228:231], v[120:123], v[8:11]\n\t"
    "v_add_u32 v230, 0x10000, v230\n\t"
    "v_mfma_f32_16x16x32_bf16 v[8:11], a[232:235], v[136:139], v[8:11]\n\t"
    "s_waitcnt lgkmcnt(2)\n\t"
    "ds_read_b64_tr_b16 a[254:255], v229 offset:14336\n\t"
    "v_mfma_f32_16x16x32_bf16 v[8:11], a[236:239], v[152:155], v[8:11]\n\t"
    "buffer_store_dwordx4 v[248:251], v237, s[56:59], s83 offen offset:3072 sc1\n\t"
    "v_mfma_f32_16x16x32_bf16 v[12:15], a[236:239], v[156:159], v[12:15]\n\t"
    "ds_read_b64_tr_b16 a[236:237], v230 offset:0\n\t"
    "v_mfma_f32_16x16x32_bf16 v[12:15], a[232:235], v[140:143], v[12:15]\n\t"
    "v_mfma_f32_16x16x32_bf16 v[12:15], a[228:231], v[124:127], v[12:15]\n\t"
    "ds_read_b64_tr_b16 a[238:239], v230 offset:2048\n\t"
    "v_mfma_f32_16x16x32_bf16 v[12:15], a[224:227], v[108:111], v[12:15]\n\t"
    "s_waitcnt lgkmcnt(4)\n\t"
    "v_mfma_f32_16x16x32_bf16 v[16:19], a[240:243], v[96:99], v[16:19]\n\t"
    "ds_read_b64_tr_b16 a[232:233], v230 offset:4096\n\t"
    "v_mfma_f32_16x16x32_bf16 v[16:19], a[244:247], v[112:115], v[16:19]\n\t"
    "v_mfma_f32_16x16x32_bf16 v[16:19], a[248:251], v[128:131], v[16:19]\n\t"
    "s_waitcnt lgkmcnt(3)\n\t"
    "ds_read_b64_tr_b16 a[234:235], v230 offset:6144\n\t"
    "v_mfma_f32_16x16x32_bf16 v[16:19], a[252:255], v[144:147], v[16:19]\n\t"
    "v_mfma_f32_16x16x32_bf16 v[20:23], a[252:255], v[148:151], v[20:23]\n\t"
    "ds_read_b64_tr_b16 a[228:229], v230 offset:8192\n\t"
    "v_mfma_f32_16x16x32_bf16 v[20:23], a[248:251], v[132:135], v[20:23]\n\t"
    "v_mfma_f32_16x16x32_bf16 v[20:23], a[244:247], v[116:119], v[20:23]\n\t"
    "ds_read_b64_tr_b16 a[230:231], v230 offset:10240\n\t"
    "v_mfma_f32_16x16x32_bf16 v[20:23], a[240:243], v[100:103], v[20:23]\n\t"
    "buffer_load_dwordx4 v233, s[44:47], s71 offen offset:1024 nt lds\n\t"
    "v_mfma_f32_16x16x32_bf16 v[24:27], a[240:243], v[104:107], v[24:27]\n\t"
    "ds_read_b64_tr_b16 a[224:225], v230 offset:12288\n\t"
    "v_mfma_f32_16x16x32_bf16 v[24:27], a[244:247], v[120:123], v[24:27]\n\t"
    "v_mfma_f32_16x16x32_bf16 v[24:27], a[248:251], v[136:139], v[24:27]\n\t"
    "ds_read_b64_tr_b16 a[226:227], v230 offset:14336\n\t"
    "v_mfma_f32_16x16x32_bf16 v[24:27], a[252:255], v[152:155], v[24:27]\n\t"
    "v_add_u32 v231, 0x10000, v231\n\t"
    "v_mfma_f32_16x16x32_bf16 v[28:31], a[252:255], v[156:159], v[28:31]\n\t"
    "ds_read_b64_tr_b16 a[252:253], v231 offset:0\n\t"
    "v_mfma_f32_16x16x32_bf16 v[28:31], a[248:251], v[140:143], v[28:31]\n\t"
    "v_mfma_f32_16x16x32_bf16 v[28:31], a[244:247], v[124:127], v[28:31]\n\t"
    "ds_read_b64_tr_b16 a[254:255], v231 offset:2048\n\t"
    "v_mfma_f32_16x16x32_bf16 v[28:31], a[240:243], v[108:111], v[28:31]\n\t"
    "s_waitcnt lgkmcnt(4)\n\t"
    "v_mfma_f32_16x16x32_bf16 v[32:35], a[236:239], v[96:99], v[32:35]\n\t"
    "ds_read_b64_tr_b16 a[248:249], v231 offset:4096\n\t"
    "v_mfma_f32_16x16x32_bf16 v[32:35], a[232:235], v[112:115], v[32:35]\n\t"
    "v_mfma_f32_16x16x32_bf16 v[32:35], a[228:231], v[128:131], v[32:35]\n\t"
    "s_waitcnt lgkmcnt(3)\n\t"
    "ds_read_b64_tr_b16 a[250:251], v231 offset:6144\n\t"
    "v_mfma_f32_16x16x32_bf16 v[32:35], a[224:227], v[144:147], v[32:35]\n\t"
    "v_mfma_f32_16x16x32_bf16 v[36:39], a[224:227], v[148:151], v[36:39]\n\t"
    "ds_read_b64_tr_b16 a[244:245], v231 offset:8192\n\t"
    "ds_read_b128 v[244:247], v236 offset:1024\n\t"
    "v_mfma_f32_16x16x32_bf16 v[36:39], a[228:231], v[132:135], v[36:39]\n\t"
    "v_mfma_f32_16x16x32_bf16 v[36:39], a[232:235], v[116:119], v[36:39]\n\t"
    "ds_read_b64_tr_b16 a[246:247], v231 offset:10240\n\t"
    "v_mfma_f32_16x16x32_bf16 v[36:39], a[236:239], v[100:103], v[36:39]\n\t"
    "buffer_load_dwordx4 v232, s[44:47], s71 offen offset:2048 nt lds\n\t"
    "v_mfma_f32_16x16x32_bf16 v[40:43], a[236:239], v[104:107], v[40:43]\n\t"
    "ds_read_b64_tr_b16 a[240:241], v231 offset:12288\n\t"
    "v_mfma_f32_16x16x32_bf16 v[40:43], a[232:235], v[120:123], v[40:43]\n\t"
    "s_xor_b32 s83, s68, 0x4100\n\t"
    "v_mfma_f32_16x16x32_bf16 v[40:43], a[228:231], v[136:139], v[40:43]\n\t"
    "s_waitcnt lgkmcnt(2)\n\t"
    "ds_read_b64_tr_b16 a[242:243], v231 offset:14336\n\t"
    "v_mfma_f32_16x16x32_bf16 v[40:43], a[224:227], v[152:155], v[40:43]\n\t"
    "buffer_store_dwordx4 v[244:247], v237, s[56:59], s83 offen offset:0 sc1\n\t"
    "v_mfma_f32_16x16x32_bf16 v[44:47], a[224:227], v[156:159], v[44:47]\n\t"
    "v_mfma_f32_16x16x32_bf16 v[44:47], a[228:231], v[140:143], v[44:47]\n\t"
    "v_mfma_f32_16x16x32_bf16 v[44:47], a[232:235], v[124:127], v[44:47]\n\t"
    "v_mfma_f32_16x16x32_bf16 v[44:47], a[236:239], v[108:111], v[44:47]\n\t"
    "s_waitcnt lgkmcnt(2)\n\t"
    "v_mfma_f32_16x16x32_bf16 v[48:51], a[252:255], v[96:99], v[48:51]\n\t"
    "v_mfma_f32_16x16x32_bf16 v[48:51], a[248:251], v[112:115], v[48:51]\n\t"
    "s_waitcnt lgkmcnt(0)\n\t"
    "v_mfma_f32_16x16x32_bf16 v[48:51], a[244:247], v[128:131], v[48:51]\n\t"
    "s_waitcnt vmcnt(17)\n\t"
    "v_mfma_f32_16x16x32_bf16 v[48:51], a[240:243], v[144:147], v[48:51]\n\t"
    "s_barrier\n\t"
    "v_mfma_f32_16x16x32_bf16 v[52:55], a[240:243], v[148:151], v[52:55]\n\t"
    "ds_read_b64_tr_b16 a[224:225], v228 offset:16384\n\t"
    "ds_read_b64_tr_b16 a[226:227], v228 offset:18432\n\t"
    "v_mfma_f32_16x16x32_bf16 v[52:55], a[244:247], v[132:135], v[52:55]\n\t"
    "ds_read_b64_tr_b16 a[228:229], v228 offset:20480\n\t"
    "ds_read_b64_tr_b16 a[230:231], v228 offset:22528\n\t"
    "v_mfma_f32_16x16x32_bf16 v[52:55], a[248:251], v[116:119], v[52:55]\n\t"
    "ds_read_b64_tr_b16 a[232:233], v228 offset:24576\n\t"
    "v_mfma_f32_16x16x32_bf16 v[52:55], a[252:255], v[100:103], v[52:55]\n\t"
    "ds_read_b64_tr_b16 a[234:235], v228 offset:26624\n\t"
    "v_mfma_f32_16x16x32_bf16 v[56:59], a[252:255], v[104:107], v[56:59]\n\t"
    "ds_read_b64_tr_b16 a[236:237], v228 offset:28672\n\t"
    "v_mfma_f32_16x16x32_bf16 v[56:59], a[248:251], v[120:123], v[56:59]\n\t"
    "buffer_load_dwordx4 v233, s[44:47], s71 offen offset:3072 nt lds\n\t"
    "v_mfma_f32_16x16x32_bf16 v[56:59], a[244:247], v[136:139], v[56:59]\n\t"
    "ds_read_b64_tr_b16 a[238:239], v228 offset:30720\n\t"
    "v_mfma_f32_16x16x32_bf16 v[56:59], a[240:243], v[152:155], v[56:59]\n\t"
    "v_mfma_f32_16x16x32_bf16 v[60:63], a[240:243], v[156:159], v[60:63]\n\t"
    "ds_read_b64_tr_b16 a[240:241], v229 offset:16384\n\t"
    "v_mfma_f32_16x16x32_bf16 v[60:63], a[244:247], v[140:143], v[60:63]\n\t"
    "v_mfma_f32_16x16x32_bf16 v[60:63], a[248:251], v[124:127], v[60:63]\n\t"
    "ds_read_b64_tr_b16 a[242:243], v229 offset:18432\n\t"
    "v_mfma_f32_16x16x32_bf16 v[60:63], a[252:255], v[108:111], v[60:63]\n\t"
    "s_waitcnt lgkmcnt(4)\n\t"
    "v_mfma_f32_16x16x32_bf16 v[0:3], a[224:227], v[160:163], v[0:3]\n\t"
    "ds_read_b64_tr_b16 a[244:245], v229 offset:20480\n\t"
    "v_mfma_f32_16x16x32_bf16 v[0:3], a[228:231], v[176:179], v[0:3]\n\t"
    "v_mfma_f32_16x16x32_bf16 v[0:3], a[232:235], v[192:195], v[0:3]\n\t"
    "s_waitcnt lgkmcnt(3)\n\t"
    "ds_read_b64_tr_b16 a[246:247], v229 offset:22528\n\t"
    "v_mfma_f32_16x16x32_bf16 v[0:3], a[236:239], v[208:211], v[0:3]\n\t"
    "v_mfma_f32_16x16x32_bf16 v[4:7], a[236:239], v[212:215], v[4:7]\n\t"
    "ds_read_b64_tr_b16 a[248:249], v229 offset:24576\n\t"
    "ds_read_b128 v[248:251], v236 offset:1280\n\t"
    "v_mfma_f32_16x16x32_bf16 v[4:7], a[232:235], v[196:199], v[4:7]\n\t"
    "s_xor_b32 s71, s65, 0xc000\n\t"
    "s_add_u32 m0, s70, 0xc000\n\t"
    "v_mfma_f32_16x16x32_bf16 v[4:7], a[228:231], v[180:183], v[4:7]\n\t"
    "ds_read_b64_tr_b16 a[250:251], v229 offset:26624\n\t"
    "v_mfma_f32_16x16x32_bf16 v[4:7], a[224:227], v[164:167], v[4:7]\n\t"
    "buffer_load_dwordx4 v232, s[44:47], s71 offen offset:0 nt lds\n\t"
    "v_mfma_f32_16x16x32_bf16 v[8:11], a[224:227], v[168:171], v[8:11]\n\t"
    "ds_read_b64_tr_b16 a[252:253], v229 offset:28672\n\t"
    "v_mul_f32 v0, v252, v0\n\t"
    "v_mfma_f32_16x16x32_bf16 v[8:11], a[228:231], v[184:187], v[8:11]\n\t"
    "v_mul_f32 v1, v252, v1\n\t"
    "v_mul_f32 v2, v252, v2\n\t"
    "v_mfma_f32_16x16x32_bf16 v[8:11], a[232:235], v[200:203], v[8:11]\n\t"
    "ds_read_b64_tr_b16 a[254:255], v229 offset:30720\n\t"
    "v_mul_f32 v3, v252, v3\n\t"
    "v_mfma_f32_16x16x32_bf16 v[8:11], a[236:239], v[216:219], v[8:11]\n\t"
    "s_waitcnt lgkmcnt(3)\n\t"
    "v_cvt_pk_bf16_f32 v0, v0, v1\n\t"
    "v_cvt_pk_bf16_f32 v1, v2, v3\n\t"
    "v_mfma_f32_16x16x32_bf16 v[12:15], a[236:239], v[220:223], v[12:15]\n\t"
    "ds_read_b64_tr_b16 a[236:237], v230 offset:16384\n\t"
    "v_mul_f32 v4, v253, v4\n\t"
    "v_mfma_f32_16x16x32_bf16 v[12:15], a[232:235], v[204:207], v[12:15]\n\t"
    "buffer_store_dwordx4 v[248:251], v237, s[56:59], s83 offen offset:1024 sc1\n\t"
    "v_mul_f32 v5, v253, v5\n\t"
    "v_mfma_f32_16x16x32_bf16 v[12:15], a[228:231], v[188:191], v[12:15]\n\t"
    "s_waitcnt lgkmcnt(3)\n\t"
    "ds_read_b64_tr_b16 a[238:239], v230 offset:18432\n\t"
    "v_mfma_f32_16x16x32_bf16 v[12:15], a[224:227], v[172:175], v[12:15]\n\t"
    "ds_write_b64 v235, v[0:1] offset:0\n\t"
    "v_mfma_f32_16x16x32_bf16 v[16:19], a[240:243], v[160:163], v[16:19]\n\t"
    "ds_read_b64_tr_b16 a[232:233], v230 offset:20480\n\t"
    "v_mul_f32 v6, v253, v6\n\t"
    "v_mfma_f32_16x16x32_bf16 v[16:19], a[244:247], v[176:179], v[16:19]\n\t"
    "s_waitcnt lgkmcnt(4)\n\t"
    "v_mul_f32 v7, v253, v7\n\t"
    "v_cvt_pk_bf16_f32 v4, v4, v5\n\t"
    "v_mfma_f32_16x16x32_bf16 v[16:19], a[248:251], v[192:195], v[16:19]\n\t"
    "ds_read_b64_tr_b16 a[234:235], v230 offset:22528\n\t"
    "v_cvt_pk_bf16_f32 v5, v6, v7\n\t"
    "v_mfma_f32_16x16x32_bf16 v[16:19], a[252:255], v[208:211], v[16:19]\n\t"
    "ds_write_b64 v235, v[4:5] offset:512\n\t"
    "v_mfma_f32_16x16x32_bf16 v[20:23], a[252:255], v[212:215], v[20:23]\n\t"
    "ds_read_b64_tr_b16 a[228:229], v230 offset:24576\n\t"
    "v_mfma_f32_16x16x32_bf16 v[20:23], a[248:251], v[196:199], v[20:23]\n\t"
    "v_mfma_f32_16x16x32_bf16 v[20:23], a[244:247], v[180:183], v[20:23]\n\t"
    "ds_read_b64_tr_b16 a[230:231], v230 offset:26624\n\t"
    "v_add_u32 v224, 0xffff0000, v224\n\t"
    "v_mfma_f32_16x16x32_bf16 v[20:23], a[240:243], v[164:167], v[20:23]\n\t"
    "buffer_load_dwordx4 v233, s[44:47], s71 offen offset:1024 nt lds\n\t"
    "v_mfma_f32_16x16x32_bf16 v[24:27], a[240:243], v[168:171], v[24:27]\n\t"
    "ds_read_b64_tr_b16 a[224:225], v230 offset:28672\n\t"
    "v_mul_f32 v16, v252, v16\n\t"
    "v_mfma_f32_16x16x32_bf16 v[24:27], a[244:247], v[184:187], v[24:27]\n\t"
    "v_mul_f32 v17, v252, v17\n\t"
    "v_mul_f32 v18, v252, v18\n\t"
    "v_mfma_f32_16x16x32_bf16 v[24:27], a[248:251], v[200:203], v[24:27]\n\t"
    "ds_read_b64_tr_b16 a[226:227], v230 offset:30720\n\t"
    "v_mul_f32 v19, v252, v19\n\t"
    "v_mfma_f32_16x16x32_bf16 v[24:27], a[252:255], v[216:219], v[24:27]\n\t"
    "v_cvt_pk_bf16_f32 v16, v16, v17\n\t"
    "v_cvt_pk_bf16_f32 v17, v18, v19\n\t"
    "v_mfma_f32_16x16x32_bf16 v[28:31], a[252:255], v[220:223], v[28:31]\n\t"
    "ds_read_b64_tr_b16 a[252:253], v231 offset:16384\n\t"
    "v_mul_f32 v20, v253, v20\n\t"
    "v_mfma_f32_16x16x32_bf16 v[28:31], a[248:251], v[204:207], v[28:31]\n\t"
    "v_mul_f32 v21, v253, v21\n\t"
    "v_add_u32 v225, 0xffff0000, v225\n\t"
    "v_mfma_f32_16x16x32_bf16 v[28:31], a[244:247], v[188:191], v[28:31]\n\t"
    "s_waitcnt lgkmcnt(3)\n\t"
    "ds_read_b64_tr_b16 a[254:255], v231 offset:18432\n\t"
    "v_mfma_f32_16x16x32_bf16 v[28:31], a[240:243], v[172:175], v[28:31]\n\t"
    "ds_write_b64 v235, v[16:17] offset:128\n\t"
    "v_mfma_f32_16x16x32_bf16 v[32:35], a[236:239], v[160:163], v[32:35]\n\t"
    "ds_read_b64_tr_b16 a[248:249], v231 offset:20480\n\t"
    "v_mul_f32 v22, v253, v22\n\t"
    "v_mfma_f32_16x16x32_bf16 v[32:35], a[232:235], v[176:179], v[32:35]\n\t"
    "s_waitcnt lgkmcnt(4)\n\t"
    "v_mul_f32 v23, v253, v23\n\t"
    "v_cvt_pk_bf16_f32 v20, v20, v21\n\t"
    "v_mfma_f32_16x16x32_bf16 v[32:35], a[228:231], v[192:195], v[32:35]\n\t"
    "ds_read_b64_tr_b16 a[250:251], v231 offset:22528\n\t"
    "v_cvt_pk_bf16_f32 v21, v22, v23\n\t"
    "v_mfma_f32_16x16x32_bf16 v[32:35], a[224:227], v[208:211], v[32:35]\n\t"
    "v_mul_f32 v24, v254, v24\n\t"
    "v_mul_f32 v25, v254, v25\n\t"
    "v_mfma_f32_16x16x32_bf16 v[36:39], a[224:227], v[212:215], v[36:39]\n\t"
    "ds_read_b64_tr_b16 a[244:245], v231 offset:24576\n\t"
    "ds_read_b128 v[244:247], v236 offset:1536\n\t"
    "v_mfma_f32_16x16x32_bf16 v[36:39], a[228:231], v[196:199], v[36:39]\n\t"
    "ds_write_b64 v235, v[20:21] offset:640\n\t"
    "v_mfma_f32_16x16x32_bf16 v[36:39], a[232:235], v[180:183], v[36:39]\n\t"
    "ds_read_b64_tr_b16 a[246:247], v231 offset:26624\n\t"
    "v_mul_f32 v26, v254, v26\n\t"
    "v_mfma_f32_16x16x32_bf16 v[36:39], a[236:239], v[164:167], v[36:39]\n\t"
    "buffer_load_dwordx4 v232, s[44:47], s71 offen offset:2048 nt lds\n\t"
    "v_mfma_f32_16x16x32_bf16 v[40:43], a[236:239], v[168:171], v[40:43]\n\t"
    "ds_read_b64_tr_b16 a[240:241], v231 offset:28672\n\t"
    "v_mul_f32 v27, v254, v27\n\t"
    "v_mfma_f32_16x16x32_bf16 v[40:43], a[232:235], v[184:187], v[40:43]\n\t"
    "s_waitcnt lgkmcnt(3)\n\t"
    "v_cvt_pk_bf16_f32 v24, v24, v25\n\t"
    "v_cvt_pk_bf16_f32 v25, v26, v27\n\t"
    "v_mfma_f32_16x16x32_bf16 v[40:43], a[228:231], v[200:203], v[40:43]\n\t"
    "ds_read_b64_tr_b16 a[242:243], v231 offset:30720\n\t"
    "v_mul_f32 v28, v255, v28\n\t"
    "v_mfma_f32_16x16x32_bf16 v[40:43], a[224:227], v[216:219], v[40:43]\n\t"
    "buffer_store_dwordx4 v[244:247], v237, s[56:59], s83 offen offset:2048 sc1\n\t"
    "v_mul_f32 v29, v255, v29\n\t"
    "v_mfma_f32_16x16x32_bf16 v[44:47], a[224:227], v[220:223], v[44:47]\n\t"
    "ds_write_b64 v235, v[24:25] offset:1152\n\t"
    "v_mfma_f32_16x16x32_bf16 v[44:47], a[228:231], v[204:207], v[44:47]\n\t"
    "v_mul_f32 v30, v255, v30\n\t"
    "v_mul_f32 v31, v255, v31\n\t"
    "v_mfma_f32_16x16x32_bf16 v[44:47], a[232:235], v[188:191], v[44:47]\n\t"
    "ds_read_b128 v[248:251], v236 offset:1792\n\t"
    "v_cvt_pk_bf16_f32 v28, v28, v29\n\t"
    "v_cvt_pk_bf16_f32 v29, v30, v31\n\t"
    "v_mfma_f32_16x16x32_bf16 v[44:47], a[236:239], v[172:175], v[44:47]\n\t"
    "s_waitcnt lgkmcnt(4)\n\t"
    "ds_write_b64 v235, v[28:29] offset:1664\n\t"
    "v_mfma_f32_16x16x32_bf16 v[48:51], a[252:255], v[160:163], v[48:51]\n\t"
    "v_mul_f32 v32, v252, v32\n\t"
    "v_mul_f32 v33, v252, v33\n\t"
    "v_mfma_f32_16x16x32_bf16 v[48:51], a[248:251], v[176:179], v[48:51]\n\t"
    "s_waitcnt lgkmcnt(3)\n\t"
    "v_mul_f32 v34, v252, v34\n\t"
    "v_mul_f32 v35, v252, v35\n\t"
    "v_mfma_f32_16x16x32_bf16 v[48:51], a[244:247], v[192:195], v[48:51]\n\t"
    "s_waitcnt vmcnt(17)\n\t"
    "v_cvt_pk_bf16_f32 v32, v32, v33\n\t"
    "v_cvt_pk_bf16_f32 v33, v34, v35\n\t"
    "v_mfma_f32_16x16x32_bf16 v[48:51], a[240:243], v[208:211], v[48:51]\n\t"
    "s_barrier\n\t"
    "v_mfma_f32_16x16x32_bf16 v[52:55], a[240:243], v[212:215], v[52:55]\n\t"
    "ds_read_b128 a[224:227], v224 offset:0\n\t"
    "v_mul_f32 v36, v253, v36\n\t"
    "v_mfma_f32_16x16x32_bf16 v[52:55], a[244:247], v[196:199], v[52:55]\n\t"
    "ds_read_b128 a[228:231], v225 offset:0\n\t"
    "v_mul_f32 v37, v253, v37\n\t"
    "v_mfma_f32_16x16x32_bf16 v[52:55], a[248:251], v[180:183], v[52:55]\n\t"
    "ds_read_b128 a[232:235], v224 offset:4096\n\t"
    "v_mfma_f32_16x16x32_bf16 v[52:55], a[252:255], v[164:167], v[52:55]\n\t"
    "ds_write_b64 v235, v[32:33] offset:16384\n\t"
    "v_mfma_f32_16x16x32_bf16 v[56:59], a[252:255], v[168:171], v[56:59]\n\t"
    "buffer_load_dwordx4 v233, s[44:47], s71 offen offset:3072 nt lds\n\t"
    "v_mfma_f32_16x16x32_bf16 v[56:59], a[248:251], v[184:187], v[56:59]\n\t"
    "v_mul_f32 v38, v253, v38\n\t"
    "v_mul_f32 v39, v253, v39\n\t"
    "v_mfma_f32_16x16x32_bf16 v[56:59], a[244:247], v[200:203], v[56:59]\n\t"
    "ds_read_b128 a[236:239], v225 offset:4096\n\t"
    "v_cvt_pk_bf16_f32 v36, v36, v37\n\t"
    "v_mfma_f32_16x16x32_bf16 v[56:59], a[240:243], v[216:219], v[56:59]\n\t"
    "v_cvt_pk_bf16_f32 v37, v38, v39\n\t"
    "v_mul_f32 v40, v254, v40\n\t"
    "v_mfma_f32_16x16x32_bf16 v[60:63], a[240:243], v[220:223], v[60:63]\n\t"
    "v_mul_f32 v41, v254, v41\n\t"
    "v_mfma_f32_16x16x32_bf16 v[60:63], a[244:247], v[204:207], v[60:63]\n\t"
    "ds_write_b64 v235, v[36:37] offset:16896\n\t"
    "v_mfma_f32_16x16x32_bf16 v[60:63], a[248:251], v[188:191], v[60:63]\n\t"
    "ds_read_b128 a[240:243], v224 offset:8192\n\t"
    "v_mul_f32 v42, v254, v42\n\t"
    "v_mfma_f32_16x16x32_bf16 v[60:63], a[252:255], v[172:175], v[60:63]\n\t"
    "s_waitcnt lgkmcnt(4)\n\t"
    "v_mul_f32 v43, v254, v43\n\t"
    "v_cvt_pk_bf16_f32 v40, v40, v41\n\t"
    "s_cmp_lt_u32 s73, 4\n\t"
    "s_cbranch_scc1 1b\n\t"
    "buffer_store_dwordx4 v[248:251], v237, s[56:59], s83 offen offset:3072 sc1\n\t"
    "v_mul_f32 v48, v252, v48\n\t"
    "v_mul_f32 v49, v252, v49\n\t"
    "v_mul_f32 v50, v252, v50\n\t"
    "v_mul_f32 v51, v252, v51\n\t"
    "v_cvt_pk_bf16_f32 v48, v48, v49\n\t"
    "v_cvt_pk_bf16_f32 v49, v50, v51\n\t"
    "ds_write_b64 v235, v[48:49] offset:16512\n\t"
    "ds_read_b128 v[64:67], v236 offset:0\n\t"
    "ds_read_b128 v[68:71], v236 offset:256\n\t"
    "v_mul_f32 v52, v253, v52\n\t"
    "v_mul_f32 v53, v253, v53\n\t"
    "v_mul_f32 v54, v253, v54\n\t"
    "v_mul_f32 v55, v253, v55\n\t"
    "v_cvt_pk_bf16_f32 v52, v52, v53\n\t"
    "v_cvt_pk_bf16_f32 v53, v54, v55\n\t"
    "ds_write_b64 v235, v[52:53] offset:17024\n\t"
    "ds_read_b128 v[72:75], v236 offset:512\n\t"
    "ds_read_b128 v[76:79], v236 offset:768\n\t"
    "s_xor_b32 s83, s68, 0x180\n\t"
    "s_waitcnt lgkmcnt(4)\n\t"
    "buffer_store_dwordx4 v[64:67], v237, s[56:59], s83 offen offset:0 sc1\n\t"
    "s_waitcnt lgkmcnt(3)\n\t"
    "buffer_store_dwordx4 v[68:71], v237, s[56:59], s83 offen offset:1024 sc1\n\t"
    "v_cvt_pk_bf16_f32 v41, v42, v43\n\t"
    "ds_write_b64 v235, v[40:41] offset:17408\n\t"
    "v_mul_f32 v8, v254, v8\n\t"
    "v_mul_f32 v9, v254, v9\n\t"
    "v_mul_f32 v10, v254, v10\n\t"
    "v_mul_f32 v11, v254, v11\n\t"
    "v_cvt_pk_bf16_f32 v8, v8, v9\n\t"
    "v_cvt_pk_bf16_f32 v9, v10, v11\n\t"
    "ds_write_b64 v235, v[8:9] offset:1024\n\t"
    "v_mul_f32 v56, v254, v56\n\t"
    "v_mul_f32 v57, v254, v57\n\t"
    "v_mul_f32 v58, v254, v58\n\t"
    "v_mul_f32 v59, v254, v59\n\t"
    "v_cvt_pk_bf16_f32 v56, v56, v57\n\t"
    "v_cvt_pk_bf16_f32 v57, v58, v59\n\t"
    "ds_write_b64 v235, v[56:57] offset:17536\n\t"
    "ds_read_b128 v[80:83], v236 offset:1024\n\t"
    "ds_read_b128 v[84:87], v236 offset:1280\n\t"
    "s_waitcnt lgkmcnt(6)\n\t"
    "buffer_store_dwordx4 v[72:75], v237, s[56:59], s83 offen offset:2048 sc1\n\t"
    "s_waitcnt lgkmcnt(5)\n\t"
    "buffer_store_dwordx4 v[76:79], v237, s[56:59], s83 offen offset:3072 sc1\n\t"
    "v_mul_f32 v44, v255, v44\n\t"
    "v_mul_f32 v45, v255, v45\n\t"
    "v_mul_f32 v46, v255, v46\n\t"
    "v_mul_f32 v47, v255, v47\n\t"
    "v_cvt_pk_bf16_f32 v44, v44, v45\n\t"
    "v_cvt_pk_bf16_f32 v45, v46, v47\n\t"
    "ds_write_b64 v235, v[44:45] offset:17920\n\t"
    "v_mul_f32 v12, v255, v12\n\t"
    "v_mul_f32 v13, v255, v13\n\t"
    "v_mul_f32 v14, v255, v14\n\t"
    "v_mul_f32 v15, v255, v15\n\t"
    "v_cvt_pk_bf16_f32 v12, v12, v13\n\t"
    "v_cvt_pk_bf16_f32 v13, v14, v15\n\t"
    "ds_write_b64 v235, v[12:13] offset:1536\n\t"
    "v_mul_f32 v60, v255, v60\n\t"
    "v_mul_f32 v61, v255, v61\n\t"
    "v_mul_f32 v62, v255, v62\n\t"
    "v_mul_f32 v63, v255, v63\n\t"
    "v_cvt_pk_bf16_f32 v60, v60, v61\n\t"
    "v_cvt_pk_bf16_f32 v61, v62, v63\n\t"
    "ds_write_b64 v235, v[60:61] offset:18048\n\t"
    "ds_read_b128 v[88:91], v236 offset:1536\n\t"
    "ds_read_b128 v[92:95], v236 offset:1792\n\t"
    "s_xor_b32 s83, s68, 0x4180\n\t"
    "s_waitcnt lgkmcnt(6)\n\t"
    "buffer_store_dwordx4 v[80:83], v237, s[56:59], s83 offen offset:0 sc1\n\t"
    "s_waitcnt lgkmcnt(5)\n\t"
    "buffer_store_dwordx4 v[84:87], v237, s[56:59], s83 offen offset:1024 sc1\n\t"
    "s_waitcnt lgkmcnt(1)\n\t"
    "buffer_store_dwordx4 v[88:91], v237, s[56:59], s83 offen offset:2048 sc1\n\t"
    "s_waitcnt lgkmcnt(0)\n\t"
    "buffer_store_dwordx4 v[92:95], v237, s[56:59], s83 offen offset:3072 sc1\n\t"
    "s_waitcnt vmcnt(0) lgkmcnt(0)\n\t"
    "s_mov_b32 %0, s74\n\t"
    : "=s"(flags)
    : "s"(__builtin_amdgcn_kernarg_segment_ptr()), "n"(0), "n"(0), "n"(0), "s"(wid), "s"(bx), "n"(0), "s"(lds0), "n"(0), "n"(0)
    : "memory", "vcc", "scc", "v0", "v1", "v2", "v3", "v4", "v5", "v6", "v7", "v8", "v9", "v10", "v11", "v12", "v13", "v14", "v15", "v16", "v17", "v18", "v19", "v20", "v21", "v22", "v23", "v24", "v25", "v26", "v27", "v28", "v29", "v30", "v31", "v32", "v33", "v34", "v35", "v36", "v37", "v38", "v39", "v40", "v41", "v42", "v43", "v44", "v45", "v46", "v47", "v48", "v49", "v50", "v51", "v52", "v53", "v54", "v55", "v56", "v57", "v58", "v59", "v60", "v61", "v62", "v63", "v64", "v65", "v66", "v67", "v68", "v69", "v70", "v71", "v72", "v73", "v74", "v75", "v76", "v77", "v78", "v79", "v80", "v81", "v82", "v83", "v84", "v85", "v86", "v87", "v88", "v89", "v90", "v91", "v92", "v93", "v94", "v95", "v96", "v97", "v98", "v99", "v100", "v101", "v102", "v103", "v104", "v105", "v106", "v107", "v108", "v109", "v110", "v111", "v112", "v113", "v114", "v115", "v116", "v117", "v118", "v119", "v120", "v121", "v122", "v123", "v124", "v125", "v126", "v127", "v128", "v129", "v130", "v131", "v132", "v133", "v134", "v135", "v136", "v137", "v138", "v139", "v140", "v141", "v142", "v143", "v144", "v145", "v146", "v147", "v148", "v149", "v150", "v151", "v152", "v153", "v154", "v155", "v156", "v157", "v158", "v159", "v160", "v161", "v162", "v163", "v164", "v165", "v166", "v167", "v168", "v169", "v170", "v171", "v172", "v173", "v174", "v175", "v176", "v177", "v178", "v179", "v180", "v181", "v182", "v183", "v184", "v185", "v186", "v187", "v188", "v189", "v190", "v191", "v192", "v193", "v194", "v195", "v196", "v197", "v198", "v199", "v200", "v201", "v202", "v203", "v204", "v205", "v206", "v207", "v208", "v209", "v210", "v211", "v212", "v213", "v214", "v215", "v216", "v217", "v218", "v219", "v220", "v221", "v222", "v223", "v224", "v225", "v226", "v227", "v228", "v229", "v230", "v231", "v232", "v233", "v234", "v235", "v236", "v237", "v238", "v239", "v240", "v241", "v242", "v243", "v244", "v245", "v246", "v247", "v248", "v249", "v250", "v251", "v252", "v253", "v254", "v255", "a0", "a1", "a2", "a3", "a4", "a5", "a6", "a7", "a8", "a9", "a10", "a11", "a12", "a13", "a14", "a15", "a16", "a17", "a18", "a19", "a20", "a21", "a22", "a23", "a24", "a25", "a26", "a27", "a28", "a29", "a30", "a31", "a32", "a33", "a34", "a35", "a36", "a37", "a38", "a39", "a40", "a41", "a42", "a43", "a44", "a45", "a46", "a47", "a48", "a49", "a50", "a51", "a52", "a53", "a54", "a55", "a56", "a57", "a58", "a59", "a60", "a61", "a62", "a63", "a64", "a65", "a66", "a67", "a68", "a69", "a70", "a71", "a72", "a73", "a74", "a75", "a76", "a77", "a78", "a79", "a80", "a81", "a82", "a83", "a84", "a85", "a86", "a87", "a88", "a89", "a90", "a91", "a92", "a93", "a94", "a95", "a96", "a97", "a98", "a99", "a100", "a101", "a102", "a103", "a104", "a105", "a106", "a107", "a108", "a109", "a110", "a111", "a112", "a113", "a114", "a115", "a116", "a117", "a118", "a119", "a120", "a121", "a122", "a123", "a124", "a125", "a126", "a127", "a128", "a129", "a130", "a131", "a132", "a133", "a134", "a135", "a136", "a137", "a138", "a139", "a140", "a141", "a142", "a143", "a144", "a145", "a146", "a147", "a148", "a149", "a150", "a151", "a152", "a153", "a154", "a155", "a156", "a157", "a158", "a159", "a160", "a161", "a162", "a163", "a164", "a165", "a166", "a167", "a168", "a169", "a170", "a171", "a172", "a173", "a174", "a175", "a176", "a177", "a178", "a179", "a180", "a181", "a182", "a183", "a184", "a185", "a186", "a187", "a188", "a189", "a190", "a191", "a192", "a193", "a194", "a195", "a196", "a197", "a198", "a199", "a200", "a201", "a202", "a203", "a204", "a205", "a206", "a207", "a208", "a209", "a210", "a211", "a212", "a213", "a214", "a215", "a216", "a217", "a218", "a219", "a220", "a221", "a222", "a223", "a224", "a225", "a226", "a227", "a228", "a229", "a230", "a231", "a232", "a233", "a234", "a235", "a236", "a237", "a238", "a239", "a240", "a241", "a242", "a243", "a244", "a245", "a246", "a247", "a248", "a249", "a250", "a251", "a252", "a253", "a254", "a255", "s34", "s35", "s36", "s37", "s38", "s39", "s40", "s41", "s42", "s43", "s44", "s45", "s46", "s47", "s48", "s49", "s50", "s51", "s52", "s53", "s54", "s55", "s56", "s57", "s58", "s59", "s60", "s61", "s62", "s63", "s64", "s65", "s66", "s67", "s68", "s69", "s70", "s71", "s72", "s73", "s74", "s75", "s76", "s77", "s78", "s79", "s80", "s81", "s82", "s83"
  );
  if (__builtin_expect(flags != 0, 0)) {
    int lane;
    asm volatile("v_mbcnt_lo_u32_b32 %0, -1, 0\n\tv_mbcnt_hi_u32_b32 %0, -1, %0" : "=v"(lane));
#pragma unroll 1
    for (int it = 0; it < 4; ++it) {
      const int item = it >> 1, half = it & 1;
      if (!((flags >> item) & 1)) continue;
      const size_t kvp = (size_t)item * 128 + pair, bh = kvp * 2 + hsel;
      const size_t row0 = bh * SQ + wid * 64 + half * 32;
      slow_rows(Q + row0 * D, K + kvp * SK * D, V + kvp * SK * D, O + row0 * D, lane);
    }
  }
}

extern "C" void kernel_launch(void* const* d_in, const int* in_sizes, int n_in, void* d_out, int out_size, void* d_ws, size_t ws_size, hipStream_t stream) {
  (void)hipFuncSetAttribute((const void*)attn_c64, hipFuncAttributeMaxDynamicSharedMemorySize, LDS_BYTES);
  attn_c64<<<256, 256, LDS_BYTES, stream>>>((const unsigned short*)d_in[0], (const unsigned short*)d_in[1], (const unsigned short*)d_in[2], (unsigned short*)d_out);
}
```
